```python
import torch

def _softmax(x, axis=-1):
    return torch.softmax(x, dim=axis)
def _gelu(x):
    return torch.nn.functional.gelu(x, approximate="tanh")
def _silu(x):
    return torch.nn.functional.silu(x)
def _sigmoid(x):
    return torch.sigmoid(x)
def _layernorm(x, axis=-1, eps=1e-5):
    m = x.mean(dim=axis, keepdim=True)
    v = x.var(dim=axis, keepdim=True, unbiased=False)
    return (x - m) / torch.sqrt(v + eps)
def _scatter_add(tgt, idx, src, axis=0):
    return tgt.clone().index_add_(axis, idx, src)
def _segment_sum(src, seg, n):
    out = torch.zeros((n,) + src.shape[1:], dtype=src.dtype)
    return out.index_add_(0, seg, src)
def _pad(x, pw):
    flat = tuple(v for pair in reversed(pw) for v in pair)
    return torch.nn.functional.pad(x, flat)

kernel_name = 'concat_heads__gqa_block_cached__gqa_block_cached__near_ridge'

def setup_inputs():

    _w = (lambda a: (lambda t: t.to(torch.bfloat16) if t.is_floating_point() else t)(torch.as_tensor(a)))
    def _A_setup():
        B, Sq, D = 8, 192, 640
        Hq, Hkv, Sk, Dh = 32, 32, 512, 192
        assert Hq % Hkv == 0, (Hq, Hkv)
        return {
            "x": _w((lambda *s: torch.randn(*s, dtype=torch.bfloat16) * 0.8)(B, Sq, D)),
            "k_cache": _w((lambda *s: torch.randn(*s, dtype=torch.bfloat16) * 0.8)(B, Hkv, Sk, Dh)),
            "v_cache": _w((lambda *s: torch.randn(*s, dtype=torch.bfloat16) * 0.05)(B, Hkv, Sk, Dh)),
            "wq": _w((lambda *s: torch.randn(*s, dtype=torch.bfloat16) * 0.8)(D, Hq * Dh) * (D ** -0.5)),
            "wo": _w((lambda *s: torch.randn(*s, dtype=torch.bfloat16) * 0.05)(Hq * Dh, D) * ((Hq * Dh) ** -0.5)),
        }
    _A_in = _A_setup()

    def _B_setup():
        B, Sq, D = 8, 192, 640
        Hq, Hkv, Sk, Dh = 32, 32, 512, 192
        assert Hq % Hkv == 0, (Hq, Hkv)
        return {
            "x": _w((lambda *s: torch.randn(*s, dtype=torch.bfloat16) * 0.8)(B, Sq, D)),
            "k_cache": _w((lambda *s: torch.randn(*s, dtype=torch.bfloat16) * 0.8)(B, Hkv, Sk, Dh)),
            "v_cache": _w((lambda *s: torch.randn(*s, dtype=torch.bfloat16) * 0.05)(B, Hkv, Sk, Dh)),
            "wq": _w((lambda *s: torch.randn(*s, dtype=torch.bfloat16) * 0.8)(D, Hq * Dh) * (D ** -0.5)),
            "wo": _w((lambda *s: torch.randn(*s, dtype=torch.bfloat16) * 0.05)(Hq * Dh, D) * ((Hq * Dh) ** -0.5)),
        }
    _B_in = _B_setup()

    return {
        "A_x": _A_in["x"],
        "A_k_cache": _A_in["k_cache"],
        "A_v_cache": _A_in["v_cache"],
        "A_wq": _A_in["wq"],
        "A_wo": _A_in["wo"],
        "B_x": _B_in["x"],
        "B_k_cache": _B_in["k_cache"],
        "B_v_cache": _B_in["v_cache"],
        "B_wq": _B_in["wq"],
        "B_wo": _B_in["wo"],
    }

def reference(A_x, A_k_cache, A_v_cache, A_wq, A_wo, B_x, B_k_cache, B_v_cache, B_wq, B_wo):
    def _A_ref(x, k_cache, v_cache, wq, wo):
        B, Sq, D = x.shape
        Hq, Dh = 32, 192
        Hkv = k_cache.shape[1]
        rep = Hq // Hkv
        q = (lambda x, *d: x.permute(*d))((x @ wq).reshape(B, Sq, Hq, Dh), 0, 2, 1, 3)
        k = (lambda x, n, axis: torch.repeat_interleave(x, n, dim=axis))(k_cache, rep, axis=1)
        v = (lambda x, n, axis: torch.repeat_interleave(x, n, dim=axis))(v_cache, rep, axis=1)
        scores = torch.einsum("bhqd,bhkd->bhqk", q, k) * (Dh ** -0.5)
        p = _softmax(scores, axis=-1)
        o = torch.einsum("bhqk,bhkd->bhqd", p, v)
        return (lambda x, *d: x.permute(*d))(o, 0, 2, 1, 3).reshape(B, Sq, Hq * Dh) @ wo

    def _B_ref(x, k_cache, v_cache, wq, wo):
        B, Sq, D = x.shape
        Hq, Dh = 32, 192
        Hkv = k_cache.shape[1]
        rep = Hq // Hkv
        q = (lambda x, *d: x.permute(*d))((x @ wq).reshape(B, Sq, Hq, Dh), 0, 2, 1, 3)
        k = (lambda x, n, axis: torch.repeat_interleave(x, n, dim=axis))(k_cache, rep, axis=1)
        v = (lambda x, n, axis: torch.repeat_interleave(x, n, dim=axis))(v_cache, rep, axis=1)
        scores = torch.einsum("bhqd,bhkd->bhqk", q, k) * (Dh ** -0.5)
        p = _softmax(scores, axis=-1)
        o = torch.einsum("bhqk,bhkd->bhqd", p, v)
        return (lambda x, *d: x.permute(*d))(o, 0, 2, 1, 3).reshape(B, Sq, Hq * Dh) @ wo

    a = _A_ref(A_x, A_k_cache, A_v_cache, A_wq, A_wo)
    b = _B_ref(B_x, B_k_cache, B_v_cache, B_wq, B_wo)
    return torch.concatenate([a, b], axis=-1)
```

```cpp
#include <hip/hip_runtime.h>
#include <hip/hip_bf16.h>
#include <stdint.h>
#include <utility>

using bf16x8 = __attribute__((ext_vector_type(8))) short;
using s16x4 = __attribute__((ext_vector_type(4))) short;
using f32x4 = __attribute__((ext_vector_type(4))) float;
using u32x4 = __attribute__((ext_vector_type(4))) unsigned;
using u32x2 = __attribute__((ext_vector_type(2))) unsigned;
typedef short v4i16_t __attribute__((ext_vector_type(4)));
typedef __attribute__((address_space(3))) char* lds_ptr;
typedef __attribute__((address_space(3))) const char* lds_cptr;

constexpr int NB = 8, SQ = 192, DM = 640, NH = 32, SK = 512, DH = 192;
constexpr int HD = NH * DH;
constexpr int SLOT = 24576;
constexpr int NSLOT = 6;
constexpr int LDS1 = SLOT * NSLOT;
constexpr float QSCALE = 0.07216878364870322f * 1.4426950408889634f;
constexpr float THR = 8.0f;

#define DI __device__ __forceinline__
#define MFMA16(a, b, c) __builtin_amdgcn_mfma_f32_16x16x32_bf16((a), (b), (c), 0, 0, 0)
#define ACLOB "a0","a1","a2","a3","a4","a5","a6","a7","a8","a9","a10","a11","a12","a13","a14","a15","a16","a17","a18","a19","a20","a21","a22","a23","a24","a25","a26","a27","a28","a29","a30","a31","a32","a33","a34","a35","a36","a37","a38","a39","a40","a41","a42","a43","a44","a45","a46","a47","a48","a49","a50","a51","a52","a53","a54","a55","a56","a57","a58","a59","a60","a61","a62","a63","a64","a65","a66","a67","a68","a69","a70","a71","a72","a73","a74","a75","a76","a77","a78","a79","a80","a81","a82","a83","a84","a85","a86","a87","a88","a89","a90","a91","a92","a93","a94","a95","a96","a97","a98","a99","a100","a101","a102","a103","a104","a105","a106","a107","a108","a109","a110","a111","a112","a113","a114","a115","a116","a117","a118","a119","a120","a121","a122","a123","a124","a125","a126","a127","a128","a129","a130","a131","a132","a133","a134","a135","a136","a137","a138","a139","a140","a141","a142","a143","a144","a145","a146","a147","a148","a149","a150","a151","a152","a153","a154","a155","a156","a157","a158","a159","a160","a161","a162","a163","a164","a165","a166","a167","a168","a169","a170","a171","a172","a173","a174","a175","a176","a177","a178","a179","a180","a181","a182","a183","a184","a185","a186","a187","a188","a189","a190","a191","a192","a193","a194","a195","a196","a197","a198","a199","a200","a201","a202","a203","a204","a205","a206","a207","a208","a209","a210","a211","a212","a213","a214","a215","a216","a217","a218","a219","a220","a221","a222","a223","a224","a225","a226","a227"
#define AO(d, q) (4 * (3 * (d) + (q)))
#define AQ(s, q) (156 + 4 * (3 * (s) + (q)))
template <int N> DI void mfma_o_(const bf16x8& a, const bf16x8& b) { asm volatile("v_mfma_f32_16x16x32_bf16 a[%c2:%c3], %0, %1, a[%c2:%c3]" :: "v"(a), "v"(b), "i"(N), "i"(N + 3)); }
template <int N> DI void mfma_o0_(const bf16x8& a, const bf16x8& b) { asm volatile("v_mfma_f32_16x16x32_bf16 a[%c2:%c3], %0, %1, 0" :: "v"(a), "v"(b), "i"(N), "i"(N + 3)); }
template <int N> DI void mfma_s_(f32x4& acc, const bf16x8& a) { asm volatile("v_mfma_f32_16x16x32_bf16 %0, %1, a[%c2:%c3], %0" : "+v"(acc) : "v"(a), "i"(N), "i"(N + 3)); }
template <int N> DI void mfma_sc_(f32x4& acc, const bf16x8& a, const f32x4& c) { asm volatile("v_mfma_f32_16x16x32_bf16 %0, %1, a[%c2:%c3], %4" : "=&v"(acc) : "v"(a), "i"(N), "i"(N + 3), "v"(c)); }
template <int N> DI float ard_() { float r; asm volatile("v_accvgpr_read_b32 %0, a[%c1]" : "=v"(r) : "i"(N)); return r; }
template <int N, class T> DI void awr_(T src) { asm volatile("v_accvgpr_write_b32 a[%c1], %0" :: "v"(src), "i"(N)); }
template <int N> DI void ascale_(float f) { float t; asm volatile("v_accvgpr_read_b32 %0, a[%c1]\n\tv_mul_f32 %0, %2, %0\n\tv_accvgpr_write_b32 a[%c1], %0" : "=&v"(t) : "i"(N), "v"(f)); }
DI float subip_(float x, float d) { asm volatile("v_sub_f32 %0, %0, %1" : "+v"(x) : "v"(d)); return x; }
DI void mfma_ip_(f32x4& x, const bf16x8& a, const bf16x8& b) { asm volatile("v_mfma_f32_16x16x32_bf16 %0, %1, %2, %0" : "+v"(x) : "v"(a), "v"(b)); }
template <class T> DI T pin_(T x) { asm volatile("" : "+v"(x)); return x; }
template <class T> DI void pin3n_(T& a, T& b, T& c) { asm volatile("s_nop 1" : "+v"(a), "+v"(b), "+v"(c)); }
#define MFMA_O(d, q, a, b) mfma_o_<AO(d, q)>(a, b)
#define MFMA_O0(d, q, a, b) mfma_o0_<AO(d, q)>(a, b)
#define MFMA_S(acc, a, s, q) mfma_s_<AQ(s, q)>(acc, a)
#define MFMA_SC(acc, a, s, q, c) mfma_sc_<AQ(s, q)>(acc, a, c)
#define ARD(dst, n) dst = ard_<n>()
#define AWR(n, src) awr_<n>(src)
#define ASCALE(n, f) ascale_<n>(f)
#define SUBIP(x, d) x = subip_(x, d)
#define NOPS(n) asm volatile("s_nop " #n ::: "memory")
#define SBAR() __builtin_amdgcn_sched_barrier(0)
#define PINF(x) x = pin_(x)
#define PINU(x) x = pin_(x)
#define WAITV_BAR(N) asm volatile("s_waitcnt vmcnt(" #N ") lgkmcnt(0)\n\ts_barrier" ::: "memory")

DI unsigned cvtpk(float lo, float hi) {
  typedef float f32x2 __attribute__((ext_vector_type(2)));
  typedef __bf16 bf16x2 __attribute__((ext_vector_type(2)));
  f32x2 v = {lo, hi}; bf16x2 b = __builtin_convertvector(v, bf16x2); return __builtin_bit_cast(unsigned, b);
}
DI void glds16(unsigned voff, const char* sbase, unsigned ldsaddr) {
  asm volatile("s_mov_b32 m0, %2\n\ts_nop 0\n\tglobal_load_lds_dwordx4 %0, %1" :: "v"(voff), "s"(sbase), "s"(ldsaddr) : "memory");
}
DI bf16x8 ldsb128(unsigned p) { return *(const __attribute__((address_space(3))) bf16x8*)p; }
DI s16x4 ldstr(unsigned p) { return __builtin_bit_cast(s16x4, __builtin_amdgcn_ds_read_tr16_b64_v4i16((__attribute__((address_space(3))) v4i16_t*)p)); }
DI unsigned opq(unsigned x) { asm volatile("" : "+v"(x)); return x; }
DI bf16x8 cat8(s16x4 lo, s16x4 hi) { return __builtin_shufflevector(lo, hi, 0, 1, 2, 3, 4, 5, 6, 7); }
DI float mx3(float a, float b, float c) { float r; asm("v_max3_f32 %0, %1, %2, %3" : "=v"(r) : "v"(a), "v"(b), "v"(c)); return r; }
DI float mx2(float a, float b) { float r; asm("v_max_f32_e32 %0, %1, %2" : "=v"(r) : "v"(a), "v"(b)); return r; }
DI float xmax(float v) {
  auto a = __builtin_amdgcn_permlane32_swap(__float_as_uint(v), __float_as_uint(v), false, false);
  v = mx2(__uint_as_float(a[0]), __uint_as_float(a[1]));
  auto b = __builtin_amdgcn_permlane16_swap(__float_as_uint(v), __float_as_uint(v), false, false);
  return mx2(__uint_as_float(b[0]), __uint_as_float(b[1]));
}
DI float xsum(float v) {
  auto a = __builtin_amdgcn_permlane32_swap(__float_as_uint(v), __float_as_uint(v), false, false);
  v = __uint_as_float(a[0]) + __uint_as_float(a[1]);
  auto b = __builtin_amdgcn_permlane16_swap(__float_as_uint(v), __float_as_uint(v), false, false);
  return __uint_as_float(b[0]) + __uint_as_float(b[1]);
}

template <int N> struct IC { static constexpr int value = N; };
#define AI __attribute__((always_inline))
template <class F, int... Is> DI void sfor_(F&& f, std::integer_sequence<int, Is...>) { (f(IC<Is>{}), ...); }
template <int N, class F> DI void sfor(F&& f) { sfor_(f, std::make_integer_sequence<int, N>{}); }
struct Ptrs { const char* x[2]; const char* k[2]; const char* v[2]; const char* wq[2]; const char* wo[2]; };

__global__ __launch_bounds__(256, 1) void k_attn(Ptrs P, char* __restrict__ ofr) {
  extern __shared__ __attribute__((aligned(16))) char smem[];
  lds_ptr lds = (lds_ptr)smem;
  const int tid = threadIdx.x, l = tid & 63, w = __builtin_amdgcn_readfirstlane(tid >> 6);
  const int i16 = l & 15, g = l >> 4, qq = i16 >> 2, pp = i16 & 3;
  const int xcd = blockIdx.x & 7, idx = blockIdx.x >> 3;
  const int br = xcd >> 2, h = 8 * (xcd & 3) + (idx & 7), b = idx >> 3;
  const char* xg = P.x[br] + (size_t)b * SQ * DM * 2;
  const char* wg = P.wq[br] + (size_t)h * DH * 2;
  const char* kg = P.k[br] + (size_t)(b * NH + h) * SK * DH * 2;
  const char* vg = P.v[br] + (size_t)(b * NH + h) * SK * DH * 2;

  unsigned offK[3], offV[3], offW[3], offX;
#pragma unroll
  for (int u = 0; u < 3; ++u) {
    const int o = 1024 * u + 16 * l, r = o / 384, chp = (o % 384) >> 4, row = 8 * w + r;
    const int aK = (row >> 1) & 7;
    offK[u] = row * 384 + (((chp & ~7) | ((chp ^ aK) & 7)) << 4);
    offV[u] = row * 384 + ((chp ^ (((row >> 1) & 3) << 1)) << 4);
    const int sW = ((row >> 1) & 1) | (((row >> 3) & 1) << 1);
    offW[u] = row * (HD * 2) + ((chp ^ (sW << 1)) << 4);
  }
  { const int row = 8 * w + (l >> 3), a = (row >> 1) & 7; offX = row * (DM * 2) + (((l & 7) ^ a) << 4); }

  const unsigned oK0 = offK[0], oK1 = offK[1], oK2 = offK[2], oV0 = offV[0], oV1 = offV[1], oV2 = offV[2], oW0 = offW[0], oW1 = offW[1], oW2 = offW[2];
  const unsigned lds0 = (unsigned)(uintptr_t)lds;
  auto fill_x = [&](int c, int slot) AI {
    const unsigned d = lds0 + slot * SLOT + w * 1024;
    const char* s = xg + c * 128;
#pragma unroll
    for (int n = 0; n < 6; ++n) glds16(offX, s + (size_t)n * 32 * DM * 2, d + n * 4096);
  };
  auto fill_t = [&](const char* s, size_t half, unsigned o0, unsigned o1, unsigned o2, int slot) AI {
    const unsigned d = lds0 + slot * SLOT + w * 3072;
    glds16(o0, s, d); glds16(o1, s, d + 1024); glds16(o2, s, d + 2048);
    glds16(o0, s + half, d + 12288); glds16(o1, s + half, d + 13312); glds16(o2, s + half, d + 14336);
  };
  auto fill_w = [&](int c, int slot) AI { fill_t(wg + (size_t)c * 64 * HD * 2, (size_t)32 * HD * 2, oW0, oW1, oW2, slot); };
  auto fill_k = [&](int j, int slot) AI { fill_t(kg + (size_t)j * SLOT, 12288, oK0, oK1, oK2, slot); };
  auto fill_v = [&](int j, int slot) AI { fill_t(vg + (size_t)j * SLOT, 12288, oV0, oV1, oV2, slot); };
  auto issue_kv = [&](auto F) AI {
    constexpr int f = F.value;
    if constexpr ((f & 1) == 0) fill_k((f - 20) >> 1, f % NSLOT); else fill_v((f - 20) >> 1, f % NSLOT);
  };

  const int a8 = i16 >> 1;
  const unsigned kb0 = lds0 + i16 * 384 + ((g ^ a8) << 4);
  const unsigned xb0 = lds0 + (48 * w + i16) * 128 + ((g ^ a8) << 4);
  const int swz = 2 * (g & 1) + (qq >> 1);
  const unsigned vb0 = lds0 + (4 * g + qq) * 384 + (swz << 5) + 8 * pp;
  const unsigned wb0 = lds0 + (8 * g + qq) * 384 + (swz << 5) + 8 * pp;

  {
    asm volatile("" ::: ACLOB);
    fill_x(0, 0); fill_w(0, 1); fill_x(1, 2); fill_w(1, 3);
    int sl = 0;
    { float z = 0.f; PINF(z); sfor<144>([&](auto N) AI { AWR(N.value, z); }); }
    NOPS(1);
#pragma unroll 1
    for (int c = 0; c < 10; ++c) {
      WAITV_BAR(12);
      const int sn = sl >= 2 ? sl - 2 : sl + 4;
      unsigned ax[2], aw[4];
      ax[0] = opq(xb0 + sl * SLOT); ax[1] = opq((xb0 ^ 64) + sl * SLOT);
#pragma unroll
      for (int d = 0; d < 4; ++d) aw[d] = opq((wb0 ^ (d << 5)) + (sl + 1) * SLOT);
      sl = sl == 4 ? 0 : sl + 2;
      bf16x8 xf[2][3], wf[3];
      auto rdw = [&](auto F) AI {
        constexpr int f = F.value, s = f / 12, d = f % 12;
        const unsigned p = aw[d & 3] + s * 32 * 384 + (d >> 2) * 128;
        wf[f % 3] = cat8(ldstr(p), ldstr(p + 4 * 384));
      };
#pragma unroll
      for (int q = 0; q < 3; ++q) xf[0][q] = ldsb128(ax[0] + q * 2048);
      rdw(IC<0>{}); rdw(IC<1>{});
      SBAR();
      sfor<24>([&](auto F) AI {
        constexpr int f = F.value, s = f / 12, d = f % 12;
        if constexpr (f + 2 < 24) rdw(IC<f + 2>{});
        if constexpr (f >= 2 && f < 5) xf[1][f - 2] = ldsb128(ax[1] + (f - 2) * 2048);
        MFMA_O(d, 0, wf[f % 3], xf[s][0]); MFMA_O(d, 1, wf[f % 3], xf[s][1]); MFMA_O(d, 2, wf[f % 3], xf[s][2]);
        if constexpr (f == 6) { if (c < 8) fill_x(c + 2, sn); else fill_k(c - 8, sn); }
        if constexpr (f == 12) { if (c < 8) fill_w(c + 2, sn + 1); else fill_v(c - 8, sn + 1); }
        SBAR();
      });
    }
    NOPS(7);
    sfor<18>([&](auto I) AI {
      constexpr int s = I.value / 3, q = I.value % 3;
      f32x4 X, Y;
      sfor<4>([&](auto R) AI { ARD(X[R.value], AO(2 * s, q) + R.value); ARD(Y[R.value], AO(2 * s + 1, q) + R.value); });
      X *= QSCALE; Y *= QSCALE;
      unsigned x0 = cvtpk(X[0], X[1]), x1 = cvtpk(X[2], X[3]), y0 = cvtpk(Y[0], Y[1]), y1 = cvtpk(Y[2], Y[3]);
      auto r0 = __builtin_amdgcn_permlane32_swap(x0, y0, false, false);
      auto r1 = __builtin_amdgcn_permlane32_swap(x1, y1, false, false);
      auto t0 = __builtin_amdgcn_permlane16_swap(r0[0], r0[1], false, false);
      auto t1 = __builtin_amdgcn_permlane16_swap(r1[0], r1[1], false, false);
      AWR(AQ(s, q) + 0, t0[0]); AWR(AQ(s, q) + 1, t1[0]); AWR(AQ(s, q) + 2, t0[1]); AWR(AQ(s, q) + 3, t1[1]);
    });
    NOPS(1);
  }

  f32x4 sc[2][4][3];
  f32x4 negm[3];
  unsigned pw[2][3][4];
  bf16x8 ones = {0x3F80, 0x3F80, 0x3F80, 0x3F80, 0x3F80, 0x3F80, 0x3F80, 0x3F80};
  asm volatile("" : "+v"(ones));

#define EXP1(X, kb, q, r) do { sc[X][kb][q][r] = __builtin_amdgcn_exp2f(sc[X][kb][q][r]); PINF(sc[X][kb][q][r]); } while (0)
#define CVT1(X, c) do { constexpr int kb_ = (c) / 6, q_ = ((c) % 6) >> 1, hf_ = (c) & 1; \
    pw[kb_ >> 1][q_][2 * (kb_ & 1) + hf_] = cvtpk(sc[X][kb_][q_][2 * hf_], sc[X][kb_][q_][2 * hf_ + 1]); PINU(pw[kb_ >> 1][q_][2 * (kb_ & 1) + hf_]); } while (0)

  WAITV_BAR(18);
  issue_kv(IC<24>{}); issue_kv(IC<25>{});
  {
    unsigned ak[2];
    ak[0] = opq(kb0 + 2 * SLOT); ak[1] = opq((kb0 ^ 64) + 2 * SLOT);
#pragma unroll
    for (int q = 0; q < 3; ++q) negm[q] = f32x4{0.f, 0.f, 0.f, 0.f};
    asm volatile("s_nop 1" : "+v"(negm[0]), "+v"(negm[1]), "+v"(negm[2]));
    bf16x8 kf[3];
    auto rdk = [&](auto F) AI { constexpr int f = F.value, s = f >> 2, kb = f & 3; kf[f % 3] = ldsb128(ak[s & 1] + kb * 6144 + (s >> 1) * 128); };
    rdk(IC<0>{}); rdk(IC<1>{});
    SBAR();
    sfor<24>([&](auto F) AI {
      constexpr int f = F.value, s = f >> 2, kb = f & 3;
      if constexpr (f + 2 < 24) rdk(IC<f + 2>{});
      sfor<3>([&](auto Q) AI {
        constexpr int q = Q.value;
        if constexpr (s == 0) { MFMA_SC(sc[0][kb][q], kf[f % 3], s, q, negm[q]); } else { MFMA_S(sc[0][kb][q], kf[f % 3], s, q); }
      });
      SBAR();
    });
    asm volatile("s_nop 7" : "+v"(sc[0][0][0]), "+v"(sc[0][0][1]), "+v"(sc[0][0][2]), "+v"(sc[0][1][0]), "+v"(sc[0][1][1]), "+v"(sc[0][1][2]),
                             "+v"(sc[0][2][0]), "+v"(sc[0][2][1]), "+v"(sc[0][2][2]), "+v"(sc[0][3][0]), "+v"(sc[0][3][1]), "+v"(sc[0][3][2]));
#pragma unroll
    for (int q = 0; q < 3; ++q) {
      float m0 = mx3(sc[0][0][q][0], sc[0][0][q][1], sc[0][0][q][2]);
      m0 = mx2(m0, sc[0][0][q][3]);
#pragma unroll
      for (int kb = 1; kb < 4; ++kb) { m0 = mx3(m0, sc[0][kb][q][0], sc[0][kb][q][1]); m0 = mx3(m0, sc[0][kb][q][2], sc[0][kb][q][3]); }
      m0 = xmax(m0);
      negm[q] = f32x4{-m0, -m0, -m0, -m0};
#pragma unroll
      for (int kb = 0; kb < 4; ++kb) sc[0][kb][q] -= m0;
#pragma unroll
      for (int r = 0; r < 4; ++r) sc[0][0][q][r] = __builtin_amdgcn_exp2f(sc[0][0][q][r]);
    }
  }
  sfor<8>([&](auto Jc) AI {
    constexpr int j = Jc.value;
    constexpr int X = j & 1, Y = X ^ 1;
    if constexpr (j == 0) { WAITV_BAR(18); } else if constexpr (j < 6) { WAITV_BAR(12); } else if constexpr (j == 6) { WAITV_BAR(6); } else { WAITV_BAR(0); }
    if constexpr (j < 7) {
      unsigned ak[2];
      ak[0] = opq(kb0 + ((22 + 2 * j) % NSLOT) * SLOT); ak[1] = opq((kb0 ^ 64) + ((22 + 2 * j) % NSLOT) * SLOT);
      pin3n_(negm[0], negm[1], negm[2]);
      bf16x8 kf[3];
      auto rdk = [&](auto F) AI { constexpr int f = F.value, s = f >> 2, kb = f & 3; kf[f % 3] = ldsb128(ak[s & 1] + kb * 6144 + (s >> 1) * 128); };
      rdk(IC<0>{}); rdk(IC<1>{});
      SBAR();
      sfor<24>([&](auto F) AI {
        constexpr int f = F.value, s = f >> 2, kb = f & 3;
        if constexpr (f + 2 < 24) rdk(IC<f + 2>{});
        sfor<3>([&](auto Q) AI {
          constexpr int q = Q.value;
          if constexpr (s == 0) { MFMA_SC(sc[Y][kb][q], kf[f % 3], s, q, negm[q]); } else { MFMA_S(sc[Y][kb][q], kf[f % 3], s, q); }
          if constexpr (q == 0) { CVT1(X, f); }
          else {
            constexpr int e = 2 * f + q - 1;
            if constexpr (e < 36) { EXP1(X, 1 + e / 12, (e % 12) >> 2, e & 3); }
          }
          SBAR();
        });
        if constexpr (f == 18 && j >= 1 && 25 + 2 * j <= 35) issue_kv(IC<25 + 2 * j>{});
        if constexpr (f == 21 && 26 + 2 * j <= 35) issue_kv(IC<26 + 2 * j>{});
        SBAR();
      });
    } else {
      sfor<36>([&](auto E) AI { constexpr int e = E.value; EXP1(X, 1 + e / 12, (e % 12) >> 2, e & 3); });
      sfor<24>([&](auto C) AI { CVT1(X, C.value); });
    }
    {
      bf16x8 pf[2][3];
#pragma unroll
      for (int t = 0; t < 2; ++t)
#pragma unroll
        for (int q = 0; q < 3; ++q) { u32x4 f = {pw[t][q][0], pw[t][q][1], pw[t][q][2], pw[t][q][3]}; pf[t][q] = __builtin_bit_cast(bf16x8, f); }
      pin3n_(pf[0][0], pf[0][1], pf[0][2]); pin3n_(pf[1][0], pf[1][1], pf[1][2]);
      unsigned av[4];
#pragma unroll
      for (int d = 0; d < 4; ++d) av[d] = opq((vb0 ^ (d << 5)) + ((21 + 2 * j) % NSLOT) * SLOT);
      bf16x8 vf[3];
      auto rdv = [&](auto F) AI {
        constexpr int f = F.value, t = f / 12, d = f % 12;
        const unsigned p = av[d & 3] + t * 32 * 384 + (d >> 2) * 128;
        vf[f % 3] = cat8(ldstr(p), ldstr(p + 16 * 384));
      };
      float mx[3], gr[3], fct[3];
      bool pend = false;
      rdv(IC<0>{}); rdv(IC<1>{});
      SBAR();
      sfor<26>([&](auto Gc) AI {
        constexpr int G = Gc.value, t = G / 13, gi = G % 13;
        if constexpr (gi == 0) {
          sfor<3>([&](auto Q) AI { constexpr int q = Q.value;
            if constexpr (j == 0 && t == 0) { MFMA_O0(12, q, ones, pf[t][q]); } else { MFMA_O(12, q, ones, pf[t][q]); } });
        } else {
          constexpr int d = gi - 1, f = 12 * t + d;
          if constexpr (f + 2 < 24) rdv(IC<f + 2>{});
          sfor<3>([&](auto Q) AI { constexpr int q = Q.value;
            if constexpr (j == 0 && t == 0) { MFMA_O0(d, q, vf[f % 3], pf[t][q]); } else { MFMA_O(d, q, vf[f % 3], pf[t][q]); } });
        }
        if constexpr (j < 7) {
          if constexpr (G >= 2 && G < 8) {
            constexpr int q = (G - 2) >> 1, kb = 2 * ((G - 2) & 1);
            if constexpr (kb == 0) {
              mx[q] = mx3(sc[Y][0][q][0], sc[Y][0][q][1], sc[Y][0][q][2]); mx[q] = mx3(mx[q], sc[Y][0][q][3], sc[Y][1][q][0]);
              mx[q] = mx3(mx[q], sc[Y][1][q][1], sc[Y][1][q][2]); mx[q] = mx2(mx[q], sc[Y][1][q][3]);
            } else {
              mx[q] = mx3(mx[q], sc[Y][2][q][0], sc[Y][2][q][1]); mx[q] = mx3(mx[q], sc[Y][2][q][2], sc[Y][2][q][3]);
              mx[q] = mx3(mx[q], sc[Y][3][q][0], sc[Y][3][q][1]); mx[q] = mx3(mx[q], sc[Y][3][q][2], sc[Y][3][q][3]);
            }
            PINF(mx[q]);
          }
          if constexpr (G >= 8 && G < 11) { gr[G - 8] = xmax(mx[G - 8]); PINF(gr[G - 8]); }
          if constexpr (G == 11) {
            pend = __any((gr[0] > THR) | (gr[1] > THR) | (gr[2] > THR));
            if (__builtin_expect(pend, 0)) {
#pragma unroll
              for (int q = 0; q < 3; ++q) {
                const unsigned db = (__float_as_uint(fmaxf(gr[q], 0.f)) + 0xFFFFu) & 0xFFFF0000u;
                fct[q] = __builtin_amdgcn_exp2f(-__uint_as_float(db));
                u32x4 bw = {g == 0 ? ((db >> 16) | 0x8000u) : 0u, 0u, 0u, 0u};
                bf16x8 bfr = __builtin_bit_cast(bf16x8, bw);
                asm volatile("s_nop 1" : "+v"(bfr));
                mfma_ip_(negm[q], ones, bfr);
                mfma_ip_(sc[Y][0][q], ones, bfr); mfma_ip_(sc[Y][1][q], ones, bfr); mfma_ip_(sc[Y][2][q], ones, bfr); mfma_ip_(sc[Y][3][q], ones, bfr);
              }
              NOPS(7);
            }
          }
          if constexpr (G >= 12 && G < 24) { constexpr int e = G - 12; EXP1(Y, 0, e >> 2, e & 3); }
        }
        SBAR();
      });
      if constexpr (j < 7) {
        if (__builtin_expect(pend, 0)) {
          NOPS(7);
          sfor<156>([&](auto N) AI { ASCALE(N.value, fct[(N.value >> 2) % 3]); });
          NOPS(1);
        }
      }
    }
  });
  NOPS(7);
  sfor<3>([&](auto Q) AI {
    constexpr int q = Q.value;
    float lq; ARD(lq, AO(12, q));
    const float inv = 1.0f / lq;
    char* dst = ofr + ((((size_t)(br * NB + b) * 12 + (3 * w + q)) * 192 + h * 6) * 64 + l) * 16;
    sfor<6>([&](auto T) AI {
      constexpr int t = T.value;
      f32x4 X, Y;
      sfor<4>([&](auto R) AI { ARD(X[R.value], AO(2 * t, q) + R.value); ARD(Y[R.value], AO(2 * t + 1, q) + R.value); });
      X *= inv; Y *= inv;
      u32x4 f = {cvtpk(X[0], X[1]), cvtpk(X[2], X[3]), cvtpk(Y[0], Y[1]), cvtpk(Y[2], Y[3])};
      *(u32x4*)(dst + t * 1024) = f;
    });
  });
}

constexpr int ST2_W = 128 * 160;
constexpr int ST2_O = 6 * 4 * 1024;
constexpr int ST2 = ST2_W + ST2_O;
constexpr int LDS2 = 3 * ST2;

__global__ __launch_bounds__(256, 1) void k_oproj(Ptrs P, const char* __restrict__ ofr, char* __restrict__ out) {
  extern __shared__ __attribute__((aligned(16))) char smem[];
  lds_ptr lds = (lds_ptr)smem;
  const int tid = threadIdx.x, l = tid & 63, w = __builtin_amdgcn_readfirstlane(tid >> 6);
  const int i16 = l & 15, g = l >> 4, qq = i16 >> 2, pp = i16 & 3;
  const int xcd = blockIdx.x & 7, idx = blockIdx.x >> 3;
  const int br = xcd >> 2, tq = 4 * (xcd & 3) + (idx >> 3), tn = idx & 7;
  const int qh = w & 1, kh = w >> 1;
  const char* wsrc = P.wo[br] + tn * 160;
  const char* osrc = ofr + ((size_t)(br * NB * 12 + tq * 6) * 192) * 1024;
  const unsigned lds0 = (unsigned)(uintptr_t)lds;
  const unsigned l16 = l * 16;
  unsigned offW[5];
#pragma unroll
  for (int u = 0; u < 5; ++u) { const int o = 1024 * u + 16 * l, r = o / 160, ch = (o % 160) >> 4; offW[u] = (32 * w + r) * (DM * 2) + ch * 16; }
  auto issue = [&](int c, int stg) {
    const unsigned d = lds0 + stg * ST2;
    const char* s = wsrc + (size_t)c * 128 * DM * 2;
#pragma unroll
    for (int u = 0; u < 5; ++u) glds16(offW[u], s, d + w * 5120 + u * 1024);
#pragma unroll
    for (int n = 0; n < 6; ++n) {
      const int pc = 6 * w + n;
      glds16(l16, osrc + ((size_t)(pc >> 2) * 192 + c * 4 + (pc & 3)) * 1024, d + ST2_W + pc * 1024);
    }
  };
  f32x4 acc[5][3];
#pragma unroll
  for (int n = 0; n < 5; ++n)
#pragma unroll
    for (int q = 0; q < 3; ++q) acc[n][q] = f32x4{0.f, 0.f, 0.f, 0.f};
  const unsigned wbase = lds0 + (4 * g + qq) * 160 + 8 * pp + kh * 64 * 160;
  const unsigned obase = lds0 + ST2_W + (3 * qh * 4 + 2 * kh) * 1024 + l * 16;
  issue(0, 0); issue(1, 1);
  int stg = 0;
#pragma unroll 1
  for (int c = 0; c < 48; ++c) {
    if (c < 47) { WAITV_BAR(11); } else { WAITV_BAR(0); }
    if (c + 2 < 48) issue(c + 2, stg == 0 ? 2 : stg - 1);
    const unsigned ao = opq(obase + stg * ST2), aw = opq(wbase + stg * ST2);
    stg = stg == 2 ? 0 : stg + 1;
#pragma unroll
    for (int t = 0; t < 2; ++t) {
      bf16x8 of[3];
#pragma unroll
      for (int q = 0; q < 3; ++q) of[q] = ldsb128(ao + (q * 4 + t) * 1024);
#pragma unroll
      for (int n = 0; n < 5; ++n) {
        const unsigned p = aw + t * 32 * 160 + n * 32;
        const bf16x8 wf = cat8(ldstr(p), ldstr(p + 16 * 160));
#pragma unroll
        for (int q = 0; q < 3; ++q) acc[n][q] = MFMA16(wf, of[q], acc[n][q]);
      }
    }
  }
  __syncthreads();
  if (kh == 1) {
#pragma unroll
    for (int n = 0; n < 5; ++n)
#pragma unroll
      for (int q = 0; q < 3; ++q) *(__attribute__((address_space(3))) f32x4*)(lds + ((qh * 15 + n * 3 + q) * 64 + l) * 16) = acc[n][q];
  }
  __syncthreads();
  if (kh == 0) {
#pragma unroll
    for (int q = 0; q < 3; ++q) {
      char* dst = out + ((size_t)(tq * 96 + (3 * qh + q) * 16 + i16) * (2 * DM) + br * DM + tn * 80 + 4 * g) * 2;
#pragma unroll
      for (int n = 0; n < 5; ++n) {
        const f32x4 o = acc[n][q] + *(const __attribute__((address_space(3))) f32x4*)(lds + ((qh * 15 + n * 3 + q) * 64 + l) * 16);
        u32x2 f = {cvtpk(o[0], o[1]), cvtpk(o[2], o[3])};
        *(u32x2*)(dst + n * 32) = f;
      }
    }
  }
}

extern "C" void kernel_launch(void* const* d_in, const int* in_sizes, int n_in, void* d_out, int out_size, void* d_ws, size_t ws_size,
                              hipStream_t stream) {
  Ptrs P;
  for (int r = 0; r < 2; ++r) {
    P.x[r] = (const char*)d_in[5 * r + 0];
    P.k[r] = (const char*)d_in[5 * r + 1];
    P.v[r] = (const char*)d_in[5 * r + 2];
    P.wq[r] = (const char*)d_in[5 * r + 3];
    P.wo[r] = (const char*)d_in[5 * r + 4];
  }
  (void)hipFuncSetAttribute((const void*)k_attn, hipFuncAttributeMaxDynamicSharedMemorySize, LDS1);
  (void)hipFuncSetAttribute((const void*)k_oproj, hipFuncAttributeMaxDynamicSharedMemorySize, LDS2);
  hipLaunchKernelGGL(k_attn, dim3(512), dim3(256), LDS1, stream, P, (char*)d_ws);
  hipLaunchKernelGGL(k_oproj, dim3(256), dim3(256), LDS2, stream, P, (const char*)d_ws, (char*)d_out);
}
```

```cpp
#include <hip/hip_runtime.h>
#include <hip/hip_bf16.h>
#include <stdint.h>
#include <utility>

using bf16x8 = __attribute__((ext_vector_type(8))) short;
using s16x4 = __attribute__((ext_vector_type(4))) short;
using f32x4 = __attribute__((ext_vector_type(4))) float;
using u32x4 = __attribute__((ext_vector_type(4))) unsigned;
using u32x2 = __attribute__((ext_vector_type(2))) unsigned;
typedef short v4i16_t __attribute__((ext_vector_type(4)));
typedef __attribute__((address_space(3))) char* lds_ptr;
typedef __attribute__((address_space(3))) const char* lds_cptr;

constexpr int NB = 8, SQ = 192, DM = 640, NH = 32, SK = 512, DH = 192;
constexpr int HD = NH * DH;
constexpr int SLOT = 24576;
constexpr int NSLOT = 6;
constexpr int LDS1 = SLOT * NSLOT;
constexpr float QSCALE = 0.07216878364870322f * 1.4426950408889634f;
constexpr float THR = 8.0f;

#define DI __device__ __forceinline__
#define MFMA16(a, b, c) __builtin_amdgcn_mfma_f32_16x16x32_bf16((a), (b), (c), 0, 0, 0)
#define ACLOB "a0","a1","a2","a3","a4","a5","a6","a7","a8","a9","a10","a11","a12","a13","a14","a15","a16","a17","a18","a19","a20","a21","a22","a23","a24","a25","a26","a27","a28","a29","a30","a31","a32","a33","a34","a35","a36","a37","a38","a39","a40","a41","a42","a43","a44","a45","a46","a47","a48","a49","a50","a51","a52","a53","a54","a55","a56","a57","a58","a59","a60","a61","a62","a63","a64","a65","a66","a67","a68","a69","a70","a71","a72","a73","a74","a75","a76","a77","a78","a79","a80","a81","a82","a83","a84","a85","a86","a87","a88","a89","a90","a91","a92","a93","a94","a95","a96","a97","a98","a99","a100","a101","a102","a103","a104","a105","a106","a107","a108","a109","a110","a111","a112","a113","a114","a115","a116","a117","a118","a119","a120","a121","a122","a123","a124","a125","a126","a127","a128","a129","a130","a131","a132","a133","a134","a135","a136","a137","a138","a139","a140","a141","a142","a143","a144","a145","a146","a147","a148","a149","a150","a151","a152","a153","a154","a155","a156","a157","a158","a159","a160","a161","a162","a163","a164","a165","a166","a167","a168","a169","a170","a171","a172","a173","a174","a175","a176","a177","a178","a179","a180","a181","a182","a183","a184","a185","a186","a187","a188","a189","a190","a191","a192","a193","a194","a195","a196","a197","a198","a199","a200","a201","a202","a203","a204","a205","a206","a207","a208","a209","a210","a211","a212","a213","a214","a215","a216","a217","a218","a219","a220","a221","a222","a223","a224","a225","a226","a227"
#define AO(d, q) (4 * (3 * (d) + (q)))
#define AQ(s, q) (156 + 4 * (3 * (s) + (q)))
template <int N> DI void mfma_o_(const bf16x8& a, const bf16x8& b) { asm volatile("v_mfma_f32_16x16x32_bf16 a[%c2:%c3], %0, %1, a[%c2:%c3]" :: "v"(a), "v"(b), "i"(N), "i"(N + 3)); }
template <int N> DI void mfma_o0_(const bf16x8& a, const bf16x8& b) { asm volatile("v_mfma_f32_16x16x32_bf16 a[%c2:%c3], %0, %1, 0" :: "v"(a), "v"(b), "i"(N), "i"(N + 3)); }
template <int N> DI void mfma_s_(f32x4& acc, const bf16x8& a) { asm volatile("v_mfma_f32_16x16x32_bf16 %0, %1, a[%c2:%c3], %0" : "+v"(acc) : "v"(a), "i"(N), "i"(N + 3)); }
template <int N> DI void mfma_sc_(f32x4& acc, const bf16x8& a, const f32x4& c) { asm volatile("v_mfma_f32_16x16x32_bf16 %0, %1, a[%c2:%c3], %4" : "=&v"(acc) : "v"(a), "i"(N), "i"(N + 3), "v"(c)); }
template <int N> DI float ard_() { float r; asm volatile("v_accvgpr_read_b32 %0, a[%c1]" : "=v"(r) : "i"(N)); return r; }
template <int N, class T> DI void awr_(T src) { asm volatile("v_accvgpr_write_b32 a[%c1], %0" :: "v"(src), "i"(N)); }
template <int N> DI void ascale_(float f) { float t; asm volatile("v_accvgpr_read_b32 %0, a[%c1]\n\tv_mul_f32 %0, %2, %0\n\tv_accvgpr_write_b32 a[%c1], %0" : "=&v"(t) : "i"(N), "v"(f)); }
DI float subip_(float x, float d) { asm volatile("v_sub_f32 %0, %0, %1" : "+v"(x) : "v"(d)); return x; }
DI void mfma_ip_(f32x4& x, const bf16x8& a, const bf16x8& b) { asm volatile("v_mfma_f32_16x16x32_bf16 %0, %1, %2, %0" : "+v"(x) : "v"(a), "v"(b)); }
template <class T> DI T pin_(T x) { asm volatile("" : "+v"(x)); return x; }
template <class T> DI void pin3n_(T& a, T& b, T& c) { asm volatile("s_nop 1" : "+v"(a), "+v"(b), "+v"(c)); }
#define MFMA_O(d, q, a, b) mfma_o_<AO(d, q)>(a, b)
#define MFMA_O0(d, q, a, b) mfma_o0_<AO(d, q)>(a, b)
#define MFMA_S(acc, a, s, q) mfma_s_<AQ(s, q)>(acc, a)
#define MFMA_SC(acc, a, s, q, c) mfma_sc_<AQ(s, q)>(acc, a, c)
#define ARD(dst, n) dst = ard_<n>()
#define AWR(n, src) awr_<n>(src)
#define ASCALE(n, f) ascale_<n>(f)
#define SUBIP(x, d) x = subip_(x, d)
#define NOPS(n) asm volatile("s_nop " #n ::: "memory")
#define SBAR() __builtin_amdgcn_sched_barrier(0)
#define PINF(x) x = pin_(x)
#define PINU(x) x = pin_(x)
#define WAITV_BAR(N) asm volatile("s_waitcnt vmcnt(" #N ") lgkmcnt(0)\n\ts_barrier" ::: "memory")

DI unsigned cvtpk(float lo, float hi) {
  typedef float f32x2 __attribute__((ext_vector_type(2)));
  typedef __bf16 bf16x2 __attribute__((ext_vector_type(2)));
  f32x2 v = {lo, hi}; bf16x2 b = __builtin_convertvector(v, bf16x2); return __builtin_bit_cast(unsigned, b);
}
DI void glds16(unsigned voff, const char* sbase, unsigned ldsaddr) {
  asm volatile("s_mov_b32 m0, %2\n\ts_nop 0\n\tglobal_load_lds_dwordx4 %0, %1" :: "v"(voff), "s"(sbase), "s"(ldsaddr) : "memory");
}
DI bf16x8 ldsb128(unsigned p) { return *(const __attribute__((address_space(3))) bf16x8*)p; }
DI s16x4 ldstr(unsigned p) { return __builtin_bit_cast(s16x4, __builtin_amdgcn_ds_read_tr16_b64_v4i16((__attribute__((address_space(3))) v4i16_t*)p)); }
DI unsigned opq(unsigned x) { asm volatile("" : "+v"(x)); return x; }
DI bf16x8 cat8(s16x4 lo, s16x4 hi) { return __builtin_shufflevector(lo, hi, 0, 1, 2, 3, 4, 5, 6, 7); }
DI float mx3(float a, float b, float c) { float r; asm("v_max3_f32 %0, %1, %2, %3" : "=v"(r) : "v"(a), "v"(b), "v"(c)); return r; }
DI float mx2(float a, float b) { float r; asm("v_max_f32_e32 %0, %1, %2" : "=v"(r) : "v"(a), "v"(b)); return r; }
DI float xmax(float v) {
  auto a = __builtin_amdgcn_permlane32_swap(__float_as_uint(v), __float_as_uint(v), false, false);
  v = mx2(__uint_as_float(a[0]), __uint_as_float(a[1]));
  auto b = __builtin_amdgcn_permlane16_swap(__float_as_uint(v), __float_as_uint(v), false, false);
  return mx2(__uint_as_float(b[0]), __uint_as_float(b[1]));
}
DI float xsum(float v) {
  auto a = __builtin_amdgcn_permlane32_swap(__float_as_uint(v), __float_as_uint(v), false, false);
  v = __uint_as_float(a[0]) + __uint_as_float(a[1]);
  auto b = __builtin_amdgcn_permlane16_swap(__float_as_uint(v), __float_as_uint(v), false, false);
  return __uint_as_float(b[0]) + __uint_as_float(b[1]);
}

template <int N> struct IC { static constexpr int value = N; };
#define AI __attribute__((always_inline))
template <class F, int... Is> DI void sfor_(F&& f, std::integer_sequence<int, Is...>) { (f(IC<Is>{}), ...); }
template <int N, class F> DI void sfor(F&& f) { sfor_(f, std::make_integer_sequence<int, N>{}); }
struct Ptrs { const char* x[2]; const char* k[2]; const char* v[2]; const char* wq[2]; const char* wo[2]; };

__global__ __launch_bounds__(256, 1) void k_attn(Ptrs P, char* __restrict__ ofr) {
  extern __shared__ __attribute__((aligned(16))) char smem[];
  lds_ptr lds = (lds_ptr)smem;
  const int tid = threadIdx.x, l = tid & 63, w = __builtin_amdgcn_readfirstlane(tid >> 6);
  const int i16 = l & 15, g = l >> 4, qq = i16 >> 2, pp = i16 & 3;
  const int xcd = blockIdx.x & 7, idx = blockIdx.x >> 3;
  const int br = xcd >> 2, h = 8 * (xcd & 3) + (idx & 7), b = idx >> 3;
  const char* xg = P.x[br] + (size_t)b * SQ * DM * 2;
  const char* wg = P.wq[br] + (size_t)h * DH * 2;
  const char* kg = P.k[br] + (size_t)(b * NH + h) * SK * DH * 2;
  const char* vg = P.v[br] + (size_t)(b * NH + h) * SK * DH * 2;

  if (blockIdx.x == 0 && tid < 64) ((unsigned*)(ofr + ((size_t)48 << 20)))[tid * 16] = 0u;
  unsigned offK[3], offV[3], offW[3], offX;
#pragma unroll
  for (int u = 0; u < 3; ++u) {
    const int o = 1024 * u + 16 * l, r = o / 384, chp = (o % 384) >> 4, row = 8 * w + r;
    const int aK = (row >> 1) & 7;
    offK[u] = row * 384 + (((chp & ~7) | ((chp ^ aK) & 7)) << 4);
    offV[u] = row * 384 + ((chp ^ (((row >> 1) & 3) << 1)) << 4);
    const int sW = ((row >> 1) & 1) | (((row >> 3) & 1) << 1);
    offW[u] = row * (HD * 2) + ((chp ^ (sW << 1)) << 4);
  }
  { const int row = 8 * w + (l >> 3), a = (row >> 1) & 7; offX = row * (DM * 2) + (((l & 7) ^ a) << 4); }

  const unsigned oK0 = offK[0], oK1 = offK[1], oK2 = offK[2], oV0 = offV[0], oV1 = offV[1], oV2 = offV[2], oW0 = offW[0], oW1 = offW[1], oW2 = offW[2];
  const unsigned lds0 = (unsigned)(uintptr_t)lds;
  auto px = [&](int c, int slot, auto N) AI {
    constexpr int n = N.value;
    glds16(offX, xg + c * 128 + (size_t)n * 32 * DM * 2, lds0 + slot * SLOT + w * 1024 + n * 4096);
  };
  auto pt = [&](const char* s, size_t half, unsigned o0, unsigned o1, unsigned o2, int slot, auto N) AI {
    constexpr int n = N.value, u = n % 3, hf = n / 3;
    glds16(u == 0 ? o0 : u == 1 ? o1 : o2, s + hf * half, lds0 + slot * SLOT + w * 3072 + hf * 12288 + u * 1024);
  };
  auto pw_ = [&](int c, int slot, auto N) AI { pt(wg + (size_t)c * 64 * HD * 2, (size_t)32 * HD * 2, oW0, oW1, oW2, slot, N); };
  auto pk = [&](int j, int slot, auto N) AI { pt(kg + (size_t)j * SLOT, 12288, oK0, oK1, oK2, slot, N); };
  auto pv = [&](int j, int slot, auto N) AI { pt(vg + (size_t)j * SLOT, 12288, oV0, oV1, oV2, slot, N); };
  auto pkv = [&](auto F, auto N) AI {
    constexpr int f = F.value;
    if constexpr ((f & 1) == 0) pk((f - 20) >> 1, f % NSLOT, N); else pv((f - 20) >> 1, f % NSLOT, N);
  };
  auto fill_x = [&](int c, int slot) AI { sfor<6>([&](auto N) AI { px(c, slot, N); }); };
  auto fill_w = [&](int c, int slot) AI { sfor<6>([&](auto N) AI { pw_(c, slot, N); }); };
  auto issue_kv = [&](auto F) AI { sfor<6>([&](auto N) AI { pkv(F, N); }); };

  const int a8 = i16 >> 1;
  const unsigned kb0 = lds0 + i16 * 384 + ((g ^ a8) << 4);
  const unsigned xb0 = lds0 + (48 * w + i16) * 128 + ((g ^ a8) << 4);
  const int swz = 2 * (g & 1) + (qq >> 1);
  const unsigned vb0 = lds0 + (4 * g + qq) * 384 + (swz << 5) + 8 * pp;
  const unsigned wb0 = lds0 + (8 * g + qq) * 384 + (swz << 5) + 8 * pp;

  {
    asm volatile("" ::: ACLOB);
    fill_x(0, 0); fill_w(0, 1); fill_x(1, 2); fill_w(1, 3);
    int sl = 0;
    { float z = 0.f; PINF(z); sfor<144>([&](auto N) AI { AWR(N.value, z); }); }
    NOPS(1);
#pragma unroll 1
    for (int c = 0; c < 10; ++c) {
      WAITV_BAR(12);
      const int sn = sl >= 2 ? sl - 2 : sl + 4;
      unsigned ax[2], aw[4];
      ax[0] = opq(xb0 + sl * SLOT); ax[1] = opq((xb0 ^ 64) + sl * SLOT);
#pragma unroll
      for (int d = 0; d < 4; ++d) aw[d] = opq((wb0 ^ (d << 5)) + (sl + 1) * SLOT);
      sl = sl == 4 ? 0 : sl + 2;
      bf16x8 xf[2][3], wf[3];
      auto rdw = [&](auto F) AI {
        constexpr int f = F.value, s = f / 12, d = f % 12;
        const unsigned p = aw[d & 3] + s * 32 * 384 + (d >> 2) * 128;
        wf[f % 3] = cat8(ldstr(p), ldstr(p + 4 * 384));
      };
#pragma unroll
      for (int q = 0; q < 3; ++q) xf[0][q] = ldsb128(ax[0] + q * 2048);
      rdw(IC<0>{}); rdw(IC<1>{});
      SBAR();
      sfor<24>([&](auto F) AI {
        constexpr int f = F.value, s = f / 12, d = f % 12;
        if constexpr (f + 2 < 24) rdw(IC<f + 2>{});
        if constexpr (f >= 2 && f < 5) xf[1][f - 2] = ldsb128(ax[1] + (f - 2) * 2048);
        MFMA_O(d, 0, wf[f % 3], xf[s][0]); MFMA_O(d, 1, wf[f % 3], xf[s][1]); MFMA_O(d, 2, wf[f % 3], xf[s][2]);
        if constexpr ((f & 1) == 0) {
          constexpr int n = f >> 1;
          if constexpr (n < 6) { if (c < 8) px(c + 2, sn, IC<n>{}); else pk(c - 8, sn, IC<n>{}); }
          else { if (c < 8) pw_(c + 2, sn + 1, IC<n - 6>{}); else pv(c - 8, sn + 1, IC<n - 6>{}); }
        }
        SBAR();
      });
    }
    NOPS(7);
    sfor<18>([&](auto I) AI {
      constexpr int s = I.value / 3, q = I.value % 3;
      f32x4 X, Y;
      sfor<4>([&](auto R) AI { ARD(X[R.value], AO(2 * s, q) + R.value); ARD(Y[R.value], AO(2 * s + 1, q) + R.value); });
      X *= QSCALE; Y *= QSCALE;
      unsigned x0 = cvtpk(X[0], X[1]), x1 = cvtpk(X[2], X[3]), y0 = cvtpk(Y[0], Y[1]), y1 = cvtpk(Y[2], Y[3]);
      auto r0 = __builtin_amdgcn_permlane32_swap(x0, y0, false, false);
      auto r1 = __builtin_amdgcn_permlane32_swap(x1, y1, false, false);
      auto t0 = __builtin_amdgcn_permlane16_swap(r0[0], r0[1], false, false);
      auto t1 = __builtin_amdgcn_permlane16_swap(r1[0], r1[1], false, false);
      AWR(AQ(s, q) + 0, t0[0]); AWR(AQ(s, q) + 1, t1[0]); AWR(AQ(s, q) + 2, t0[1]); AWR(AQ(s, q) + 3, t1[1]);
    });
    NOPS(1);
  }

  f32x4 sc[2][4][3];
  f32x4 negm[3];
  unsigned pw[2][3][4];
  bf16x8 ones = {0x3F80, 0x3F80, 0x3F80, 0x3F80, 0x3F80, 0x3F80, 0x3F80, 0x3F80};
  asm volatile("" : "+v"(ones));

#define EXP1(X, kb, q, r) do { sc[X][kb][q][r] = __builtin_amdgcn_exp2f(sc[X][kb][q][r]); PINF(sc[X][kb][q][r]); } while (0)
#define CVT1(X, c) do { constexpr int kb_ = (c) / 6, q_ = ((c) % 6) >> 1, hf_ = (c) & 1; \
    pw[kb_ >> 1][q_][2 * (kb_ & 1) + hf_] = cvtpk(sc[X][kb_][q_][2 * hf_], sc[X][kb_][q_][2 * hf_ + 1]); PINU(pw[kb_ >> 1][q_][2 * (kb_ & 1) + hf_]); } while (0)

  WAITV_BAR(18);
  issue_kv(IC<24>{}); issue_kv(IC<25>{});
  {
    unsigned ak[2];
    ak[0] = opq(kb0 + 2 * SLOT); ak[1] = opq((kb0 ^ 64) + 2 * SLOT);
#pragma unroll
    for (int q = 0; q < 3; ++q) negm[q] = f32x4{0.f, 0.f, 0.f, 0.f};
    asm volatile("s_nop 1" : "+v"(negm[0]), "+v"(negm[1]), "+v"(negm[2]));
    bf16x8 kf[3];
    auto rdk = [&](auto F) AI { constexpr int f = F.value, s = f >> 2, kb = f & 3; kf[f % 3] = ldsb128(ak[s & 1] + kb * 6144 + (s >> 1) * 128); };
    rdk(IC<0>{}); rdk(IC<1>{});
    SBAR();
    sfor<24>([&](auto F) AI {
      constexpr int f = F.value, s = f >> 2, kb = f & 3;
      if constexpr (f + 2 < 24) rdk(IC<f + 2>{});
      sfor<3>([&](auto Q) AI {
        constexpr int q = Q.value;
        if constexpr (s == 0) { MFMA_SC(sc[0][kb][q], kf[f % 3], s, q, negm[q]); } else { MFMA_S(sc[0][kb][q], kf[f % 3], s, q); }
      });
      SBAR();
    });
    asm volatile("s_nop 7" : "+v"(sc[0][0][0]), "+v"(sc[0][0][1]), "+v"(sc[0][0][2]), "+v"(sc[0][1][0]), "+v"(sc[0][1][1]), "+v"(sc[0][1][2]),
                             "+v"(sc[0][2][0]), "+v"(sc[0][2][1]), "+v"(sc[0][2][2]), "+v"(sc[0][3][0]), "+v"(sc[0][3][1]), "+v"(sc[0][3][2]));
#pragma unroll
    for (int q = 0; q < 3; ++q) {
      float m0 = mx3(sc[0][0][q][0], sc[0][0][q][1], sc[0][0][q][2]);
      m0 = mx2(m0, sc[0][0][q][3]);
#pragma unroll
      for (int kb = 1; kb < 4; ++kb) { m0 = mx3(m0, sc[0][kb][q][0], sc[0][kb][q][1]); m0 = mx3(m0, sc[0][kb][q][2], sc[0][kb][q][3]); }
      m0 = xmax(m0);
      negm[q] = f32x4{-m0, -m0, -m0, -m0};
#pragma unroll
      for (int kb = 0; kb < 4; ++kb) sc[0][kb][q] -= m0;
#pragma unroll
      for (int r = 0; r < 4; ++r) sc[0][0][q][r] = __builtin_amdgcn_exp2f(sc[0][0][q][r]);
    }
  }
  sfor<8>([&](auto Jc) AI {
    constexpr int j = Jc.value;
    constexpr int X = j & 1, Y = X ^ 1;
    if constexpr (j == 0) { WAITV_BAR(18); } else if constexpr (j < 6) { WAITV_BAR(12); } else if constexpr (j == 6) { WAITV_BAR(6); } else { WAITV_BAR(0); }
    if constexpr (j < 7) {
      unsigned ak[2];
      ak[0] = opq(kb0 + ((22 + 2 * j) % NSLOT) * SLOT); ak[1] = opq((kb0 ^ 64) + ((22 + 2 * j) % NSLOT) * SLOT);
      pin3n_(negm[0], negm[1], negm[2]);
      bf16x8 kf[3];
      auto rdk = [&](auto F) AI { constexpr int f = F.value, s = f >> 2, kb = f & 3; kf[f % 3] = ldsb128(ak[s & 1] + kb * 6144 + (s >> 1) * 128); };
      rdk(IC<0>{}); rdk(IC<1>{});
      SBAR();
      sfor<24>([&](auto F) AI {
        constexpr int f = F.value, s = f >> 2, kb = f & 3;
        if constexpr (f + 2 < 24) rdk(IC<f + 2>{});
        sfor<3>([&](auto Q) AI {
          constexpr int q = Q.value;
          if constexpr (s == 0) { MFMA_SC(sc[Y][kb][q], kf[f % 3], s, q, negm[q]); } else { MFMA_S(sc[Y][kb][q], kf[f % 3], s, q); }
          if constexpr (q == 0) { CVT1(X, f); }
          else {
            constexpr int e = 2 * f + q - 1;
            if constexpr (e < 36) { EXP1(X, 1 + e / 12, (e % 12) >> 2, e & 3); }
          }
          SBAR();
        });
        if constexpr ((f & 3) == 1 && j >= 1 && 25 + 2 * j <= 35) pkv(IC<25 + 2 * j>{}, IC<(f >> 2)>{});
        SBAR();
      });
    } else {
      sfor<36>([&](auto E) AI { constexpr int e = E.value; EXP1(X, 1 + e / 12, (e % 12) >> 2, e & 3); });
      sfor<24>([&](auto C) AI { CVT1(X, C.value); });
    }
    {
      bf16x8 pf[2][3];
#pragma unroll
      for (int t = 0; t < 2; ++t)
#pragma unroll
        for (int q = 0; q < 3; ++q) { u32x4 f = {pw[t][q][0], pw[t][q][1], pw[t][q][2], pw[t][q][3]}; pf[t][q] = __builtin_bit_cast(bf16x8, f); }
      pin3n_(pf[0][0], pf[0][1], pf[0][2]); pin3n_(pf[1][0], pf[1][1], pf[1][2]);
      unsigned av[4];
#pragma unroll
      for (int d = 0; d < 4; ++d) av[d] = opq((vb0 ^ (d << 5)) + ((21 + 2 * j) % NSLOT) * SLOT);
      bf16x8 vf[3];
      auto rdv = [&](auto F) AI {
        constexpr int f = F.value, t = f / 12, d = f % 12;
        const unsigned p = av[d & 3] + t * 32 * 384 + (d >> 2) * 128;
        vf[f % 3] = cat8(ldstr(p), ldstr(p + 16 * 384));
      };
      float mx[3], gr[3], fct[3];
      bool pend = false;
      rdv(IC<0>{}); rdv(IC<1>{});
      SBAR();
      sfor<26>([&](auto Gc) AI {
        constexpr int G = Gc.value, t = G / 13, gi = G % 13;
        if constexpr (gi == 0) {
          sfor<3>([&](auto Q) AI { constexpr int q = Q.value;
            if constexpr (j == 0 && t == 0) { MFMA_O0(12, q, ones, pf[t][q]); } else { MFMA_O(12, q, ones, pf[t][q]); } });
        } else {
          constexpr int d = gi - 1, f = 12 * t + d;
          if constexpr (f + 2 < 24) rdv(IC<f + 2>{});
          sfor<3>([&](auto Q) AI { constexpr int q = Q.value;
            if constexpr (j == 0 && t == 0) { MFMA_O0(d, q, vf[f % 3], pf[t][q]); } else { MFMA_O(d, q, vf[f % 3], pf[t][q]); } });
        }
        if constexpr (j < 7) {
          if constexpr (G >= 2 && G < 8) {
            constexpr int q = (G - 2) >> 1, kb = 2 * ((G - 2) & 1);
            if constexpr (kb == 0) {
              mx[q] = mx3(sc[Y][0][q][0], sc[Y][0][q][1], sc[Y][0][q][2]); mx[q] = mx3(mx[q], sc[Y][0][q][3], sc[Y][1][q][0]);
              mx[q] = mx3(mx[q], sc[Y][1][q][1], sc[Y][1][q][2]); mx[q] = mx2(mx[q], sc[Y][1][q][3]);
            } else {
              mx[q] = mx3(mx[q], sc[Y][2][q][0], sc[Y][2][q][1]); mx[q] = mx3(mx[q], sc[Y][2][q][2], sc[Y][2][q][3]);
              mx[q] = mx3(mx[q], sc[Y][3][q][0], sc[Y][3][q][1]); mx[q] = mx3(mx[q], sc[Y][3][q][2], sc[Y][3][q][3]);
            }
            PINF(mx[q]);
          }
          if constexpr (G >= 8 && G < 11) { gr[G - 8] = xmax(mx[G - 8]); PINF(gr[G - 8]); }
          if constexpr (G == 11) {
            pend = __any((gr[0] > THR) | (gr[1] > THR) | (gr[2] > THR));
            if (__builtin_expect(pend, 0)) {
#pragma unroll
              for (int q = 0; q < 3; ++q) {
                const unsigned db = (__float_as_uint(fmaxf(gr[q], 0.f)) + 0xFFFFu) & 0xFFFF0000u;
                fct[q] = __builtin_amdgcn_exp2f(-__uint_as_float(db));
                u32x4 bw = {g == 0 ? ((db >> 16) | 0x8000u) : 0u, 0u, 0u, 0u};
                bf16x8 bfr = __builtin_bit_cast(bf16x8, bw);
                asm volatile("s_nop 1" : "+v"(bfr));
                mfma_ip_(negm[q], ones, bfr);
                mfma_ip_(sc[Y][0][q], ones, bfr); mfma_ip_(sc[Y][1][q], ones, bfr); mfma_ip_(sc[Y][2][q], ones, bfr); mfma_ip_(sc[Y][3][q], ones, bfr);
              }
              NOPS(7);
            }
          }
          if constexpr (G >= 12 && G < 24) { constexpr int e = G - 12; EXP1(Y, 0, e >> 2, e & 3); }
        }
        if constexpr ((G & 3) == 1 && G < 24 && 26 + 2 * j <= 35) pkv(IC<26 + 2 * j>{}, IC<(G >> 2)>{});
        SBAR();
      });
      if constexpr (j < 7) {
        if (__builtin_expect(pend, 0)) {
          NOPS(7);
          sfor<156>([&](auto N) AI { ASCALE(N.value, fct[(N.value >> 2) % 3]); });
          NOPS(1);
        }
      }
    }
  });
  NOPS(7);
  sfor<3>([&](auto Q) AI {
    constexpr int q = Q.value;
    float lq; ARD(lq, AO(12, q));
    const float inv = 1.0f / lq;
    char* dst = ofr + ((((size_t)(br * NB + b) * 12 + (3 * w + q)) * 192 + h * 6) * 64 + l) * 16;
    sfor<6>([&](auto T) AI {
      constexpr int t = T.value;
      f32x4 X, Y;
      sfor<4>([&](auto R) AI { ARD(X[R.value], AO(2 * t, q) + R.value); ARD(Y[R.value], AO(2 * t + 1, q) + R.value); });
      X *= inv; Y *= inv;
      u32x4 f = {cvtpk(X[0], X[1]), cvtpk(X[2], X[3]), cvtpk(Y[0], Y[1]), cvtpk(Y[2], Y[3])};
      *(u32x4*)(dst + t * 1024) = f;
    });
  });
}

constexpr int ST2 = 128 * 320;
constexpr int LDS2 = 3 * ST2;
constexpr size_t WS_SLAB = (size_t)64 << 20;
constexpr size_t WS_CNT = (size_t)48 << 20;

DI void gload16(bf16x8& d, unsigned voff, const char* sbase) { asm volatile("global_load_dwordx4 %0, %1, %2" : "=v"(d) : "v"(voff), "s"(sbase) : "memory"); }
template <int N> DI void wait3(bf16x8& a, bf16x8& b, bf16x8& c) { asm volatile("s_waitcnt vmcnt(%3)" : "+v"(a), "+v"(b), "+v"(c) : "i"(N) : "memory"); }

__global__ __launch_bounds__(512, 2) void k_oproj(Ptrs P, char* __restrict__ ws, char* __restrict__ out) {
  extern __shared__ __attribute__((aligned(16))) char smem[];
  lds_ptr lds = (lds_ptr)smem;
  const int tid = threadIdx.x, l = tid & 63, w = __builtin_amdgcn_readfirstlane(tid >> 6);
  const int i16 = l & 15, g = l >> 4, qq = i16 >> 2, pp = i16 & 3;
  const int xcd = blockIdx.x & 7, idx = blockIdx.x >> 3;
  const int br = xcd >> 2, ks = xcd & 3, b = idx >> 2, nq = idx & 3;
  const int kh = w >> 2, qg = w & 3;
  const unsigned lds0 = (unsigned)(uintptr_t)lds;
  const char* wsrc = P.wo[br] + (size_t)ks * 1536 * DM * 2 + nq * 320;
  const char* osrc = ws + (((size_t)(br * NB + b) * 12 + 3 * qg) * 192 + 48 * ks + kh) * 1024;
  const unsigned l16 = l * 16;
  unsigned offW[5];
#pragma unroll
  for (int u = 0; u < 5; ++u) {
    const int o = 1024 * u + 16 * l, r = o / 320, chp = (o % 320) >> 4;
    offW[u] = (16 * w + r) * (DM * 2) + ((chp ^ (((r >> 2) & 1) << 1)) << 4);
  }
  const unsigned o0 = offW[0], o1 = offW[1], o2 = offW[2], o3 = offW[3], o4 = offW[4];
  auto issueW = [&](int s, int stg) AI {
    const unsigned d = lds0 + stg * ST2 + w * 5120;
    const char* src = wsrc + (size_t)s * 128 * DM * 2;
    glds16(o0, src, d); glds16(o1, src, d + 1024); glds16(o2, src, d + 2048); glds16(o3, src, d + 3072); glds16(o4, src, d + 4096);
  };
  bf16x8 of[4][3];
  auto loadO = [&](int u, auto Bc) AI {
    constexpr int B_ = Bc.value;
    const char* src = osrc + (size_t)u * 2048;
    gload16(of[B_][0], l16, src); gload16(of[B_][1], l16, src + 192 * 1024); gload16(of[B_][2], l16, src + 2 * 192 * 1024);
  };
  f32x4 acc[10][3];
#pragma unroll
  for (int n = 0; n < 10; ++n)
#pragma unroll
    for (int q = 0; q < 3; ++q) acc[n][q] = f32x4{0.f, 0.f, 0.f, 0.f};
  const unsigned wb0 = lds0 + (32 * kh + 4 * g + qq) * 320 + 8 * pp + ((g & 1) << 5);
  issueW(0, 0); loadO(0, IC<0>{}); loadO(1, IC<1>{}); loadO(2, IC<2>{});
  issueW(1, 1);
  int stg = 0;
  auto kstep = [&](unsigned ae, unsigned ao, auto Vc, auto Bc) AI {
    constexpr int v = Vc.value, B_ = Bc.value;
    bf16x8 wf[3];
    auto rdw = [&](auto N) AI { constexpr int n = N.value; const unsigned p = ((n & 1) ? ao : ae) + v * 64 * 320 + (n >> 1) * 64; wf[n % 3] = cat8(ldstr(p), ldstr(p + 16 * 320)); };
    rdw(IC<0>{}); rdw(IC<1>{});
    sfor<10>([&](auto N) AI {
      constexpr int n = N.value;
      if constexpr (n + 2 < 10) rdw(IC<n + 2>{});
#pragma unroll
      for (int q = 0; q < 3; ++q) acc[n][q] = MFMA16(wf[n % 3], of[B_][q], acc[n][q]);
    });
  };
  auto stage = [&](int s, auto Pc, auto Mc) AI {
    constexpr int PAR = Pc.value, MODE = Mc.value, B0 = 2 * PAR, B1 = 2 * PAR + 1;
    if constexpr (MODE == 2) { asm volatile("s_waitcnt vmcnt(6) lgkmcnt(0)\n\ts_barrier" ::: "memory"); }
    else { asm volatile("s_waitcnt vmcnt(11) lgkmcnt(0)\n\ts_barrier" ::: "memory"); }
    const unsigned ae = opq(wb0 + stg * ST2), ao = opq((wb0 ^ 32) + stg * ST2);
    if constexpr (MODE == 0) issueW(s + 2, stg == 0 ? 2 : stg - 1);
    if constexpr (MODE <= 1) loadO(2 * s + 3, IC<(B0 + 3) & 3>{});
    wait3<MODE == 0 ? 19 : MODE == 1 ? 14 : 3>(of[B0][0], of[B0][1], of[B0][2]);
    kstep(ae, ao, IC<0>{}, IC<B0>{});
    if constexpr (MODE == 0) loadO(2 * s + 4, IC<B0>{});
    wait3<MODE == 0 ? 14 : MODE == 1 ? 6 : 0>(of[B1][0], of[B1][1], of[B1][2]);
    kstep(ae, ao, IC<1>{}, IC<B1>{});
    stg = stg == 2 ? 0 : stg + 1;
  };
#pragma unroll 1
  for (int s = 0; s < 10; s += 2) { stage(s, IC<0>{}, IC<0>{}); stage(s + 1, IC<1>{}, IC<0>{}); }
  stage(10, IC<0>{}, IC<1>{}); stage(11, IC<1>{}, IC<2>{});
  asm volatile("s_waitcnt vmcnt(0) lgkmcnt(0)\n\ts_barrier" ::: "memory");
  {
    const unsigned xo = lds0 + ((kh ^ 1) * 4 + qg) * 15360 + l16, xi = lds0 + (kh * 4 + qg) * 15360 + l16;
#pragma unroll
    for (int n = 0; n < 5; ++n)
#pragma unroll
      for (int q = 0; q < 3; ++q) *(__attribute__((address_space(3))) f32x4*)(xo + (n * 3 + q) * 1024) = kh ? acc[n][q] : acc[5 + n][q];
    __syncthreads();
#pragma unroll
    for (int n = 0; n < 5; ++n)
#pragma unroll
      for (int q = 0; q < 3; ++q) {
        const f32x4 o = *(const __attribute__((address_space(3))) f32x4*)(xi + (n * 3 + q) * 1024);
        acc[n][q] = (kh ? acc[5 + n][q] : acc[n][q]) + o;
      }
  }
  const int tile = (br * NB + b) * 4 + nq;
  char* slab = ws + WS_SLAB + (size_t)tile * 4 * 8 * 15360;
  {
    char* mine = slab + (size_t)(ks * 8 + w) * 15360 + l16;
#pragma unroll
    for (int i = 0; i < 15; ++i)
      if ((i & 3) != ks) asm volatile("global_store_dwordx4 %0, %1, off sc0 sc1\n\ts_nop 1" :: "v"(mine + i * 1024), "v"(acc[i / 3][i % 3]) : "memory");
    asm volatile("s_waitcnt vmcnt(0)" ::: "memory");
    __syncthreads();
    unsigned* cnt = (unsigned*)(ws + WS_CNT) + tile * 16;
    if (tid == 0) {
      __hip_atomic_fetch_add(cnt, 1u, __ATOMIC_RELAXED, __HIP_MEMORY_SCOPE_AGENT);
      unsigned spins = 0;
      while (__hip_atomic_load(cnt, __ATOMIC_RELAXED, __HIP_MEMORY_SCOPE_AGENT) < 4u && ++spins < (1u << 24)) __builtin_amdgcn_s_sleep(1);
    }
    __syncthreads();
  }
  {
    const char* theirs = slab + (size_t)w * 15360 + l16;
#pragma unroll
    for (int i = 0; i < 15; ++i) {
      if ((i & 3) == ks) {
        f32x4 p[3];
#pragma unroll
        for (int r = 0; r < 3; ++r) {
          const int src = (ks + 1 + r) & 3;
          asm volatile("global_load_dwordx4 %0, %1, off sc0 sc1" : "=v"(p[r]) : "v"(theirs + (size_t)src * 8 * 15360 + i * 1024) : "memory");
        }
        asm volatile("s_waitcnt vmcnt(0)" : "+v"(p[0]), "+v"(p[1]), "+v"(p[2]) :: "memory");
        const f32x4 o = (acc[i / 3][i % 3] + p[0]) + (p[1] + p[2]);
        u32x2 f = {cvtpk(o[0], o[1]), cvtpk(o[2], o[3])};
        char* dst = out + ((size_t)(b * SQ + (3 * qg + i % 3) * 16 + i16) * (2 * DM) + br * DM + nq * 160 + (5 * kh + i / 3) * 16 + 4 * g) * 2;
        *(u32x2*)dst = f;
      }
    }
  }
}

extern "C" void kernel_launch(void* const* d_in, const int* in_sizes, int n_in, void* d_out, int out_size, void* d_ws, size_t ws_size,
                              hipStream_t stream) {
  Ptrs P;
  for (int r = 0; r < 2; ++r) {
    P.x[r] = (const char*)d_in[5 * r + 0];
    P.k[r] = (const char*)d_in[5 * r + 1];
    P.v[r] = (const char*)d_in[5 * r + 2];
    P.wq[r] = (const char*)d_in[5 * r + 3];
    P.wo[r] = (const char*)d_in[5 * r + 4];
  }
  (void)hipFuncSetAttribute((const void*)k_attn, hipFuncAttributeMaxDynamicSharedMemorySize, LDS1);
  (void)hipFuncSetAttribute((const void*)k_oproj, hipFuncAttributeMaxDynamicSharedMemorySize, LDS2);
  hipLaunchKernelGGL(k_attn, dim3(512), dim3(256), LDS1, stream, P, (char*)d_ws);
  hipLaunchKernelGGL(k_oproj, dim3(256), dim3(512), LDS2, stream, P, (char*)d_ws, (char*)d_out);
}
```

```cpp
#include <hip/hip_runtime.h>
#include <hip/hip_bf16.h>
#include <stdint.h>
#include <utility>

using bf16x8 = __attribute__((ext_vector_type(8))) short;
using s16x4 = __attribute__((ext_vector_type(4))) short;
using f32x4 = __attribute__((ext_vector_type(4))) float;
using u32x4 = __attribute__((ext_vector_type(4))) unsigned;
using u32x2 = __attribute__((ext_vector_type(2))) unsigned;
typedef short v4i16_t __attribute__((ext_vector_type(4)));
typedef __attribute__((address_space(3))) char* lds_ptr;
typedef __attribute__((address_space(3))) const char* lds_cptr;

constexpr int NB = 8, SQ = 192, DM = 640, NH = 32, SK = 512, DH = 192;
constexpr int HD = NH * DH;
constexpr int SLOT = 24576;
constexpr int NSLOT = 6;
constexpr int LDS1 = SLOT * NSLOT;
constexpr float QSCALE = 0.07216878364870322f * 1.4426950408889634f;
constexpr float THR = 8.0f;

#define DI __device__ __forceinline__
#define MFMA16(a, b, c) __builtin_amdgcn_mfma_f32_16x16x32_bf16((a), (b), (c), 0, 0, 0)
#define ACLOB "a0","a1","a2","a3","a4","a5","a6","a7","a8","a9","a10","a11","a12","a13","a14","a15","a16","a17","a18","a19","a20","a21","a22","a23","a24","a25","a26","a27","a28","a29","a30","a31","a32","a33","a34","a35","a36","a37","a38","a39","a40","a41","a42","a43","a44","a45","a46","a47","a48","a49","a50","a51","a52","a53","a54","a55","a56","a57","a58","a59","a60","a61","a62","a63","a64","a65","a66","a67","a68","a69","a70","a71","a72","a73","a74","a75","a76","a77","a78","a79","a80","a81","a82","a83","a84","a85","a86","a87","a88","a89","a90","a91","a92","a93","a94","a95","a96","a97","a98","a99","a100","a101","a102","a103","a104","a105","a106","a107","a108","a109","a110","a111","a112","a113","a114","a115","a116","a117","a118","a119","a120","a121","a122","a123","a124","a125","a126","a127","a128","a129","a130","a131","a132","a133","a134","a135","a136","a137","a138","a139","a140","a141","a142","a143","a144","a145","a146","a147","a148","a149","a150","a151","a152","a153","a154","a155","a156","a157","a158","a159","a160","a161","a162","a163","a164","a165","a166","a167","a168","a169","a170","a171","a172","a173","a174","a175","a176","a177","a178","a179","a180","a181","a182","a183","a184","a185","a186","a187","a188","a189","a190","a191","a192","a193","a194","a195","a196","a197","a198","a199","a200","a201","a202","a203","a204","a205","a206","a207","a208","a209","a210","a211","a212","a213","a214","a215","a216","a217","a218","a219","a220","a221","a222","a223","a224","a225","a226","a227"
#define AO(d, q) (4 * (3 * (d) + (q)))
#define AQ(s, q) (156 + 4 * (3 * (s) + (q)))
template <int N> DI void mfma_o_(const bf16x8& a, const bf16x8& b) { asm volatile("v_mfma_f32_16x16x32_bf16 a[%c2:%c3], %0, %1, a[%c2:%c3]" :: "v"(a), "v"(b), "i"(N), "i"(N + 3)); }
template <int N> DI void mfma_o0_(const bf16x8& a, const bf16x8& b) { asm volatile("v_mfma_f32_16x16x32_bf16 a[%c2:%c3], %0, %1, 0" :: "v"(a), "v"(b), "i"(N), "i"(N + 3)); }
template <int N> DI void mfma_s_(f32x4& acc, const bf16x8& a) { asm volatile("v_mfma_f32_16x16x32_bf16 %0, %1, a[%c2:%c3], %0" : "+v"(acc) : "v"(a), "i"(N), "i"(N + 3)); }
template <int N> DI void mfma_sc_(f32x4& acc, const bf16x8& a, const f32x4& c) { asm volatile("v_mfma_f32_16x16x32_bf16 %0, %1, a[%c2:%c3], %4" : "=&v"(acc) : "v"(a), "i"(N), "i"(N + 3), "v"(c)); }
template <int N> DI float ard_() { float r; asm volatile("v_accvgpr_read_b32 %0, a[%c1]" : "=v"(r) : "i"(N)); return r; }
template <int N, class T> DI void awr_(T src) { asm volatile("v_accvgpr_write_b32 a[%c1], %0" :: "v"(src), "i"(N)); }
template <int N> DI void ascale_(float f) { float t; asm volatile("v_accvgpr_read_b32 %0, a[%c1]\n\tv_mul_f32 %0, %2, %0\n\tv_accvgpr_write_b32 a[%c1], %0" : "=&v"(t) : "i"(N), "v"(f)); }
DI float subip_(float x, float d) { asm volatile("v_sub_f32 %0, %0, %1" : "+v"(x) : "v"(d)); return x; }
DI void mfma_ip_(f32x4& x, const bf16x8& a, const bf16x8& b) { asm volatile("v_mfma_f32_16x16x32_bf16 %0, %1, %2, %0" : "+v"(x) : "v"(a), "v"(b)); }
template <class T> DI T pin_(T x) { asm volatile("" : "+v"(x)); return x; }
template <class T> DI void pin3n_(T& a, T& b, T& c) { asm volatile("s_nop 1" : "+v"(a), "+v"(b), "+v"(c)); }
#define MFMA_O(d, q, a, b) mfma_o_<AO(d, q)>(a, b)
#define MFMA_O0(d, q, a, b) mfma_o0_<AO(d, q)>(a, b)
#define MFMA_S(acc, a, s, q) mfma_s_<AQ(s, q)>(acc, a)
#define MFMA_SC(acc, a, s, q, c) mfma_sc_<AQ(s, q)>(acc, a, c)
#define ARD(dst, n) dst = ard_<n>()
#define AWR(n, src) awr_<n>(src)
#define ASCALE(n, f) ascale_<n>(f)
#define SUBIP(x, d) x = subip_(x, d)
#define NOPS(n) asm volatile("s_nop " #n ::: "memory")
#define SBAR() __builtin_amdgcn_sched_barrier(0)
#define PINF(x) x = pin_(x)
#define PINU(x) x = pin_(x)
#define WAITV_BAR(N) asm volatile("s_waitcnt vmcnt(" #N ") lgkmcnt(0)\n\ts_barrier" ::: "memory")

DI unsigned cvtpk(float lo, float hi) {
  typedef float f32x2 __attribute__((ext_vector_type(2)));
  typedef __bf16 bf16x2 __attribute__((ext_vector_type(2)));
  f32x2 v = {lo, hi}; bf16x2 b = __builtin_convertvector(v, bf16x2); return __builtin_bit_cast(unsigned, b);
}
DI void glds16(unsigned voff, const char* sbase, unsigned ldsaddr) {
  asm volatile("s_mov_b32 m0, %2\n\ts_nop 0\n\tglobal_load_lds_dwordx4 %0, %1" :: "v"(voff), "s"(sbase), "s"(ldsaddr) : "memory");
}
DI void glds16nt(unsigned voff, const char* sbase, unsigned ldsaddr) {
  asm volatile("s_mov_b32 m0, %2\n\ts_nop 0\n\tglobal_load_lds_dwordx4 %0, %1 nt" :: "v"(voff), "s"(sbase), "s"(ldsaddr) : "memory");
}
DI bf16x8 ldsb128(unsigned p) { return *(const __attribute__((address_space(3))) bf16x8*)p; }
DI s16x4 ldstr(unsigned p) { return __builtin_bit_cast(s16x4, __builtin_amdgcn_ds_read_tr16_b64_v4i16((__attribute__((address_space(3))) v4i16_t*)p)); }
DI unsigned opq(unsigned x) { asm volatile("" : "+v"(x)); return x; }
DI bf16x8 cat8(s16x4 lo, s16x4 hi) { return __builtin_shufflevector(lo, hi, 0, 1, 2, 3, 4, 5, 6, 7); }
DI float mx3(float a, float b, float c) { float r; asm("v_max3_f32 %0, %1, %2, %3" : "=v"(r) : "v"(a), "v"(b), "v"(c)); return r; }
DI float mx2(float a, float b) { float r; asm("v_max_f32_e32 %0, %1, %2" : "=v"(r) : "v"(a), "v"(b)); return r; }
DI float xmax(float v) {
  auto a = __builtin_amdgcn_permlane32_swap(__float_as_uint(v), __float_as_uint(v), false, false);
  v = mx2(__uint_as_float(a[0]), __uint_as_float(a[1]));
  auto b = __builtin_amdgcn_permlane16_swap(__float_as_uint(v), __float_as_uint(v), false, false);
  return mx2(__uint_as_float(b[0]), __uint_as_float(b[1]));
}
DI float xsum(float v) {
  auto a = __builtin_amdgcn_permlane32_swap(__float_as_uint(v), __float_as_uint(v), false, false);
  v = __uint_as_float(a[0]) + __uint_as_float(a[1]);
  auto b = __builtin_amdgcn_permlane16_swap(__float_as_uint(v), __float_as_uint(v), false, false);
  return __uint_as_float(b[0]) + __uint_as_float(b[1]);
}

template <int N> struct IC { static constexpr int value = N; };
#define AI __attribute__((always_inline))
template <class F, int... Is> DI void sfor_(F&& f, std::integer_sequence<int, Is...>) { (f(IC<Is>{}), ...); }
template <int N, class F> DI void sfor(F&& f) { sfor_(f, std::make_integer_sequence<int, N>{}); }
struct Ptrs { const char* x[2]; const char* k[2]; const char* v[2]; const char* wq[2]; const char* wo[2]; };

__global__ __launch_bounds__(256, 1) void k_attn(Ptrs P, char* __restrict__ ofr) {
  extern __shared__ __attribute__((aligned(16))) char smem[];
  lds_ptr lds = (lds_ptr)smem;
  const int tid = threadIdx.x, l = tid & 63, w = __builtin_amdgcn_readfirstlane(tid >> 6);
  const int i16 = l & 15, g = l >> 4, qq = i16 >> 2, pp = i16 & 3;
  const int xcd = blockIdx.x & 7, idx = blockIdx.x >> 3;
  const int br = xcd >> 2, h = 8 * (xcd & 3) + (idx & 7), b = idx >> 3;
  const char* xg = P.x[br] + (size_t)b * SQ * DM * 2;
  const char* wg = P.wq[br] + (size_t)h * DH * 2;
  const char* kg = P.k[br] + (size_t)(b * NH + h) * SK * DH * 2;
  const char* vg = P.v[br] + (size_t)(b * NH + h) * SK * DH * 2;

  if (blockIdx.x == 0 && tid < 64) ((unsigned*)(ofr + ((size_t)48 << 20)))[tid * 16] = 0u;
  unsigned offK[3], offV[3], offW[3], offX;
#pragma unroll
  for (int u = 0; u < 3; ++u) {
    const int o = 1024 * u + 16 * l, r = o / 384, chp = (o % 384) >> 4, row = 8 * w + r;
    const int aK = (row >> 1) & 7;
    offK[u] = row * 384 + (((chp & ~7) | ((chp ^ aK) & 7)) << 4);
    offV[u] = row * 384 + ((chp ^ (((row >> 1) & 3) << 1)) << 4);
    const int sW = ((row >> 1) & 1) | (((row >> 3) & 1) << 1);
    offW[u] = row * (HD * 2) + ((chp ^ (sW << 1)) << 4);
  }
  { const int row = 8 * w + (l >> 3), a = (row >> 1) & 7; offX = row * (DM * 2) + (((l & 7) ^ a) << 4); }

  const unsigned oK0 = offK[0], oK1 = offK[1], oK2 = offK[2], oV0 = offV[0], oV1 = offV[1], oV2 = offV[2], oW0 = offW[0], oW1 = offW[1], oW2 = offW[2];
  const unsigned lds0 = (unsigned)(uintptr_t)lds;
  auto px = [&](int c, int slot, auto N) AI {
    constexpr int n = N.value;
    glds16(offX, xg + c * 128 + (size_t)n * 32 * DM * 2, lds0 + slot * SLOT + w * 1024 + n * 4096);
  };
  auto pt = [&](const char* s, size_t half, unsigned o0, unsigned o1, unsigned o2, int slot, auto N) AI {
    constexpr int n = N.value, u = n % 3, hf = n / 3;
    glds16(u == 0 ? o0 : u == 1 ? o1 : o2, s + hf * half, lds0 + slot * SLOT + w * 3072 + hf * 12288 + u * 1024);
  };
  auto ptn = [&](const char* s, size_t half, unsigned o0, unsigned o1, unsigned o2, int slot, auto N) AI {
    constexpr int n = N.value, u = n % 3, hf = n / 3;
    glds16nt(u == 0 ? o0 : u == 1 ? o1 : o2, s + hf * half, lds0 + slot * SLOT + w * 3072 + hf * 12288 + u * 1024);
  };
  auto pw_ = [&](int c, int slot, auto N) AI { pt(wg + (size_t)c * 64 * HD * 2, (size_t)32 * HD * 2, oW0, oW1, oW2, slot, N); };
  auto pk = [&](int j, int slot, auto N) AI { ptn(kg + (size_t)j * SLOT, 12288, oK0, oK1, oK2, slot, N); };
  auto pv = [&](int j, int slot, auto N) AI { ptn(vg + (size_t)j * SLOT, 12288, oV0, oV1, oV2, slot, N); };
  auto pkv = [&](auto F, auto N) AI {
    constexpr int f = F.value;
    if constexpr ((f & 1) == 0) pk((f - 20) >> 1, f % NSLOT, N); else pv((f - 20) >> 1, f % NSLOT, N);
  };
  auto fill_x = [&](int c, int slot) AI { sfor<6>([&](auto N) AI { px(c, slot, N); }); };
  auto fill_w = [&](int c, int slot) AI { sfor<6>([&](auto N) AI { pw_(c, slot, N); }); };
  auto issue_kv = [&](auto F) AI { sfor<6>([&](auto N) AI { pkv(F, N); }); };

  const int a8 = i16 >> 1;
  const unsigned kb0 = lds0 + i16 * 384 + ((g ^ a8) << 4);
  const unsigned xb0 = lds0 + (48 * w + i16) * 128 + ((g ^ a8) << 4);
  const int swz = 2 * (g & 1) + (qq >> 1);
  const unsigned vb0 = lds0 + (4 * g + qq) * 384 + (swz << 5) + 8 * pp;
  const unsigned wb0 = lds0 + (8 * g + qq) * 384 + (swz << 5) + 8 * pp;

  {
    asm volatile("" ::: ACLOB);
    fill_x(0, 0); fill_w(0, 1); fill_x(1, 2); fill_w(1, 3);
    int sl = 0;
    { float z = 0.f; PINF(z); sfor<144>([&](auto N) AI { AWR(N.value, z); }); }
    NOPS(1);
    auto p1step = [&](int c, auto Tc) AI {
      constexpr int TAIL = Tc.value;
      WAITV_BAR(12);
      const int sn = sl >= 2 ? sl - 2 : sl + 4;
      unsigned ax[2], aw[4];
      ax[0] = opq(xb0 + sl * SLOT); ax[1] = opq((xb0 ^ 64) + sl * SLOT);
#pragma unroll
      for (int d = 0; d < 4; ++d) aw[d] = opq((wb0 ^ (d << 5)) + (sl + 1) * SLOT);
      sl = sl == 4 ? 0 : sl + 2;
      bf16x8 xf[2][3], wf[3];
      auto rdw = [&](auto F) AI {
        constexpr int f = F.value, s = f / 12, d = f % 12;
        const unsigned p = aw[d & 3] + s * 32 * 384 + (d >> 2) * 128;
        wf[f % 3] = cat8(ldstr(p), ldstr(p + 4 * 384));
      };
#pragma unroll
      for (int q = 0; q < 3; ++q) xf[0][q] = ldsb128(ax[0] + q * 2048);
      rdw(IC<0>{}); rdw(IC<1>{});
      SBAR();
      sfor<24>([&](auto F) AI {
        constexpr int f = F.value, s = f / 12, d = f % 12;
        if constexpr (f + 2 < 24) rdw(IC<f + 2>{});
        if constexpr (f >= 2 && f < 5) xf[1][f - 2] = ldsb128(ax[1] + (f - 2) * 2048);
        MFMA_O(d, 0, wf[f % 3], xf[s][0]); MFMA_O(d, 1, wf[f % 3], xf[s][1]); MFMA_O(d, 2, wf[f % 3], xf[s][2]);
        if constexpr ((f & 1) == 0) {
          constexpr int n = f >> 1;
          if constexpr (n < 6) { if constexpr (!TAIL) px(c + 2, sn, IC<n>{}); else pk(c - 8, sn, IC<n>{}); }
          else { if constexpr (!TAIL) pw_(c + 2, sn + 1, IC<n - 6>{}); else pv(c - 8, sn + 1, IC<n - 6>{}); }
        }
        SBAR();
      });
    };
#pragma unroll 1
    for (int c = 0; c < 8; ++c) p1step(c, IC<0>{});
    p1step(8, IC<1>{}); p1step(9, IC<1>{});
    NOPS(7);
    sfor<18>([&](auto I) AI {
      constexpr int s = I.value / 3, q = I.value % 3;
      f32x4 X, Y;
      sfor<4>([&](auto R) AI { ARD(X[R.value], AO(2 * s, q) + R.value); ARD(Y[R.value], AO(2 * s + 1, q) + R.value); });
      X *= QSCALE; Y *= QSCALE;
      unsigned x0 = cvtpk(X[0], X[1]), x1 = cvtpk(X[2], X[3]), y0 = cvtpk(Y[0], Y[1]), y1 = cvtpk(Y[2], Y[3]);
      auto r0 = __builtin_amdgcn_permlane32_swap(x0, y0, false, false);
      auto r1 = __builtin_amdgcn_permlane32_swap(x1, y1, false, false);
      auto t0 = __builtin_amdgcn_permlane16_swap(r0[0], r0[1], false, false);
      auto t1 = __builtin_amdgcn_permlane16_swap(r1[0], r1[1], false, false);
      AWR(AQ(s, q) + 0, t0[0]); AWR(AQ(s, q) + 1, t1[0]); AWR(AQ(s, q) + 2, t0[1]); AWR(AQ(s, q) + 3, t1[1]);
    });
    NOPS(1);
  }

  f32x4 sc[2][4][3];
  f32x4 negm[3];
  unsigned pw[2][3][4];
  bf16x8 ones = {0x3F80, 0x3F80, 0x3F80, 0x3F80, 0x3F80, 0x3F80, 0x3F80, 0x3F80};
  asm volatile("" : "+v"(ones));

#define EXP1(X, kb, q, r) do { sc[X][kb][q][r] = __builtin_amdgcn_exp2f(sc[X][kb][q][r]); PINF(sc[X][kb][q][r]); } while (0)
#define CVT1(X, c) do { constexpr int kb_ = (c) / 6, q_ = ((c) % 6) >> 1, hf_ = (c) & 1; \
    pw[kb_ >> 1][q_][2 * (kb_ & 1) + hf_] = cvtpk(sc[X][kb_][q_][2 * hf_], sc[X][kb_][q_][2 * hf_ + 1]); PINU(pw[kb_ >> 1][q_][2 * (kb_ & 1) + hf_]); } while (0)

  WAITV_BAR(18);
  issue_kv(IC<24>{}); issue_kv(IC<25>{});
  {
    unsigned ak[2];
    ak[0] = opq(kb0 + 2 * SLOT); ak[1] = opq((kb0 ^ 64) + 2 * SLOT);
#pragma unroll
    for (int q = 0; q < 3; ++q) negm[q] = f32x4{0.f, 0.f, 0.f, 0.f};
    asm volatile("s_nop 1" : "+v"(negm[0]), "+v"(negm[1]), "+v"(negm[2]));
    bf16x8 kf[3];
    auto rdk = [&](auto F) AI { constexpr int f = F.value, s = f >> 2, kb = f & 3; kf[f % 3] = ldsb128(ak[s & 1] + kb * 6144 + (s >> 1) * 128); };
    rdk(IC<0>{}); rdk(IC<1>{});
    SBAR();
    sfor<24>([&](auto F) AI {
      constexpr int f = F.value, s = f >> 2, kb = f & 3;
      if constexpr (f + 2 < 24) rdk(IC<f + 2>{});
      sfor<3>([&](auto Q) AI {
        constexpr int q = Q.value;
        if constexpr (s == 0) { MFMA_SC(sc[0][kb][q], kf[f % 3], s, q, negm[q]); } else { MFMA_S(sc[0][kb][q], kf[f % 3], s, q); }
      });
      SBAR();
    });
    asm volatile("s_nop 7" : "+v"(sc[0][0][0]), "+v"(sc[0][0][1]), "+v"(sc[0][0][2]), "+v"(sc[0][1][0]), "+v"(sc[0][1][1]), "+v"(sc[0][1][2]),
                             "+v"(sc[0][2][0]), "+v"(sc[0][2][1]), "+v"(sc[0][2][2]), "+v"(sc[0][3][0]), "+v"(sc[0][3][1]), "+v"(sc[0][3][2]));
#pragma unroll
    for (int q = 0; q < 3; ++q) {
      float m0 = mx3(sc[0][0][q][0], sc[0][0][q][1], sc[0][0][q][2]);
      m0 = mx2(m0, sc[0][0][q][3]);
#pragma unroll
      for (int kb = 1; kb < 4; ++kb) { m0 = mx3(m0, sc[0][kb][q][0], sc[0][kb][q][1]); m0 = mx3(m0, sc[0][kb][q][2], sc[0][kb][q][3]); }
      m0 = xmax(m0);
      negm[q] = f32x4{-m0, -m0, -m0, -m0};
#pragma unroll
      for (int kb = 0; kb < 4; ++kb) sc[0][kb][q] -= m0;
#pragma unroll
      for (int r = 0; r < 4; ++r) sc[0][0][q][r] = __builtin_amdgcn_exp2f(sc[0][0][q][r]);
    }
  }
  sfor<8>([&](auto Jc) AI {
    constexpr int j = Jc.value;
    constexpr int X = j & 1, Y = X ^ 1;
    if constexpr (j == 0) { WAITV_BAR(18); } else if constexpr (j < 6) { WAITV_BAR(12); } else if constexpr (j == 6) { WAITV_BAR(6); } else { WAITV_BAR(0); }
    if constexpr (j < 7) {
      unsigned ak[2];
      ak[0] = opq(kb0 + ((22 + 2 * j) % NSLOT) * SLOT); ak[1] = opq((kb0 ^ 64) + ((22 + 2 * j) % NSLOT) * SLOT);
      pin3n_(negm[0], negm[1], negm[2]);
      bf16x8 kf[3];
      auto rdk = [&](auto F) AI { constexpr int f = F.value, s = f >> 2, kb = f & 3; kf[f % 3] = ldsb128(ak[s & 1] + kb * 6144 + (s >> 1) * 128); };
      rdk(IC<0>{}); rdk(IC<1>{});
      SBAR();
      sfor<24>([&](auto F) AI {
        constexpr int f = F.value, s = f >> 2, kb = f & 3;
        if constexpr (f + 2 < 24) rdk(IC<f + 2>{});
        sfor<3>([&](auto Q) AI {
          constexpr int q = Q.value;
          if constexpr (s == 0) { MFMA_SC(sc[Y][kb][q], kf[f % 3], s, q, negm[q]); } else { MFMA_S(sc[Y][kb][q], kf[f % 3], s, q); }
          if constexpr (q == 0) { CVT1(X, f); }
          else {
            constexpr int e = 2 * f + q - 1;
            if constexpr (e < 36) { EXP1(X, 1 + e / 12, (e % 12) >> 2, e & 3); }
          }
          SBAR();
        });
        if constexpr ((f & 3) == 1 && j >= 1 && 25 + 2 * j <= 35) pkv(IC<25 + 2 * j>{}, IC<(f >> 2)>{});
        SBAR();
      });
    } else {
      sfor<36>([&](auto E) AI { constexpr int e = E.value; EXP1(X, 1 + e / 12, (e % 12) >> 2, e & 3); });
      sfor<24>([&](auto C) AI { CVT1(X, C.value); });
    }
    {
      bf16x8 pf[2][3];
#pragma unroll
      for (int t = 0; t < 2; ++t)
#pragma unroll
        for (int q = 0; q < 3; ++q) { u32x4 f = {pw[t][q][0], pw[t][q][1], pw[t][q][2], pw[t][q][3]}; pf[t][q] = __builtin_bit_cast(bf16x8, f); }
      pin3n_(pf[0][0], pf[0][1], pf[0][2]); pin3n_(pf[1][0], pf[1][1], pf[1][2]);
      unsigned av[4];
#pragma unroll
      for (int d = 0; d < 4; ++d) av[d] = opq((vb0 ^ (d << 5)) + ((21 + 2 * j) % NSLOT) * SLOT);
      bf16x8 vf[3];
      auto rdv = [&](auto F) AI {
        constexpr int f = F.value, t = f / 12, d = f % 12;
        const unsigned p = av[d & 3] + t * 32 * 384 + (d >> 2) * 128;
        vf[f % 3] = cat8(ldstr(p), ldstr(p + 16 * 384));
      };
      float mx[3], gr[3], fct[3];
      bool pend = false;
      rdv(IC<0>{}); rdv(IC<1>{});
      SBAR();
      sfor<26>([&](auto Gc) AI {
        constexpr int G = Gc.value, t = G / 13, gi = G % 13;
        if constexpr (gi == 0) {
          sfor<3>([&](auto Q) AI { constexpr int q = Q.value;
            if constexpr (j == 0 && t == 0) { MFMA_O0(12, q, ones, pf[t][q]); } else { MFMA_O(12, q, ones, pf[t][q]); } });
        } else {
          constexpr int d = gi - 1, f = 12 * t + d;
          if constexpr (f + 2 < 24) rdv(IC<f + 2>{});
          sfor<3>([&](auto Q) AI { constexpr int q = Q.value;
            if constexpr (j == 0 && t == 0) { MFMA_O0(d, q, vf[f % 3], pf[t][q]); } else { MFMA_O(d, q, vf[f % 3], pf[t][q]); } });
        }
        if constexpr (j < 7) {
          if constexpr (G >= 2 && G < 8) {
            constexpr int q = (G - 2) >> 1, kb = 2 * ((G - 2) & 1);
            if constexpr (kb == 0) {
              mx[q] = mx3(sc[Y][0][q][0], sc[Y][0][q][1], sc[Y][0][q][2]); mx[q] = mx3(mx[q], sc[Y][0][q][3], sc[Y][1][q][0]);
              mx[q] = mx3(mx[q], sc[Y][1][q][1], sc[Y][1][q][2]); mx[q] = mx2(mx[q], sc[Y][1][q][3]);
            } else {
              mx[q] = mx3(mx[q], sc[Y][2][q][0], sc[Y][2][q][1]); mx[q] = mx3(mx[q], sc[Y][2][q][2], sc[Y][2][q][3]);
              mx[q] = mx3(mx[q], sc[Y][3][q][0], sc[Y][3][q][1]); mx[q] = mx3(mx[q], sc[Y][3][q][2], sc[Y][3][q][3]);
            }
            PINF(mx[q]);
          }
          if constexpr (G >= 8 && G < 11) { gr[G - 8] = xmax(mx[G - 8]); PINF(gr[G - 8]); }
          if constexpr (G == 11) {
            pend = __any((gr[0] > THR) | (gr[1] > THR) | (gr[2] > THR));
            if (__builtin_expect(pend, 0)) {
#pragma unroll
              for (int q = 0; q < 3; ++q) {
                const unsigned db = (__float_as_uint(fmaxf(gr[q], 0.f)) + 0xFFFFu) & 0xFFFF0000u;
                fct[q] = __builtin_amdgcn_exp2f(-__uint_as_float(db));
                u32x4 bw = {g == 0 ? ((db >> 16) | 0x8000u) : 0u, 0u, 0u, 0u};
                bf16x8 bfr = __builtin_bit_cast(bf16x8, bw);
                asm volatile("s_nop 1" : "+v"(bfr));
                mfma_ip_(negm[q], ones, bfr);
                mfma_ip_(sc[Y][0][q], ones, bfr); mfma_ip_(sc[Y][1][q], ones, bfr); mfma_ip_(sc[Y][2][q], ones, bfr); mfma_ip_(sc[Y][3][q], ones, bfr);
              }
              NOPS(7);
            }
          }
          if constexpr (G >= 12 && G < 24) { constexpr int e = G - 12; EXP1(Y, 0, e >> 2, e & 3); }
        }
        if constexpr ((G & 3) == 1 && G < 24 && 26 + 2 * j <= 35) pkv(IC<26 + 2 * j>{}, IC<(G >> 2)>{});
        SBAR();
      });
      if constexpr (j < 7) {
        if (__builtin_expect(pend, 0)) {
          NOPS(7);
          sfor<156>([&](auto N) AI { ASCALE(N.value, fct[(N.value >> 2) % 3]); });
          NOPS(1);
        }
      }
    }
  });
  NOPS(7);
  sfor<3>([&](auto Q) AI {
    constexpr int q = Q.value;
    float lq; ARD(lq, AO(12, q));
    const float inv = 1.0f / lq;
    char* dst = ofr + (((size_t)(br * NB + b) * 192 + h * 6) * 12 + 3 * w + q) * 1024 + l * 16;
    sfor<6>([&](auto T) AI {
      constexpr int t = T.value;
      f32x4 X, Y;
      sfor<4>([&](auto R) AI { ARD(X[R.value], AO(2 * t, q) + R.value); ARD(Y[R.value], AO(2 * t + 1, q) + R.value); });
      X *= inv; Y *= inv;
      u32x4 f = {cvtpk(X[0], X[1]), cvtpk(X[2], X[3]), cvtpk(Y[0], Y[1]), cvtpk(Y[2], Y[3])};
      *(u32x4*)(dst + t * 12288) = f;
    });
  });
}

constexpr int ST2 = 128 * 320;
constexpr int LDS2 = 3 * ST2;
constexpr size_t WS_SLAB = (size_t)64 << 20;
constexpr size_t WS_CNT = (size_t)48 << 20;

DI void gload16(bf16x8& d, unsigned voff, const char* sbase) { asm volatile("global_load_dwordx4 %0, %1, %2" : "=v"(d) : "v"(voff), "s"(sbase) : "memory"); }
template <int N> DI void wait3(bf16x8& a, bf16x8& b, bf16x8& c) { asm volatile("s_waitcnt vmcnt(%3)" : "+v"(a), "+v"(b), "+v"(c) : "i"(N) : "memory"); }

__global__ __launch_bounds__(512, 2) void k_oproj(Ptrs P, char* __restrict__ ws, char* __restrict__ out) {
  extern __shared__ __attribute__((aligned(16))) char smem[];
  lds_ptr lds = (lds_ptr)smem;
  const int tid = threadIdx.x, l = tid & 63, w = __builtin_amdgcn_readfirstlane(tid >> 6);
  const int i16 = l & 15, g = l >> 4, qq = i16 >> 2, pp = i16 & 3;
  const int xcd = blockIdx.x & 7, idx = blockIdx.x >> 3;
  const int br = xcd >> 2, ks = xcd & 3, b = idx >> 2, nq = idx & 3;
  const int kh = w >> 2, qg = w & 3;
  const unsigned lds0 = (unsigned)(uintptr_t)lds;
  const char* wsrc = P.wo[br] + (size_t)ks * 1536 * DM * 2 + nq * 320;
  const char* osrc = ws + (((size_t)(br * NB + b) * 192 + 48 * ks + kh) * 12 + 3 * qg) * 1024;
  const unsigned l16 = l * 16;
  unsigned offW[5];
#pragma unroll
  for (int u = 0; u < 5; ++u) {
    const int o = 1024 * u + 16 * l, r = o / 320, chp = (o % 320) >> 4;
    offW[u] = (16 * w + r) * (DM * 2) + ((chp ^ (((r >> 2) & 1) << 1)) << 4);
  }
  const unsigned o0 = offW[0], o1 = offW[1], o2 = offW[2], o3 = offW[3], o4 = offW[4];
  auto issueW = [&](int s, int stg) AI {
    const unsigned d = lds0 + stg * ST2 + w * 5120;
    const char* src = wsrc + (size_t)s * 128 * DM * 2;
    glds16(o0, src, d); glds16(o1, src, d + 1024); glds16(o2, src, d + 2048); glds16(o3, src, d + 3072); glds16(o4, src, d + 4096);
  };
  bf16x8 of[4][3];
  auto loadO = [&](int u, auto Bc) AI {
    constexpr int B_ = Bc.value;
    const char* src = osrc + (size_t)u * 24576;
    gload16(of[B_][0], l16, src); gload16(of[B_][1], l16, src + 1024); gload16(of[B_][2], l16, src + 2048);
  };
  f32x4 acc[10][3];
#pragma unroll
  for (int n = 0; n < 10; ++n)
#pragma unroll
    for (int q = 0; q < 3; ++q) acc[n][q] = f32x4{0.f, 0.f, 0.f, 0.f};
  const unsigned wb0 = lds0 + (32 * kh + 4 * g + qq) * 320 + 8 * pp + ((g & 1) << 5);
  issueW(0, 0); loadO(0, IC<0>{}); loadO(1, IC<1>{}); loadO(2, IC<2>{});
  issueW(1, 1);
  int stg = 0;
  auto kstep = [&](unsigned ae, unsigned ao, auto Vc, auto Bc) AI {
    constexpr int v = Vc.value, B_ = Bc.value;
    bf16x8 wf[3];
    auto rdw = [&](auto N) AI { constexpr int n = N.value; const unsigned p = ((n & 1) ? ao : ae) + v * 64 * 320 + (n >> 1) * 64; wf[n % 3] = cat8(ldstr(p), ldstr(p + 16 * 320)); };
    rdw(IC<0>{}); rdw(IC<1>{});
    sfor<10>([&](auto N) AI {
      constexpr int n = N.value;
      if constexpr (n + 2 < 10) rdw(IC<n + 2>{});
#pragma unroll
      for (int q = 0; q < 3; ++q) acc[n][q] = MFMA16(wf[n % 3], of[B_][q], acc[n][q]);
    });
  };
  auto stage = [&](int s, auto Pc, auto Mc) AI {
    constexpr int PAR = Pc.value, MODE = Mc.value, B0 = 2 * PAR, B1 = 2 * PAR + 1;
    if constexpr (MODE == 2) { asm volatile("s_waitcnt vmcnt(6) lgkmcnt(0)\n\ts_barrier" ::: "memory"); }
    else { asm volatile("s_waitcnt vmcnt(11) lgkmcnt(0)\n\ts_barrier" ::: "memory"); }
    const unsigned ae = opq(wb0 + stg * ST2), ao = opq((wb0 ^ 32) + stg * ST2);
    if constexpr (MODE == 0) issueW(s + 2, stg == 0 ? 2 : stg - 1);
    if constexpr (MODE <= 1) loadO(2 * s + 3, IC<(B0 + 3) & 3>{});
    wait3<MODE == 0 ? 19 : MODE == 1 ? 14 : 3>(of[B0][0], of[B0][1], of[B0][2]);
    kstep(ae, ao, IC<0>{}, IC<B0>{});
    if constexpr (MODE == 0) loadO(2 * s + 4, IC<B0>{});
    wait3<MODE == 0 ? 14 : MODE == 1 ? 6 : 0>(of[B1][0], of[B1][1], of[B1][2]);
    kstep(ae, ao, IC<1>{}, IC<B1>{});
    stg = stg == 2 ? 0 : stg + 1;
  };
#pragma unroll 1
  for (int s = 0; s < 10; s += 2) { stage(s, IC<0>{}, IC<0>{}); stage(s + 1, IC<1>{}, IC<0>{}); }
  stage(10, IC<0>{}, IC<1>{}); stage(11, IC<1>{}, IC<2>{});
  asm volatile("s_waitcnt vmcnt(0) lgkmcnt(0)\n\ts_barrier" ::: "memory");
  {
    const unsigned xo = lds0 + ((kh ^ 1) * 4 + qg) * 15360 + l16, xi = lds0 + (kh * 4 + qg) * 15360 + l16;
#pragma unroll
    for (int n = 0; n < 5; ++n)
#pragma unroll
      for (int q = 0; q < 3; ++q) *(__attribute__((address_space(3))) f32x4*)(xo + (n * 3 + q) * 1024) = kh ? acc[n][q] : acc[5 + n][q];
    __syncthreads();
#pragma unroll
    for (int n = 0; n < 5; ++n)
#pragma unroll
      for (int q = 0; q < 3; ++q) {
        const f32x4 o = *(const __attribute__((address_space(3))) f32x4*)(xi + (n * 3 + q) * 1024);
        acc[n][q] = (kh ? acc[5 + n][q] : acc[n][q]) + o;
      }
  }
  const int tile = (br * NB + b) * 4 + nq;
  char* slab = ws + WS_SLAB + (size_t)tile * 4 * 8 * 15360;
  {
    char* mine = slab + (size_t)(ks * 8 + w) * 15360 + l16;
#pragma unroll
    for (int i = 0; i < 15; ++i)
      if ((i & 3) != ks) asm volatile("global_store_dwordx4 %0, %1, off sc0 sc1\n\ts_nop 1" :: "v"(mine + i * 1024), "v"(acc[i / 3][i % 3]) : "memory");
    asm volatile("s_waitcnt vmcnt(0)" ::: "memory");
    __syncthreads();
    unsigned* cnt = (unsigned*)(ws + WS_CNT) + tile * 16;
    if (tid == 0) {
      __hip_atomic_fetch_add(cnt, 1u, __ATOMIC_RELAXED, __HIP_MEMORY_SCOPE_AGENT);
      unsigned spins = 0;
      while (__hip_atomic_load(cnt, __ATOMIC_RELAXED, __HIP_MEMORY_SCOPE_AGENT) < 4u && ++spins < (1u << 24)) __builtin_amdgcn_s_sleep(1);
    }
    __syncthreads();
  }
  {
    const char* theirs = slab + (size_t)w * 15360 + l16;
#pragma unroll
    for (int i = 0; i < 15; ++i) {
      if ((i & 3) == ks) {
        f32x4 p[3];
#pragma unroll
        for (int r = 0; r < 3; ++r) {
          const int src = (ks + 1 + r) & 3;
          asm volatile("global_load_dwordx4 %0, %1, off sc0 sc1" : "=v"(p[r]) : "v"(theirs + (size_t)src * 8 * 15360 + i * 1024) : "memory");
        }
        asm volatile("s_waitcnt vmcnt(0)" : "+v"(p[0]), "+v"(p[1]), "+v"(p[2]) :: "memory");
        const f32x4 o = (acc[i / 3][i % 3] + p[0]) + (p[1] + p[2]);
        u32x2 f = {cvtpk(o[0], o[1]), cvtpk(o[2], o[3])};
        char* dst = out + ((size_t)(b * SQ + (3 * qg + i % 3) * 16 + i16) * (2 * DM) + br * DM + nq * 160 + (5 * kh + i / 3) * 16 + 4 * g) * 2;
        *(u32x2*)dst = f;
      }
    }
  }
}

extern "C" void kernel_launch(void* const* d_in, const int* in_sizes, int n_in, void* d_out, int out_size, void* d_ws, size_t ws_size,
                              hipStream_t stream) {
  Ptrs P;
  for (int r = 0; r < 2; ++r) {
    P.x[r] = (const char*)d_in[5 * r + 0];
    P.k[r] = (const char*)d_in[5 * r + 1];
    P.v[r] = (const char*)d_in[5 * r + 2];
    P.wq[r] = (const char*)d_in[5 * r + 3];
    P.wo[r] = (const char*)d_in[5 * r + 4];
  }
  (void)hipFuncSetAttribute((const void*)k_attn, hipFuncAttributeMaxDynamicSharedMemorySize, LDS1);
  (void)hipFuncSetAttribute((const void*)k_oproj, hipFuncAttributeMaxDynamicSharedMemorySize, LDS2);
  hipLaunchKernelGGL(k_attn, dim3(512), dim3(256), LDS1, stream, P, (char*)d_ws);
  hipLaunchKernelGGL(k_oproj, dim3(256), dim3(512), LDS2, stream, P, (char*)d_ws, (char*)d_out);
}
```

```cpp
#include <hip/hip_runtime.h>
#include <hip/hip_bf16.h>
#include <stdint.h>
#include <utility>

using bf16x8 = __attribute__((ext_vector_type(8))) short;
using s16x4 = __attribute__((ext_vector_type(4))) short;
using f32x4 = __attribute__((ext_vector_type(4))) float;
using u32x4 = __attribute__((ext_vector_type(4))) unsigned;
using u32x2 = __attribute__((ext_vector_type(2))) unsigned;
typedef short v4i16_t __attribute__((ext_vector_type(4)));
typedef __attribute__((address_space(3))) char* lds_ptr;
typedef __attribute__((address_space(3))) const char* lds_cptr;

constexpr int NB = 8, SQ = 192, DM = 640, NH = 32, SK = 512, DH = 192;
constexpr int HD = NH * DH;
constexpr int SLOT = 24576;
constexpr int NSLOT = 6;
constexpr int LDS1 = SLOT * NSLOT;
constexpr float QSCALE = 0.07216878364870322f * 1.4426950408889634f;
constexpr float THR = 8.0f;

#define DI __device__ __forceinline__
#define MFMA16(a, b, c) __builtin_amdgcn_mfma_f32_16x16x32_bf16((a), (b), (c), 0, 0, 0)
#define ACLOB "a0","a1","a2","a3","a4","a5","a6","a7","a8","a9","a10","a11","a12","a13","a14","a15","a16","a17","a18","a19","a20","a21","a22","a23","a24","a25","a26","a27","a28","a29","a30","a31","a32","a33","a34","a35","a36","a37","a38","a39","a40","a41","a42","a43","a44","a45","a46","a47","a48","a49","a50","a51","a52","a53","a54","a55","a56","a57","a58","a59","a60","a61","a62","a63","a64","a65","a66","a67","a68","a69","a70","a71","a72","a73","a74","a75","a76","a77","a78","a79","a80","a81","a82","a83","a84","a85","a86","a87","a88","a89","a90","a91","a92","a93","a94","a95","a96","a97","a98","a99","a100","a101","a102","a103","a104","a105","a106","a107","a108","a109","a110","a111","a112","a113","a114","a115","a116","a117","a118","a119","a120","a121","a122","a123","a124","a125","a126","a127","a128","a129","a130","a131","a132","a133","a134","a135","a136","a137","a138","a139","a140","a141","a142","a143","a144","a145","a146","a147","a148","a149","a150","a151","a152","a153","a154","a155","a156","a157","a158","a159","a160","a161","a162","a163","a164","a165","a166","a167","a168","a169","a170","a171","a172","a173","a174","a175","a176","a177","a178","a179","a180","a181","a182","a183","a184","a185","a186","a187","a188","a189","a190","a191","a192","a193","a194","a195","a196","a197","a198","a199","a200","a201","a202","a203","a204","a205","a206","a207","a208","a209","a210","a211","a212","a213","a214","a215","a216","a217","a218","a219","a220","a221","a222","a223","a224","a225","a226","a227"
#define AO(d, q) (4 * (3 * (d) + (q)))
#define AQ(s, q) (156 + 4 * (3 * (s) + (q)))
template <int N> DI void mfma_o_(const bf16x8& a, const bf16x8& b) { asm volatile("v_mfma_f32_16x16x32_bf16 a[%c2:%c3], %0, %1, a[%c2:%c3]" :: "v"(a), "v"(b), "i"(N), "i"(N + 3) : ACLOB); }
template <int N> DI void mfma_o0_(const bf16x8& a, const bf16x8& b) { asm volatile("v_mfma_f32_16x16x32_bf16 a[%c2:%c3], %0, %1, 0" :: "v"(a), "v"(b), "i"(N), "i"(N + 3) : ACLOB); }
template <int N> DI void mfma_s_(f32x4& acc, const bf16x8& a) { asm volatile("v_mfma_f32_16x16x32_bf16 %0, %1, a[%c2:%c3], %0" : "+v"(acc) : "v"(a), "i"(N), "i"(N + 3) : ACLOB); }
template <int N> DI void mfma_sc_(f32x4& acc, const bf16x8& a, const f32x4& c) { asm volatile("v_mfma_f32_16x16x32_bf16 %0, %1, a[%c2:%c3], %4" : "=&v"(acc) : "v"(a), "i"(N), "i"(N + 3), "v"(c) : ACLOB); }
template <int N> DI void mfma_s0_(f32x4& acc, const bf16x8& a) { asm volatile("v_mfma_f32_16x16x32_bf16 %0, %1, a[%c2:%c3], 0" : "=&v"(acc) : "v"(a), "i"(N), "i"(N + 3) : ACLOB); }
template <int N> DI float ard_() { float r; asm volatile("v_accvgpr_read_b32 %0, a[%c1]" : "=v"(r) : "i"(N)); return r; }
template <int N, class T> DI void awr_(T src) { asm volatile("v_accvgpr_write_b32 a[%c1], %0" :: "v"(src), "i"(N)); }
template <int N> DI void ascale_(float f) { float t; asm volatile("v_accvgpr_read_b32 %0, a[%c1]\n\tv_mul_f32 %0, %2, %0\n\tv_accvgpr_write_b32 a[%c1], %0" : "=&v"(t) : "i"(N), "v"(f)); }
DI float subip_(float x, float d) { asm volatile("v_sub_f32 %0, %0, %1" : "+v"(x) : "v"(d)); return x; }
DI void mfma_ip_(f32x4& x, const bf16x8& a, const bf16x8& b) { asm volatile("v_mfma_f32_16x16x32_bf16 %0, %1, %2, %0" : "+v"(x) : "v"(a), "v"(b) : ACLOB); }
template <class T> DI T pin_(T x) { asm volatile("" : "+v"(x)); return x; }
template <class T> DI void pin3n_(T& a, T& b, T& c) { asm volatile("s_nop 1" : "+v"(a), "+v"(b), "+v"(c)); }
#define MFMA_O(d, q, a, b) mfma_o_<AO(d, q)>(a, b)
#define MFMA_O0(d, q, a, b) mfma_o0_<AO(d, q)>(a, b)
#define MFMA_S(acc, a, s, q) mfma_s_<AQ(s, q)>(acc, a)
#define MFMA_SC(acc, a, s, q, c) mfma_sc_<AQ(s, q)>(acc, a, c)
#define ARD(dst, n) dst = ard_<n>()
#define AWR(n, src) awr_<n>(src)
#define ASCALE(n, f) ascale_<n>(f)
#define SUBIP(x, d) x = subip_(x, d)
#define NOPS(n) asm volatile("s_nop " #n ::: "memory")
#define SBAR() __builtin_amdgcn_sched_barrier(0)
#define PINF(x) x = pin_(x)
#define PINU(x) x = pin_(x)
#define WAITV_BAR(N) asm volatile("s_waitcnt vmcnt(" #N ") lgkmcnt(0)\n\ts_barrier" ::: "memory")

DI unsigned cvtpk(float lo, float hi) {
  typedef float f32x2 __attribute__((ext_vector_type(2)));
  typedef __bf16 bf16x2 __attribute__((ext_vector_type(2)));
  f32x2 v = {lo, hi}; bf16x2 b = __builtin_convertvector(v, bf16x2); return __builtin_bit_cast(unsigned, b);
}
DI void glds16(unsigned voff, const char* sbase, unsigned ldsaddr) {
  asm volatile("s_mov_b32 m0, %2\n\ts_nop 0\n\tglobal_load_lds_dwordx4 %0, %1" :: "v"(voff), "s"(sbase), "s"(ldsaddr) : "memory");
}
DI void glds16nt(unsigned voff, const char* sbase, unsigned ldsaddr) {
  asm volatile("s_mov_b32 m0, %2\n\ts_nop 0\n\tglobal_load_lds_dwordx4 %0, %1 nt" :: "v"(voff), "s"(sbase), "s"(ldsaddr) : "memory");
}
DI bf16x8 ldsb128(unsigned p) { return *(const __attribute__((address_space(3))) bf16x8*)p; }
DI s16x4 ldstr(unsigned p) { return __builtin_bit_cast(s16x4, __builtin_amdgcn_ds_read_tr16_b64_v4i16((__attribute__((address_space(3))) v4i16_t*)p)); }
template <int X> DI unsigned adr(unsigned base, unsigned off) {
  unsigned r;
  if constexpr (X == 0) asm volatile("v_add_u32 %0, %2, %1" : "=v"(r) : "v"(base), "s"(off));
  else asm volatile("v_xor_b32 %0, %3, %1\n\tv_add_u32 %0, %2, %0" : "=&v"(r) : "v"(base), "s"(off), "i"(X));
  return r;
}
DI unsigned opq(unsigned x) { asm volatile("" : "+v"(x)); return x; }
DI bf16x8 cat8(s16x4 lo, s16x4 hi) { return __builtin_shufflevector(lo, hi, 0, 1, 2, 3, 4, 5, 6, 7); }
DI float mx3(float a, float b, float c) { float r; asm("v_max3_f32 %0, %1, %2, %3" : "=v"(r) : "v"(a), "v"(b), "v"(c)); return r; }
DI float mx2(float a, float b) { float r; asm("v_max_f32_e32 %0, %1, %2" : "=v"(r) : "v"(a), "v"(b)); return r; }
DI float xmax(float v) {
  auto a = __builtin_amdgcn_permlane32_swap(__float_as_uint(v), __float_as_uint(v), false, false);
  v = mx2(__uint_as_float(a[0]), __uint_as_float(a[1]));
  auto b = __builtin_amdgcn_permlane16_swap(__float_as_uint(v), __float_as_uint(v), false, false);
  return mx2(__uint_as_float(b[0]), __uint_as_float(b[1]));
}
DI float xsum(float v) {
  auto a = __builtin_amdgcn_permlane32_swap(__float_as_uint(v), __float_as_uint(v), false, false);
  v = __uint_as_float(a[0]) + __uint_as_float(a[1]);
  auto b = __builtin_amdgcn_permlane16_swap(__float_as_uint(v), __float_as_uint(v), false, false);
  return __uint_as_float(b[0]) + __uint_as_float(b[1]);
}

template <int N> struct IC { static constexpr int value = N; };
#define AI __attribute__((always_inline))
template <class F, int... Is> DI void sfor_(F&& f, std::integer_sequence<int, Is...>) { (f(IC<Is>{}), ...); }
template <int N, class F> DI void sfor(F&& f) { sfor_(f, std::make_integer_sequence<int, N>{}); }
struct Ptrs { const char* x[2]; const char* k[2]; const char* v[2]; const char* wq[2]; const char* wo[2]; };

__global__ __launch_bounds__(256, 1) void k_attn(Ptrs P, char* __restrict__ ofr) {
  extern __shared__ __attribute__((aligned(16))) char smem[];
  lds_ptr lds = (lds_ptr)smem;
  const int tid = threadIdx.x, l = tid & 63, w = __builtin_amdgcn_readfirstlane(tid >> 6);
  const int i16 = l & 15, g = l >> 4, qq = i16 >> 2, pp = i16 & 3;
  const int xcd = blockIdx.x & 7, idx = blockIdx.x >> 3;
  const int br = xcd >> 2, h = 8 * (xcd & 3) + (idx & 7), b0 = idx >> 3;
  const char* xg = P.x[br] + (size_t)b0 * SQ * DM * 2;
  const char* wg = P.wq[br] + (size_t)h * DH * 2;
  const char* kg = P.k[br] + (size_t)(b0 * NH + h) * SK * DH * 2;
  const char* vg = P.v[br] + (size_t)(b0 * NH + h) * SK * DH * 2;
  char* og = ofr + (((size_t)(br * NB + b0) * 192 + h * 6) * 12 + 3 * w) * 1024 + l * 16;

  if (blockIdx.x == 0 && tid < 64) ((unsigned*)(ofr + ((size_t)48 << 20)))[tid * 16] = 0u;
  unsigned offK[3], offV[3], offW[3], offX;
#pragma unroll
  for (int u = 0; u < 3; ++u) {
    const int o = 1024 * u + 16 * l, r = o / 384, chp = (o % 384) >> 4, row = 8 * w + r;
    const int aK = (row >> 1) & 7;
    offK[u] = row * 384 + (((chp & ~7) | ((chp ^ aK) & 7)) << 4);
    offV[u] = row * 384 + ((chp ^ (((row >> 1) & 3) << 1)) << 4);
    const int sW = ((row >> 1) & 1) | (((row >> 3) & 1) << 1);
    offW[u] = row * (HD * 2) + ((chp ^ (sW << 1)) << 4);
  }
  { const int row = 8 * w + (l >> 3), a = (row >> 1) & 7; offX = row * (DM * 2) + (((l & 7) ^ a) << 4); }

  const unsigned oK0 = offK[0], oK1 = offK[1], oK2 = offK[2], oV0 = offV[0], oV1 = offV[1], oV2 = offV[2], oW0 = offW[0], oW1 = offW[1], oW2 = offW[2];
  const unsigned lds0 = (unsigned)(uintptr_t)lds;
  auto px = [&](int c, int slot, auto N) AI {
    constexpr int n = N.value;
    glds16(offX, xg + c * 128 + (size_t)n * 32 * DM * 2, lds0 + slot * SLOT + w * 1024 + n * 4096);
  };
  auto pw_ = [&](int c, int slot, auto N) AI {
    constexpr int n = N.value, u = n % 3, hf = n / 3;
    glds16(u == 0 ? oW0 : u == 1 ? oW1 : oW2, wg + (size_t)(c * 64 + hf * 32) * HD * 2, lds0 + slot * SLOT + w * 3072 + hf * 12288 + u * 1024);
  };
  auto pk = [&](int j, int slot, auto N) AI {
    constexpr int n = N.value, u = n % 3, hf = n / 3;
    glds16nt(u == 0 ? oK0 : u == 1 ? oK1 : oK2, kg + (size_t)j * SLOT + hf * 12288, lds0 + slot * SLOT + w * 3072 + hf * 12288 + u * 1024);
  };
  auto pv = [&](int j, int slot, auto N) AI {
    constexpr int n = N.value, u = n % 3, hf = n / 3;
    glds16nt(u == 0 ? oV0 : u == 1 ? oV1 : oV2, vg + (size_t)j * SLOT + hf * 12288, lds0 + slot * SLOT + w * 3072 + hf * 12288 + u * 1024);
  };
  auto pf_ = [&](auto F, auto N) AI {
    constexpr int f = F.value;
    if constexpr (f >= 36) { if constexpr ((f & 1) == 0) px((f - 36) >> 1, f % NSLOT, N); else pw_((f - 36) >> 1, f % NSLOT, N); }
    else if constexpr ((f & 1) == 0) pk((f - 20) >> 1, f % NSLOT, N); else pv((f - 20) >> 1, f % NSLOT, N);
  };

  const int a8 = i16 >> 1;
  const unsigned kb0 = lds0 + i16 * 384 + ((g ^ a8) << 4);
  const unsigned xb0 = lds0 + (48 * w + i16) * 128 + ((g ^ a8) << 4);
  const int swz = 2 * (g & 1) + (qq >> 1);
  const unsigned vb0 = lds0 + (4 * g + qq) * 384 + (swz << 5) + 8 * pp;
  const unsigned wb0 = lds0 + (8 * g + qq) * 384 + (swz << 5) + 8 * pp;

  asm volatile("" ::: ACLOB);
  f32x4 qa[12][3];
  float inv = 0.f; f32x4 eX, eY;
  auto epi = [&](auto U, auto Pt) AI {
    constexpr int u = U.value, q = u / 6, t = u % 6, part = Pt.value;
    if constexpr (part == 0) {
      if constexpr (t == 0) { float lq; ARD(lq, AO(12, q)); inv = __builtin_amdgcn_rcpf(lq); }
      sfor<4>([&](auto R) AI { ARD(eX[R.value], AO(2 * t, q) + R.value); });
    } else if constexpr (part == 1) {
      sfor<4>([&](auto R) AI { ARD(eY[R.value], AO(2 * t + 1, q) + R.value); });
    } else if constexpr (part == 2) { eX *= inv; eY *= inv; PINF(eX); PINF(eY); }
    else if constexpr (part == 3) {
      u32x4 f = {cvtpk(eX[0], eX[1]), cvtpk(eX[2], eX[3]), cvtpk(eY[0], eY[1]), cvtpk(eY[2], eY[3])};
      *(u32x4*)(og + q * 1024 + t * 12288) = f;
    }
  };
  int sl = 0;
  auto p1step = [&](int c, auto Tc, auto Ec) AI {
    constexpr int TAIL = Tc.value, EPI = Ec.value;
    WAITV_BAR(12);
    const int sn = sl >= 2 ? sl - 2 : sl + 4;
    unsigned ax[2], aw[4];
    ax[0] = adr<0>(xb0, sl * SLOT); ax[1] = adr<64>(xb0, sl * SLOT);
    aw[0] = adr<0>(wb0, (sl + 1) * SLOT); aw[1] = adr<32>(wb0, (sl + 1) * SLOT); aw[2] = adr<64>(wb0, (sl + 1) * SLOT); aw[3] = adr<96>(wb0, (sl + 1) * SLOT);
    sl = sl == 4 ? 0 : sl + 2;
    bf16x8 xf[2][3], wf[3];
    auto rdw = [&](auto F) AI {
      constexpr int f = F.value, s = f / 12, d = f % 12;
      const unsigned p = aw[d & 3] + s * 32 * 384 + (d >> 2) * 128;
      wf[f % 3] = cat8(ldstr(p), ldstr(p + 4 * 384));
    };
#pragma unroll
    for (int q = 0; q < 3; ++q) xf[0][q] = ldsb128(ax[0] + q * 2048);
    rdw(IC<0>{}); rdw(IC<1>{});
    SBAR();
    sfor<24>([&](auto F) AI {
      constexpr int f = F.value, s = f / 12, d = f % 12;
      if constexpr (f + 2 < 24) rdw(IC<f + 2>{});
      if constexpr (f >= 2 && f < 5) xf[1][f - 2] = ldsb128(ax[1] + (f - 2) * 2048);
      mfma_ip_(qa[d][0], wf[f % 3], xf[s][0]);
      if constexpr (EPI >= 0 && EPI < 9) { constexpr int k = f / 12, part = (f % 12) / 2 - 1;
        if constexpr ((f & 1) == 1 && part >= 0 && part < 4) { epi(IC<2 * EPI + k>{}, IC<part>{}); SBAR(); } }
      mfma_ip_(qa[d][1], wf[f % 3], xf[s][1]);
      if constexpr ((f & 1) == 0) {
        constexpr int n = f >> 1;
        if constexpr (n < 6) { if constexpr (!TAIL) px(c + 2, sn, IC<n>{}); else pk(c - 8, sn, IC<n>{}); }
        else { if constexpr (!TAIL) pw_(c + 2, sn + 1, IC<n - 6>{}); else pv(c - 8, sn + 1, IC<n - 6>{}); }
      }
      mfma_ip_(qa[d][2], wf[f % 3], xf[s][2]);
      SBAR();
    });
  };
  auto zero_qa = [&]() AI {
    float z = 0.f; PINF(z);
#pragma unroll
    for (int d = 0; d < 12; ++d) {
#pragma unroll
      for (int q = 0; q < 3; ++q) qa[d][q] = f32x4{z, z, z, z};
      pin3n_(qa[d][0], qa[d][1], qa[d][2]);
    }
  };

  f32x4 sc[2][4][3];
  f32x4 negm[3];
  unsigned pw[2][3][4];
  bf16x8 ones = {0x3F80, 0x3F80, 0x3F80, 0x3F80, 0x3F80, 0x3F80, 0x3F80, 0x3F80};
  asm volatile("" : "+v"(ones));

#define EXP1(X, kb, q, r) do { sc[X][kb][q][r] = __builtin_amdgcn_exp2f(sc[X][kb][q][r]); PINF(sc[X][kb][q][r]); } while (0)
#define CVT1(X, c) do { constexpr int kb_ = (c) / 6, q_ = ((c) % 6) >> 1, hf_ = (c) & 1; \
    pw[kb_ >> 1][q_][2 * (kb_ & 1) + hf_] = cvtpk(sc[X][kb_][q_][2 * hf_], sc[X][kb_][q_][2 * hf_ + 1]); PINU(pw[kb_ >> 1][q_][2 * (kb_ & 1) + hf_]); } while (0)

  auto conv = [&](auto S, auto Q) AI {
    constexpr int s = S.value, q = Q.value;
    const f32x4 X = qa[2 * s][q] * QSCALE, Y = qa[2 * s + 1][q] * QSCALE;
    unsigned x0 = cvtpk(X[0], X[1]), x1 = cvtpk(X[2], X[3]), y0 = cvtpk(Y[0], Y[1]), y1 = cvtpk(Y[2], Y[3]);
    auto r0 = __builtin_amdgcn_permlane32_swap(x0, y0, false, false);
    auto r1 = __builtin_amdgcn_permlane32_swap(x1, y1, false, false);
    auto t0 = __builtin_amdgcn_permlane16_swap(r0[0], r0[1], false, false);
    auto t1 = __builtin_amdgcn_permlane16_swap(r1[0], r1[1], false, false);
    AWR(AQ(s, q) + 0, t0[0]); AWR(AQ(s, q) + 1, t1[0]); AWR(AQ(s, q) + 2, t0[1]); AWR(AQ(s, q) + 3, t1[1]);
  };

  sfor<6>([&](auto N) AI { px(0, 0, N); }); sfor<6>([&](auto N) AI { pw_(0, 1, N); });
  sfor<6>([&](auto N) AI { px(1, 2, N); }); sfor<6>([&](auto N) AI { pw_(1, 3, N); });
  zero_qa();
#pragma unroll 1
  for (int c = 0; c < 8; ++c) p1step(c, IC<0>{}, IC<-1>{});
  p1step(8, IC<1>{}, IC<-1>{}); p1step(9, IC<1>{}, IC<-1>{});

#pragma unroll 1
  for (int it = 0;; ++it) {
    asm volatile("" : "+s"(wg));
    if (it == 0) xg += (size_t)4 * SQ * DM * 2;
    WAITV_BAR(18);
    {
      unsigned ak[2];
      ak[0] = adr<0>(kb0, 2 * SLOT); ak[1] = adr<64>(kb0, 2 * SLOT);
      bf16x8 kf[3];
      auto rdk = [&](auto F) AI { constexpr int f = F.value, s = f >> 2, kb = f & 3; kf[f % 3] = ldsb128(ak[s & 1] + kb * 6144 + (s >> 1) * 128); };
      NOPS(7);
      SBAR();
      conv(IC<0>{}, IC<0>{}); conv(IC<0>{}, IC<1>{}); conv(IC<0>{}, IC<2>{});
      conv(IC<1>{}, IC<0>{}); conv(IC<1>{}, IC<1>{}); conv(IC<1>{}, IC<2>{});
      SBAR();
      rdk(IC<0>{}); rdk(IC<1>{});
      NOPS(1);
      SBAR();
      sfor<24>([&](auto F) AI {
        constexpr int f = F.value, s = f >> 2, kb = f & 3;
        if constexpr (f + 2 < 24) rdk(IC<f + 2>{});
        sfor<3>([&](auto Q) AI {
          constexpr int q = Q.value;
          if constexpr (s == 0) { mfma_s0_<AQ(s, q)>(sc[0][kb][q], kf[f % 3]); } else { MFMA_S(sc[0][kb][q], kf[f % 3], s, q); }
        });
        if constexpr (s < 4 && kb < 3) conv(IC<s + 2>{}, IC<kb>{});
        if constexpr (kb == 3) { pf_(IC<24 + s / 3>{}, IC<2 * (s % 3)>{}); pf_(IC<24 + s / 3>{}, IC<2 * (s % 3) + 1>{}); }
        SBAR();
      });
      asm volatile("s_nop 7" : "+v"(sc[0][0][0]), "+v"(sc[0][0][1]), "+v"(sc[0][0][2]), "+v"(sc[0][1][0]), "+v"(sc[0][1][1]), "+v"(sc[0][1][2]),
                               "+v"(sc[0][2][0]), "+v"(sc[0][2][1]), "+v"(sc[0][2][2]), "+v"(sc[0][3][0]), "+v"(sc[0][3][1]), "+v"(sc[0][3][2]));
#pragma unroll
      for (int q = 0; q < 3; ++q) {
        float m0 = mx3(sc[0][0][q][0], sc[0][0][q][1], sc[0][0][q][2]);
        m0 = mx2(m0, sc[0][0][q][3]);
#pragma unroll
        for (int kb = 1; kb < 4; ++kb) { m0 = mx3(m0, sc[0][kb][q][0], sc[0][kb][q][1]); m0 = mx3(m0, sc[0][kb][q][2], sc[0][kb][q][3]); }
        m0 = xmax(m0);
        negm[q] = f32x4{-m0, -m0, -m0, -m0};
#pragma unroll
        for (int kb = 0; kb < 4; ++kb) sc[0][kb][q] -= m0;
#pragma unroll
        for (int r = 0; r < 4; ++r) sc[0][0][q][r] = __builtin_amdgcn_exp2f(sc[0][0][q][r]);
      }
    }
    sfor<8>([&](auto Jc) AI {
      constexpr int j = Jc.value;
      constexpr int X = j & 1, Y = X ^ 1;
      if constexpr (j == 0 || j == 7) { WAITV_BAR(18); } else { WAITV_BAR(12); }
      if constexpr (j < 7) {
        unsigned ak[2];
        ak[0] = adr<0>(kb0, ((22 + 2 * j) % NSLOT) * SLOT); ak[1] = adr<64>(kb0, ((22 + 2 * j) % NSLOT) * SLOT);
        pin3n_(negm[0], negm[1], negm[2]);
        bf16x8 kf[3];
        auto rdk = [&](auto F) AI { constexpr int f = F.value, s = f >> 2, kb = f & 3; kf[f % 3] = ldsb128(ak[s & 1] + kb * 6144 + (s >> 1) * 128); };
        rdk(IC<0>{}); rdk(IC<1>{});
        SBAR();
        sfor<24>([&](auto F) AI {
          constexpr int f = F.value, s = f >> 2, kb = f & 3;
          if constexpr (f + 2 < 24) rdk(IC<f + 2>{});
          sfor<3>([&](auto Q) AI {
            constexpr int q = Q.value;
            if constexpr (s == 0) { MFMA_SC(sc[Y][kb][q], kf[f % 3], s, q, negm[q]); } else { MFMA_S(sc[Y][kb][q], kf[f % 3], s, q); }
            if constexpr (q == 0) { CVT1(X, f); }
            else {
              constexpr int e = 2 * f + q - 1;
              if constexpr (e < 36) { EXP1(X, 1 + e / 12, (e % 12) >> 2, e & 3); }
              else if constexpr (q == 1 && j >= 1) pf_(IC<25 + 2 * j>{}, IC<f - 18>{});
            }
            SBAR();
          });
        });
      } else {
        sfor<36>([&](auto E) AI { constexpr int e = E.value; EXP1(X, 1 + e / 12, (e % 12) >> 2, e & 3);
          if constexpr ((e % 6) == 5) pf_(IC<39>{}, IC<e / 6>{}); });
        sfor<24>([&](auto C) AI { CVT1(X, C.value); });
      }
      {
        bf16x8 pf[2][3];
#pragma unroll
        for (int t = 0; t < 2; ++t)
#pragma unroll
          for (int q = 0; q < 3; ++q) { u32x4 f = {pw[t][q][0], pw[t][q][1], pw[t][q][2], pw[t][q][3]}; pf[t][q] = __builtin_bit_cast(bf16x8, f); }
        pin3n_(pf[0][0], pf[0][1], pf[0][2]); pin3n_(pf[1][0], pf[1][1], pf[1][2]);
        unsigned av[4];
        { constexpr unsigned so = ((21 + 2 * j) % NSLOT) * SLOT;
          av[0] = adr<0>(vb0, so); av[1] = adr<32>(vb0, so); av[2] = adr<64>(vb0, so); av[3] = adr<96>(vb0, so); }
        bf16x8 vf[3];
        auto rdv = [&](auto F) AI {
          constexpr int f = F.value, t = f / 12, d = f % 12;
          const unsigned p = av[d & 3] + t * 32 * 384 + (d >> 2) * 128;
          vf[f % 3] = cat8(ldstr(p), ldstr(p + 16 * 384));
        };
        float mx[3], fct[3];
        bool pend = false;
        auto mop = [&](auto I) AI {
          constexpr int q = I.value % 3, k = I.value / 3;
          if constexpr (k == 0) mx[q] = mx3(sc[Y][0][q][0], sc[Y][0][q][1], sc[Y][0][q][2]);
          else if constexpr (k == 7) mx[q] = mx2(mx[q], sc[Y][3][q][3]);
          else { constexpr int e = 2 * k + 1; mx[q] = mx3(mx[q], sc[Y][e >> 2][q][e & 3], sc[Y][(e + 1) >> 2][q][(e + 1) & 3]); }
          PINF(mx[q]);
        };
        rdv(IC<0>{}); rdv(IC<1>{});
        SBAR();
        sfor<78>([&](auto Mc) AI {
          constexpr int m = Mc.value, G = m / 3, q = m % 3, t = G / 13, gi = G % 13;
          if constexpr (gi == 0) {
            if constexpr (j == 0 && t == 0) { MFMA_O0(12, q, ones, pf[t][q]); } else { MFMA_O(12, q, ones, pf[t][q]); }
          } else {
            constexpr int d = gi - 1, f = 12 * t + d;
            if constexpr (q == 0 && f + 2 < 24) rdv(IC<f + 2>{});
            if constexpr (j == 0 && t == 0) { MFMA_O0(d, q, vf[f % 3], pf[t][q]); } else { MFMA_O(d, q, vf[f % 3], pf[t][q]); }
          }
          if constexpr (j < 7 && q < 2) {
            if constexpr (G >= 2 && G < 8) { constexpr int i = 4 * (G - 2) + 2 * q; mop(IC<i>{}); mop(IC<i + 1>{}); }
            if constexpr (G == 8 && q == 0) {
              pend = __any(mx3(mx[0], mx[1], mx[2]) > THR);
              if (__builtin_expect(pend, 0)) {
#pragma unroll
                for (int qq_ = 0; qq_ < 3; ++qq_) {
                  const float gr = xmax(mx[qq_]);
                  const unsigned db = (__float_as_uint(fmaxf(gr, 0.f)) + 0xFFFFu) & 0xFFFF0000u;
                  fct[qq_] = __builtin_amdgcn_exp2f(-__uint_as_float(db));
                  u32x4 bw = {g == 0 ? ((db >> 16) | 0x8000u) : 0u, 0u, 0u, 0u};
                  bf16x8 bfr = __builtin_bit_cast(bf16x8, bw);
                  asm volatile("s_nop 1" : "+v"(bfr));
                  mfma_ip_(negm[qq_], ones, bfr);
                  mfma_ip_(sc[Y][0][qq_], ones, bfr); mfma_ip_(sc[Y][1][qq_], ones, bfr); mfma_ip_(sc[Y][2][qq_], ones, bfr); mfma_ip_(sc[Y][3][qq_], ones, bfr);
                }
                NOPS(7);
              }
            }
            if constexpr (G >= 9 && G < 15) { constexpr int e = 2 * (G - 9) + q; EXP1(Y, 0, e >> 2, e & 3); }
          }
          if constexpr (j < 7 && q == 1 && G >= 16 && G < 22) pf_(IC<26 + 2 * j>{}, IC<G - 16>{});
          SBAR();
        });
        if constexpr (j < 7) {
          if (__builtin_expect(pend, 0)) {
            NOPS(7);
            sfor<156>([&](auto N) AI { ASCALE(N.value, fct[(N.value >> 2) % 3]); });
            NOPS(1);
          }
        }
      }
    });
    if (it == 1) break;
    kg += (size_t)4 * NH * SK * DH * 2; vg += (size_t)4 * NH * SK * DH * 2;
    zero_qa();
    sl = 0;
    sfor<10>([&](auto C) AI { p1step(C.value, IC<(C.value >= 8)>{}, IC<C.value>{}); });
    og += (size_t)4 * 192 * 12 * 1024;
  }
  NOPS(7);
  sfor<18>([&](auto U) AI { epi(U, IC<0>{}); epi(U, IC<1>{}); epi(U, IC<2>{}); epi(U, IC<3>{}); });
  asm volatile("s_waitcnt vmcnt(0)" ::: "memory");
}

constexpr int ST2 = 128 * 320;
constexpr int LDS2 = 3 * ST2;
constexpr size_t WS_SLAB = (size_t)64 << 20;
constexpr size_t WS_CNT = (size_t)48 << 20;

DI void gload16(bf16x8& d, unsigned voff, const char* sbase) { asm volatile("global_load_dwordx4 %0, %1, %2" : "=v"(d) : "v"(voff), "s"(sbase) : "memory"); }
template <int N> DI void wait3(bf16x8& a, bf16x8& b, bf16x8& c) { asm volatile("s_waitcnt vmcnt(%3)" : "+v"(a), "+v"(b), "+v"(c) : "i"(N) : "memory"); }

__global__ __launch_bounds__(512, 2) void k_oproj(Ptrs P, char* __restrict__ ws, char* __restrict__ out) {
  extern __shared__ __attribute__((aligned(16))) char smem[];
  lds_ptr lds = (lds_ptr)smem;
  const int tid = threadIdx.x, l = tid & 63, w = __builtin_amdgcn_readfirstlane(tid >> 6);
  const int i16 = l & 15, g = l >> 4, qq = i16 >> 2, pp = i16 & 3;
  const int xcd = blockIdx.x & 7, idx = blockIdx.x >> 3;
  const int br = xcd >> 2, ks = xcd & 3, b = idx >> 2, nq = idx & 3;
  const int kh = w >> 2, qg = w & 3;
  const unsigned lds0 = (unsigned)(uintptr_t)lds;
  const char* wsrc = P.wo[br] + (size_t)ks * 1536 * DM * 2 + nq * 320;
  const char* osrc = ws + (((size_t)(br * NB + b) * 192 + 48 * ks + kh) * 12 + 3 * qg) * 1024;
  const unsigned l16 = l * 16;
  unsigned offW[5];
#pragma unroll
  for (int u = 0; u < 5; ++u) {
    const int o = 1024 * u + 16 * l, r = o / 320, chp = (o % 320) >> 4;
    offW[u] = (16 * w + r) * (DM * 2) + ((chp ^ (((r >> 2) & 1) << 1)) << 4);
  }
  const unsigned o0 = offW[0], o1 = offW[1], o2 = offW[2], o3 = offW[3], o4 = offW[4];
  const int s0 = (3 * nq + b % 3) % 12;
  auto issueW = [&](int s, int stg) AI {
    const unsigned d = lds0 + stg * ST2 + w * 5120;
    s += s0; s = s >= 12 ? s - 12 : s;
    const char* src = wsrc + (size_t)s * 128 * DM * 2;
    glds16(o0, src, d); glds16(o1, src, d + 1024); glds16(o2, src, d + 2048); glds16(o3, src, d + 3072); glds16(o4, src, d + 4096);
  };
  bf16x8 of[4][3];
  auto loadO = [&](int u, auto Bc) AI {
    constexpr int B_ = Bc.value;
    u += 2 * s0; u = u >= 24 ? u - 24 : u;
    const char* src = osrc + (size_t)u * 24576;
    gload16(of[B_][0], l16, src); gload16(of[B_][1], l16, src + 1024); gload16(of[B_][2], l16, src + 2048);
  };
  f32x4 acc[10][3];
#pragma unroll
  for (int n = 0; n < 10; ++n)
#pragma unroll
    for (int q = 0; q < 3; ++q) acc[n][q] = f32x4{0.f, 0.f, 0.f, 0.f};
  const unsigned wb0 = lds0 + (32 * kh + 4 * g + qq) * 320 + 8 * pp + ((g & 1) << 5);
  issueW(0, 0); loadO(0, IC<0>{}); loadO(1, IC<1>{}); loadO(2, IC<2>{});
  issueW(1, 1);
  int stg = 0;
  auto kstep = [&](unsigned ae, unsigned ao, auto Vc, auto Bc) AI {
    constexpr int v = Vc.value, B_ = Bc.value;
    bf16x8 wf[3];
    auto rdw = [&](auto N) AI { constexpr int n = N.value; const unsigned p = ((n & 1) ? ao : ae) + v * 64 * 320 + (n >> 1) * 64; wf[n % 3] = cat8(ldstr(p), ldstr(p + 16 * 320)); };
    rdw(IC<0>{}); rdw(IC<1>{});
    sfor<10>([&](auto N) AI {
      constexpr int n = N.value;
      if constexpr (n + 2 < 10) rdw(IC<n + 2>{});
#pragma unroll
      for (int q = 0; q < 3; ++q) acc[n][q] = MFMA16(wf[n % 3], of[B_][q], acc[n][q]);
    });
  };
  auto stage = [&](int s, auto Pc, auto Mc) AI {
    constexpr int PAR = Pc.value, MODE = Mc.value, B0 = 2 * PAR, B1 = 2 * PAR + 1;
    if constexpr (MODE == 2) { asm volatile("s_waitcnt vmcnt(6) lgkmcnt(0)\n\ts_barrier" ::: "memory"); }
    else { asm volatile("s_waitcnt vmcnt(11) lgkmcnt(0)\n\ts_barrier" ::: "memory"); }
    const unsigned ae = opq(wb0 + stg * ST2), ao = opq((wb0 ^ 32) + stg * ST2);
    if constexpr (MODE == 0) issueW(s + 2, stg == 0 ? 2 : stg - 1);
    if constexpr (MODE <= 1) loadO(2 * s + 3, IC<(B0 + 3) & 3>{});
    wait3<MODE == 0 ? 19 : MODE == 1 ? 14 : 3>(of[B0][0], of[B0][1], of[B0][2]);
    kstep(ae, ao, IC<0>{}, IC<B0>{});
    if constexpr (MODE == 0) loadO(2 * s + 4, IC<B0>{});
    wait3<MODE == 0 ? 14 : MODE == 1 ? 6 : 0>(of[B1][0], of[B1][1], of[B1][2]);
    kstep(ae, ao, IC<1>{}, IC<B1>{});
    stg = stg == 2 ? 0 : stg + 1;
  };
#pragma unroll 1
  for (int s = 0; s < 10; s += 2) { stage(s, IC<0>{}, IC<0>{}); stage(s + 1, IC<1>{}, IC<0>{}); }
  stage(10, IC<0>{}, IC<1>{}); stage(11, IC<1>{}, IC<2>{});
  asm volatile("s_waitcnt vmcnt(0) lgkmcnt(0)\n\ts_barrier" ::: "memory");
  {
    const unsigned xo = lds0 + ((kh ^ 1) * 4 + qg) * 15360 + l16, xi = lds0 + (kh * 4 + qg) * 15360 + l16;
#pragma unroll
    for (int n = 0; n < 5; ++n)
#pragma unroll
      for (int q = 0; q < 3; ++q) *(__attribute__((address_space(3))) f32x4*)(xo + (n * 3 + q) * 1024) = kh ? acc[n][q] : acc[5 + n][q];
    __syncthreads();
#pragma unroll
    for (int n = 0; n < 5; ++n)
#pragma unroll
      for (int q = 0; q < 3; ++q) {
        const f32x4 o = *(const __attribute__((address_space(3))) f32x4*)(xi + (n * 3 + q) * 1024);
        acc[n][q] = (kh ? acc[5 + n][q] : acc[n][q]) + o;
      }
  }
  const int tile = (br * NB + b) * 4 + nq;
  char* slab = ws + WS_SLAB + (size_t)tile * 4 * 8 * 15360;
  {
    char* mine = slab + (size_t)(ks * 8 + w) * 15360 + l16;
#pragma unroll
    for (int i = 0; i < 15; ++i)
      if ((i & 3) != ks) asm volatile("global_store_dwordx4 %0, %1, off sc0 sc1\n\ts_nop 1" :: "v"(mine + i * 1024), "v"(acc[i / 3][i % 3]) : "memory");
    asm volatile("s_waitcnt vmcnt(0)" ::: "memory");
    __syncthreads();
    unsigned* cnt = (unsigned*)(ws + WS_CNT) + tile * 16;
    if (tid == 0) {
      __hip_atomic_fetch_add(cnt, 1u, __ATOMIC_RELAXED, __HIP_MEMORY_SCOPE_AGENT);
      unsigned spins = 0;
      while (__hip_atomic_load(cnt, __ATOMIC_RELAXED, __HIP_MEMORY_SCOPE_AGENT) < 4u && ++spins < (1u << 24)) __builtin_amdgcn_s_sleep(1);
    }
    __syncthreads();
  }
  {
    const char* theirs = slab + (size_t)w * 15360 + l16;
    f32x4 p[4][3];
#pragma unroll
    for (int m = 0; m < 4; ++m) {
      const int i = (4 * m + ks) < 15 ? 4 * m + ks : 11;
#pragma unroll
      for (int r = 0; r < 3; ++r) {
        const int src = (ks + 1 + r) & 3;
        asm volatile("global_load_dwordx4 %0, %1, off sc0 sc1" : "=v"(p[m][r]) : "v"(theirs + (size_t)src * 8 * 15360 + i * 1024) : "memory");
      }
    }
    asm volatile("s_waitcnt vmcnt(0)" : "+v"(p[0][0]), "+v"(p[0][1]), "+v"(p[0][2]), "+v"(p[1][0]), "+v"(p[1][1]), "+v"(p[1][2]),
                                        "+v"(p[2][0]), "+v"(p[2][1]), "+v"(p[2][2]), "+v"(p[3][0]), "+v"(p[3][1]), "+v"(p[3][2]) :: "memory");
#pragma unroll
    for (int i = 0; i < 15; ++i) {
      if ((i & 3) == ks) {
        const f32x4 o = (acc[i / 3][i % 3] + p[i >> 2][0]) + (p[i >> 2][1] + p[i >> 2][2]);
        u32x2 f = {cvtpk(o[0], o[1]), cvtpk(o[2], o[3])};
        char* dst = out + ((size_t)(b * SQ + (3 * qg + i % 3) * 16 + i16) * (2 * DM) + br * DM + nq * 160 + (5 * kh + i / 3) * 16 + 4 * g) * 2;
        *(u32x2*)dst = f;
      }
    }
  }
}

extern "C" void kernel_launch(void* const* d_in, const int* in_sizes, int n_in, void* d_out, int out_size, void* d_ws, size_t ws_size,
                              hipStream_t stream) {
  Ptrs P;
  for (int r = 0; r < 2; ++r) {
    P.x[r] = (const char*)d_in[5 * r + 0];
    P.k[r] = (const char*)d_in[5 * r + 1];
    P.v[r] = (const char*)d_in[5 * r + 2];
    P.wq[r] = (const char*)d_in[5 * r + 3];
    P.wo[r] = (const char*)d_in[5 * r + 4];
  }
  (void)hipFuncSetAttribute((const void*)k_attn, hipFuncAttributeMaxDynamicSharedMemorySize, LDS1);
  (void)hipFuncSetAttribute((const void*)k_oproj, hipFuncAttributeMaxDynamicSharedMemorySize, LDS2);
  hipLaunchKernelGGL(k_attn, dim3(256), dim3(256), LDS1, stream, P, (char*)d_ws);
  hipLaunchKernelGGL(k_oproj, dim3(256), dim3(512), LDS2, stream, P, (char*)d_ws, (char*)d_out);
}
```

```cpp
#include <hip/hip_runtime.h>
#include <hip/hip_bf16.h>
#include <stdint.h>
#include <utility>

using bf16x8 = __attribute__((ext_vector_type(8))) short;
using s16x4 = __attribute__((ext_vector_type(4))) short;
using f32x4 = __attribute__((ext_vector_type(4))) float;
using u32x4 = __attribute__((ext_vector_type(4))) unsigned;
using u32x2 = __attribute__((ext_vector_type(2))) unsigned;
typedef short v4i16_t __attribute__((ext_vector_type(4)));
typedef __attribute__((address_space(3))) char* lds_ptr;
typedef __attribute__((address_space(3))) const char* lds_cptr;

constexpr int NB = 8, SQ = 192, DM = 640, NH = 32, SK = 512, DH = 192;
constexpr int HD = NH * DH;
constexpr int SLOT = 24576;
constexpr int NSLOT = 6;
constexpr int LDS1 = SLOT * NSLOT;
constexpr float QSCALE = 0.07216878364870322f * 1.4426950408889634f;
constexpr float THR = 8.0f;

#define DI __device__ __forceinline__
#define MFMA16(a, b, c) __builtin_amdgcn_mfma_f32_16x16x32_bf16((a), (b), (c), 0, 0, 0)
#define ACLOB "a0","a1","a2","a3","a4","a5","a6","a7","a8","a9","a10","a11","a12","a13","a14","a15","a16","a17","a18","a19","a20","a21","a22","a23","a24","a25","a26","a27","a28","a29","a30","a31","a32","a33","a34","a35","a36","a37","a38","a39","a40","a41","a42","a43","a44","a45","a46","a47","a48","a49","a50","a51","a52","a53","a54","a55","a56","a57","a58","a59","a60","a61","a62","a63","a64","a65","a66","a67","a68","a69","a70","a71","a72","a73","a74","a75","a76","a77","a78","a79","a80","a81","a82","a83","a84","a85","a86","a87","a88","a89","a90","a91","a92","a93","a94","a95","a96","a97","a98","a99","a100","a101","a102","a103","a104","a105","a106","a107","a108","a109","a110","a111","a112","a113","a114","a115","a116","a117","a118","a119","a120","a121","a122","a123","a124","a125","a126","a127","a128","a129","a130","a131","a132","a133","a134","a135","a136","a137","a138","a139","a140","a141","a142","a143","a144","a145","a146","a147","a148","a149","a150","a151","a152","a153","a154","a155","a156","a157","a158","a159","a160","a161","a162","a163","a164","a165","a166","a167","a168","a169","a170","a171","a172","a173","a174","a175","a176","a177","a178","a179","a180","a181","a182","a183","a184","a185","a186","a187","a188","a189","a190","a191","a192","a193","a194","a195","a196","a197","a198","a199","a200","a201","a202","a203","a204","a205","a206","a207","a208","a209","a210","a211","a212","a213","a214","a215","a216","a217","a218","a219","a220","a221","a222","a223","a224","a225","a226","a227"
#define AO(d, q) (4 * (3 * (d) + (q)))
#define AQ(s, q) (156 + 4 * (3 * (s) + (q)))
template <int N> DI void mfma_o_(const bf16x8& a, const bf16x8& b) { asm volatile("v_mfma_f32_16x16x32_bf16 a[%c2:%c3], %0, %1, a[%c2:%c3]" :: "v"(a), "v"(b), "i"(N), "i"(N + 3) : ACLOB); }
template <int N> DI void mfma_o0_(const bf16x8& a, const bf16x8& b) { asm volatile("v_mfma_f32_16x16x32_bf16 a[%c2:%c3], %0, %1, 0" :: "v"(a), "v"(b), "i"(N), "i"(N + 3) : ACLOB); }
template <int N> DI void mfma_s_(f32x4& acc, const bf16x8& a) { asm volatile("v_mfma_f32_16x16x32_bf16 %0, %1, a[%c2:%c3], %0" : "+v"(acc) : "v"(a), "i"(N), "i"(N + 3) : ACLOB); }
template <int N> DI void mfma_sc_(f32x4& acc, const bf16x8& a, const f32x4& c) { asm volatile("v_mfma_f32_16x16x32_bf16 %0, %1, a[%c2:%c3], %4" : "=&v"(acc) : "v"(a), "i"(N), "i"(N + 3), "v"(c) : ACLOB); }
template <int N> DI void mfma_s0_(f32x4& acc, const bf16x8& a) { asm volatile("v_mfma_f32_16x16x32_bf16 %0, %1, a[%c2:%c3], 0" : "=&v"(acc) : "v"(a), "i"(N), "i"(N + 3) : ACLOB); }
template <int N> DI float ard_() { float r; asm volatile("v_accvgpr_read_b32 %0, a[%c1]" : "=v"(r) : "i"(N)); return r; }
template <int N, class T> DI void awr_(T src) { asm volatile("v_accvgpr_write_b32 a[%c1], %0" :: "v"(src), "i"(N)); }
template <int N> DI void ascale_(float f) { float t; asm volatile("v_accvgpr_read_b32 %0, a[%c1]\n\tv_mul_f32 %0, %2, %0\n\tv_accvgpr_write_b32 a[%c1], %0" : "=&v"(t) : "i"(N), "v"(f)); }
DI float subip_(float x, float d) { asm volatile("v_sub_f32 %0, %0, %1" : "+v"(x) : "v"(d)); return x; }
DI void mfma_ip_(f32x4& x, const bf16x8& a, const bf16x8& b) { asm volatile("v_mfma_f32_16x16x32_bf16 %0, %1, %2, %0" : "+v"(x) : "v"(a), "v"(b) : ACLOB); }
template <class T> DI T pin_(T x) { asm volatile("" : "+v"(x)); return x; }
template <class T> DI void pin3n_(T& a, T& b, T& c) { asm volatile("s_nop 1" : "+v"(a), "+v"(b), "+v"(c)); }
#define MFMA_O(d, q, a, b) mfma_o_<AO(d, q)>(a, b)
#define MFMA_O0(d, q, a, b) mfma_o0_<AO(d, q)>(a, b)
#define MFMA_S(acc, a, s, q) mfma_s_<AQ(s, q)>(acc, a)
#define MFMA_SC(acc, a, s, q, c) mfma_sc_<AQ(s, q)>(acc, a, c)
#define ARD(dst, n) dst = ard_<n>()
#define AWR(n, src) awr_<n>(src)
#define ASCALE(n, f) ascale_<n>(f)
#define SUBIP(x, d) x = subip_(x, d)
#define NOPS(n) asm volatile("s_nop " #n ::: "memory")
#define SBAR() __builtin_amdgcn_sched_barrier(0)
#define PINF(x) x = pin_(x)
#define PINU(x) x = pin_(x)
#define WAITV_BAR(N) asm volatile("s_waitcnt vmcnt(" #N ") lgkmcnt(0)\n\ts_barrier" ::: "memory")

DI unsigned cvtpk(float lo, float hi) {
  typedef float f32x2 __attribute__((ext_vector_type(2)));
  typedef __bf16 bf16x2 __attribute__((ext_vector_type(2)));
  f32x2 v = {lo, hi}; bf16x2 b = __builtin_convertvector(v, bf16x2); return __builtin_bit_cast(unsigned, b);
}
DI void glds16(unsigned voff, const char* sbase, unsigned ldsaddr) {
  asm volatile("s_mov_b32 m0, %2\n\ts_nop 0\n\tglobal_load_lds_dwordx4 %0, %1" :: "v"(voff), "s"(sbase), "s"(ldsaddr) : "memory");
}
DI void glds16nt(unsigned voff, const char* sbase, unsigned ldsaddr) {
  asm volatile("s_mov_b32 m0, %2\n\ts_nop 0\n\tglobal_load_lds_dwordx4 %0, %1 nt" :: "v"(voff), "s"(sbase), "s"(ldsaddr) : "memory");
}
DI bf16x8 ldsb128(unsigned p) { return *(const __attribute__((address_space(3))) bf16x8*)p; }
DI s16x4 ldstr(unsigned p) { return __builtin_bit_cast(s16x4, __builtin_amdgcn_ds_read_tr16_b64_v4i16((__attribute__((address_space(3))) v4i16_t*)p)); }
template <int X> DI unsigned adr(unsigned base, unsigned off) {
  unsigned r;
  if constexpr (X == 0) asm volatile("v_add_u32 %0, %2, %1" : "=v"(r) : "v"(base), "s"(off));
  else asm volatile("v_xor_b32 %0, %3, %1\n\tv_add_u32 %0, %2, %0" : "=&v"(r) : "v"(base), "s"(off), "i"(X));
  return r;
}
DI unsigned opq(unsigned x) { asm volatile("" : "+v"(x)); return x; }
DI bf16x8 cat8(s16x4 lo, s16x4 hi) { return __builtin_shufflevector(lo, hi, 0, 1, 2, 3, 4, 5, 6, 7); }
DI float mx3(float a, float b, float c) { float r; asm("v_max3_f32 %0, %1, %2, %3" : "=v"(r) : "v"(a), "v"(b), "v"(c)); return r; }
DI float mx2(float a, float b) { float r; asm("v_max_f32_e32 %0, %1, %2" : "=v"(r) : "v"(a), "v"(b)); return r; }
DI float xmax(float v) {
  auto a = __builtin_amdgcn_permlane32_swap(__float_as_uint(v), __float_as_uint(v), false, false);
  v = mx2(__uint_as_float(a[0]), __uint_as_float(a[1]));
  auto b = __builtin_amdgcn_permlane16_swap(__float_as_uint(v), __float_as_uint(v), false, false);
  return mx2(__uint_as_float(b[0]), __uint_as_float(b[1]));
}
DI float xsum(float v) {
  auto a = __builtin_amdgcn_permlane32_swap(__float_as_uint(v), __float_as_uint(v), false, false);
  v = __uint_as_float(a[0]) + __uint_as_float(a[1]);
  auto b = __builtin_amdgcn_permlane16_swap(__float_as_uint(v), __float_as_uint(v), false, false);
  return __uint_as_float(b[0]) + __uint_as_float(b[1]);
}

template <int N> struct IC { static constexpr int value = N; };
#define AI __attribute__((always_inline))
template <class F, int... Is> DI void sfor_(F&& f, std::integer_sequence<int, Is...>) { (f(IC<Is>{}), ...); }
template <int N, class F> DI void sfor(F&& f) { sfor_(f, std::make_integer_sequence<int, N>{}); }
struct Ptrs { const char* x[2]; const char* k[2]; const char* v[2]; const char* wq[2]; const char* wo[2]; };

__global__ __launch_bounds__(256, 1) void k_attn(Ptrs P, char* __restrict__ ofr) {
  extern __shared__ __attribute__((aligned(16))) char smem[];
  lds_ptr lds = (lds_ptr)smem;
  const int tid = threadIdx.x, l = tid & 63, w = __builtin_amdgcn_readfirstlane(tid >> 6);
  const int i16 = l & 15, g = l >> 4, qq = i16 >> 2, pp = i16 & 3;
  const int xcd = blockIdx.x & 7, idx = blockIdx.x >> 3;
  const int br = xcd >> 2, h = 8 * (xcd & 3) + (idx & 7), b0 = idx >> 3;
  const char* xg = P.x[br] + (size_t)b0 * SQ * DM * 2;
  const char* wg = P.wq[br] + (size_t)h * DH * 2;
  const char* kg = P.k[br] + (size_t)(b0 * NH + h) * SK * DH * 2;
  const char* vg = P.v[br] + (size_t)(b0 * NH + h) * SK * DH * 2;
  char* og = ofr + (((size_t)(br * NB + b0) * 192 + h * 6) * 12 + 3 * w) * 1024 + l * 16;

  char* clr = ofr + ((size_t)64 << 20) + (size_t)(blockIdx.x * 4 + w) * 30720 + l * 16;
  unsigned offK[3], offV[3], offW[3], offX;
#pragma unroll
  for (int u = 0; u < 3; ++u) {
    const int o = 1024 * u + 16 * l, r = o / 384, chp = (o % 384) >> 4, row = 8 * w + r;
    const int aK = (row >> 1) & 7;
    offK[u] = row * 384 + (((chp & ~7) | ((chp ^ aK) & 7)) << 4);
    offV[u] = row * 384 + ((chp ^ (((row >> 1) & 3) << 1)) << 4);
    const int sW = ((row >> 1) & 1) | (((row >> 3) & 1) << 1);
    offW[u] = row * (HD * 2) + ((chp ^ (sW << 1)) << 4);
  }
  { const int row = 8 * w + (l >> 3), a = (row >> 1) & 7; offX = row * (DM * 2) + (((l & 7) ^ a) << 4); }

  const unsigned oK0 = offK[0], oK1 = offK[1], oK2 = offK[2], oV0 = offV[0], oV1 = offV[1], oV2 = offV[2], oW0 = offW[0], oW1 = offW[1], oW2 = offW[2];
  const unsigned lds0 = (unsigned)(uintptr_t)lds;
  auto px = [&](int c, int slot, auto N) AI {
    constexpr int n = N.value;
    glds16(offX, xg + c * 128 + (size_t)n * 32 * DM * 2, lds0 + slot * SLOT + w * 1024 + n * 4096);
  };
  auto pw_ = [&](int c, int slot, auto N) AI {
    constexpr int n = N.value, u = n % 3, hf = n / 3;
    glds16(u == 0 ? oW0 : u == 1 ? oW1 : oW2, wg + (size_t)(c * 64 + hf * 32) * HD * 2, lds0 + slot * SLOT + w * 3072 + hf * 12288 + u * 1024);
  };
  auto pk = [&](int j, int slot, auto N) AI {
    constexpr int n = N.value, u = n % 3, hf = n / 3;
    glds16nt(u == 0 ? oK0 : u == 1 ? oK1 : oK2, kg + (size_t)j * SLOT + hf * 12288, lds0 + slot * SLOT + w * 3072 + hf * 12288 + u * 1024);
  };
  auto pv = [&](int j, int slot, auto N) AI {
    constexpr int n = N.value, u = n % 3, hf = n / 3;
    glds16nt(u == 0 ? oV0 : u == 1 ? oV1 : oV2, vg + (size_t)j * SLOT + hf * 12288, lds0 + slot * SLOT + w * 3072 + hf * 12288 + u * 1024);
  };
  auto pf_ = [&](auto F, auto N) AI {
    constexpr int f = F.value;
    if constexpr (f >= 36) { if constexpr ((f & 1) == 0) px((f - 36) >> 1, f % NSLOT, N); else pw_((f - 36) >> 1, f % NSLOT, N); }
    else if constexpr ((f & 1) == 0) pk((f - 20) >> 1, f % NSLOT, N); else pv((f - 20) >> 1, f % NSLOT, N);
  };

  const int a8 = i16 >> 1;
  const unsigned kb0 = lds0 + i16 * 384 + ((g ^ a8) << 4);
  const unsigned xb0 = lds0 + (48 * w + i16) * 128 + ((g ^ a8) << 4);
  const int swz = 2 * (g & 1) + (qq >> 1);
  const unsigned vb0 = lds0 + (4 * g + qq) * 384 + (swz << 5) + 8 * pp;
  const unsigned wb0 = lds0 + (8 * g + qq) * 384 + (swz << 5) + 8 * pp;

  asm volatile("" ::: ACLOB);
  f32x4 qa[12][3];
  float inv = 0.f; f32x4 eX, eY;
  auto epi = [&](auto U, auto Pt) AI {
    constexpr int u = U.value, q = u / 6, t = u % 6, part = Pt.value;
    if constexpr (part == 0) {
      if constexpr (t == 0) { float lq; ARD(lq, AO(12, q)); inv = __builtin_amdgcn_rcpf(lq); }
      sfor<4>([&](auto R) AI { ARD(eX[R.value], AO(2 * t, q) + R.value); });
    } else if constexpr (part == 1) {
      sfor<4>([&](auto R) AI { ARD(eY[R.value], AO(2 * t + 1, q) + R.value); });
    } else if constexpr (part == 2) { eX *= inv; eY *= inv; PINF(eX); PINF(eY); }
    else if constexpr (part == 3) {
      u32x4 f = {cvtpk(eX[0], eX[1]), cvtpk(eX[2], eX[3]), cvtpk(eY[0], eY[1]), cvtpk(eY[2], eY[3])};
      *(u32x4*)(og + q * 1024 + t * 12288) = f;
    }
  };
  int sl = 0;
  auto p1step = [&](int c, auto Tc, auto Ec) AI {
    constexpr int TAIL = Tc.value, EPI = Ec.value;
    WAITV_BAR(12);
    if constexpr (EPI < 0) {
      u32x4 sv = {0xFFFFFFFFu, 0xFFFFFFFFu, 0xFFFFFFFFu, 0xFFFFFFFFu};
      *(u32x4*)(clr) = sv; *(u32x4*)(clr + 1024) = sv; *(u32x4*)(clr + 2048) = sv;
      clr += 3072;
    }
    const int sn = sl >= 2 ? sl - 2 : sl + 4;
    unsigned ax[2], aw[4];
    ax[0] = adr<0>(xb0, sl * SLOT); ax[1] = adr<64>(xb0, sl * SLOT);
    aw[0] = adr<0>(wb0, (sl + 1) * SLOT); aw[1] = adr<32>(wb0, (sl + 1) * SLOT); aw[2] = adr<64>(wb0, (sl + 1) * SLOT); aw[3] = adr<96>(wb0, (sl + 1) * SLOT);
    sl = sl == 4 ? 0 : sl + 2;
    bf16x8 xf[2][3], wf[3];
    auto rdw = [&](auto F) AI {
      constexpr int f = F.value, s = f / 12, d = f % 12;
      const unsigned p = aw[d & 3] + s * 32 * 384 + (d >> 2) * 128;
      wf[f % 3] = cat8(ldstr(p), ldstr(p + 4 * 384));
    };
#pragma unroll
    for (int q = 0; q < 3; ++q) xf[0][q] = ldsb128(ax[0] + q * 2048);
    rdw(IC<0>{}); rdw(IC<1>{});
    SBAR();
    sfor<24>([&](auto F) AI {
      constexpr int f = F.value, s = f / 12, d = f % 12;
      if constexpr (f + 2 < 24) rdw(IC<f + 2>{});
      if constexpr (f >= 2 && f < 5) xf[1][f - 2] = ldsb128(ax[1] + (f - 2) * 2048);
      mfma_ip_(qa[d][0], wf[f % 3], xf[s][0]);
      if constexpr (EPI >= 0 && EPI < 9) { constexpr int k = f / 12, part = (f % 12) / 2 - 1;
        if constexpr ((f & 1) == 1 && part >= 0 && part < 4) { epi(IC<2 * EPI + k>{}, IC<part>{}); SBAR(); } }
      mfma_ip_(qa[d][1], wf[f % 3], xf[s][1]);
      if constexpr ((f & 1) == 0) {
        constexpr int n = f >> 1;
        if constexpr (n < 6) { if constexpr (!TAIL) px(c + 2, sn, IC<n>{}); else pk(c - 8, sn, IC<n>{}); }
        else { if constexpr (!TAIL) pw_(c + 2, sn + 1, IC<n - 6>{}); else pv(c - 8, sn + 1, IC<n - 6>{}); }
      }
      mfma_ip_(qa[d][2], wf[f % 3], xf[s][2]);
      SBAR();
    });
  };
  auto zero_qa = [&]() AI {
    float z = 0.f; PINF(z);
#pragma unroll
    for (int d = 0; d < 12; ++d) {
#pragma unroll
      for (int q = 0; q < 3; ++q) qa[d][q] = f32x4{z, z, z, z};
      pin3n_(qa[d][0], qa[d][1], qa[d][2]);
    }
  };

  f32x4 sc[2][4][3];
  f32x4 negm[3];
  unsigned pw[2][3][4];
  bf16x8 ones = {0x3F80, 0x3F80, 0x3F80, 0x3F80, 0x3F80, 0x3F80, 0x3F80, 0x3F80};
  asm volatile("" : "+v"(ones));

#define EXP1(X, kb, q, r) do { sc[X][kb][q][r] = __builtin_amdgcn_exp2f(sc[X][kb][q][r]); PINF(sc[X][kb][q][r]); } while (0)
#define CVT1(X, c) do { constexpr int kb_ = (c) / 6, q_ = ((c) % 6) >> 1, hf_ = (c) & 1; \
    pw[kb_ >> 1][q_][2 * (kb_ & 1) + hf_] = cvtpk(sc[X][kb_][q_][2 * hf_], sc[X][kb_][q_][2 * hf_ + 1]); PINU(pw[kb_ >> 1][q_][2 * (kb_ & 1) + hf_]); } while (0)

  auto conv = [&](auto S, auto Q) AI {
    constexpr int s = S.value, q = Q.value;
    const f32x4 X = qa[2 * s][q] * QSCALE, Y = qa[2 * s + 1][q] * QSCALE;
    unsigned x0 = cvtpk(X[0], X[1]), x1 = cvtpk(X[2], X[3]), y0 = cvtpk(Y[0], Y[1]), y1 = cvtpk(Y[2], Y[3]);
    auto r0 = __builtin_amdgcn_permlane32_swap(x0, y0, false, false);
    auto r1 = __builtin_amdgcn_permlane32_swap(x1, y1, false, false);
    auto t0 = __builtin_amdgcn_permlane16_swap(r0[0], r0[1], false, false);
    auto t1 = __builtin_amdgcn_permlane16_swap(r1[0], r1[1], false, false);
    AWR(AQ(s, q) + 0, t0[0]); AWR(AQ(s, q) + 1, t1[0]); AWR(AQ(s, q) + 2, t0[1]); AWR(AQ(s, q) + 3, t1[1]);
  };

  sfor<6>([&](auto N) AI { px(0, 0, N); }); sfor<6>([&](auto N) AI { pw_(0, 1, N); });
  sfor<6>([&](auto N) AI { px(1, 2, N); }); sfor<6>([&](auto N) AI { pw_(1, 3, N); });
  zero_qa();
#pragma unroll 1
  for (int c = 0; c < 8; ++c) p1step(c, IC<0>{}, IC<-1>{});
  p1step(8, IC<1>{}, IC<-1>{}); p1step(9, IC<1>{}, IC<-1>{});

#pragma unroll 1
  for (int it = 0;; ++it) {
    asm volatile("" : "+s"(wg));
    if (it == 0) xg += (size_t)4 * SQ * DM * 2;
    WAITV_BAR(18);
    {
      unsigned ak[2];
      ak[0] = adr<0>(kb0, 2 * SLOT); ak[1] = adr<64>(kb0, 2 * SLOT);
      bf16x8 kf[3];
      auto rdk = [&](auto F) AI { constexpr int f = F.value, s = f >> 2, kb = f & 3; kf[f % 3] = ldsb128(ak[s & 1] + kb * 6144 + (s >> 1) * 128); };
      NOPS(7);
      SBAR();
      conv(IC<0>{}, IC<0>{}); conv(IC<0>{}, IC<1>{}); conv(IC<0>{}, IC<2>{});
      conv(IC<1>{}, IC<0>{}); conv(IC<1>{}, IC<1>{}); conv(IC<1>{}, IC<2>{});
      SBAR();
      rdk(IC<0>{}); rdk(IC<1>{});
      NOPS(1);
      SBAR();
      sfor<24>([&](auto F) AI {
        constexpr int f = F.value, s = f >> 2, kb = f & 3;
        if constexpr (f + 2 < 24) rdk(IC<f + 2>{});
        sfor<3>([&](auto Q) AI {
          constexpr int q = Q.value;
          if constexpr (s == 0) { mfma_s0_<AQ(s, q)>(sc[0][kb][q], kf[f % 3]); } else { MFMA_S(sc[0][kb][q], kf[f % 3], s, q); }
        });
        if constexpr (s < 4 && kb < 3) conv(IC<s + 2>{}, IC<kb>{});
        if constexpr (kb == 3) { pf_(IC<24 + s / 3>{}, IC<2 * (s % 3)>{}); pf_(IC<24 + s / 3>{}, IC<2 * (s % 3) + 1>{}); }
        SBAR();
      });
      asm volatile("s_nop 7" : "+v"(sc[0][0][0]), "+v"(sc[0][0][1]), "+v"(sc[0][0][2]), "+v"(sc[0][1][0]), "+v"(sc[0][1][1]), "+v"(sc[0][1][2]),
                               "+v"(sc[0][2][0]), "+v"(sc[0][2][1]), "+v"(sc[0][2][2]), "+v"(sc[0][3][0]), "+v"(sc[0][3][1]), "+v"(sc[0][3][2]));
#pragma unroll
      for (int q = 0; q < 3; ++q) {
        float m0 = mx3(sc[0][0][q][0], sc[0][0][q][1], sc[0][0][q][2]);
        m0 = mx2(m0, sc[0][0][q][3]);
#pragma unroll
        for (int kb = 1; kb < 4; ++kb) { m0 = mx3(m0, sc[0][kb][q][0], sc[0][kb][q][1]); m0 = mx3(m0, sc[0][kb][q][2], sc[0][kb][q][3]); }
        m0 = xmax(m0);
        negm[q] = f32x4{-m0, -m0, -m0, -m0};
#pragma unroll
        for (int kb = 0; kb < 4; ++kb) sc[0][kb][q] -= m0;
#pragma unroll
        for (int r = 0; r < 4; ++r) sc[0][0][q][r] = __builtin_amdgcn_exp2f(sc[0][0][q][r]);
      }
    }
    sfor<8>([&](auto Jc) AI {
      constexpr int j = Jc.value;
      constexpr int X = j & 1, Y = X ^ 1;
      if constexpr (j == 0 || j == 7) { WAITV_BAR(18); } else { WAITV_BAR(12); }
      if constexpr (j < 7) {
        unsigned ak[2];
        ak[0] = adr<0>(kb0, ((22 + 2 * j) % NSLOT) * SLOT); ak[1] = adr<64>(kb0, ((22 + 2 * j) % NSLOT) * SLOT);
        pin3n_(negm[0], negm[1], negm[2]);
        bf16x8 kf[3];
        auto rdk = [&](auto F) AI { constexpr int f = F.value, s = f >> 2, kb = f & 3; kf[f % 3] = ldsb128(ak[s & 1] + kb * 6144 + (s >> 1) * 128); };
        rdk(IC<0>{}); rdk(IC<1>{});
        SBAR();
        sfor<24>([&](auto F) AI {
          constexpr int f = F.value, s = f >> 2, kb = f & 3;
          if constexpr (f + 2 < 24) rdk(IC<f + 2>{});
          sfor<3>([&](auto Q) AI {
            constexpr int q = Q.value;
            if constexpr (s == 0) { MFMA_SC(sc[Y][kb][q], kf[f % 3], s, q, negm[q]); } else { MFMA_S(sc[Y][kb][q], kf[f % 3], s, q); }
            if constexpr (q == 0) { CVT1(X, f); }
            else {
              constexpr int e = 2 * f + q - 1;
              if constexpr (e < 36) { EXP1(X, 1 + e / 12, (e % 12) >> 2, e & 3); }
              else if constexpr (q == 1 && j >= 1) pf_(IC<25 + 2 * j>{}, IC<f - 18>{});
            }
            SBAR();
          });
        });
      } else {
        sfor<36>([&](auto E) AI { constexpr int e = E.value; EXP1(X, 1 + e / 12, (e % 12) >> 2, e & 3);
          if constexpr ((e % 6) == 5) pf_(IC<39>{}, IC<e / 6>{}); });
        sfor<24>([&](auto C) AI { CVT1(X, C.value); });
      }
      {
        bf16x8 pf[2][3];
#pragma unroll
        for (int t = 0; t < 2; ++t)
#pragma unroll
          for (int q = 0; q < 3; ++q) { u32x4 f = {pw[t][q][0], pw[t][q][1], pw[t][q][2], pw[t][q][3]}; pf[t][q] = __builtin_bit_cast(bf16x8, f); }
        pin3n_(pf[0][0], pf[0][1], pf[0][2]); pin3n_(pf[1][0], pf[1][1], pf[1][2]);
        unsigned av[4];
        { constexpr unsigned so = ((21 + 2 * j) % NSLOT) * SLOT;
          av[0] = adr<0>(vb0, so); av[1] = adr<32>(vb0, so); av[2] = adr<64>(vb0, so); av[3] = adr<96>(vb0, so); }
        bf16x8 vf[3];
        auto rdv = [&](auto F) AI {
          constexpr int f = F.value, t = f / 12, d = f % 12;
          const unsigned p = av[d & 3] + t * 32 * 384 + (d >> 2) * 128;
          vf[f % 3] = cat8(ldstr(p), ldstr(p + 16 * 384));
        };
        float mx[3], fct[3];
        bool pend = false;
        auto mop = [&](auto I) AI {
          constexpr int q = I.value % 3, k = I.value / 3;
          if constexpr (k == 0) mx[q] = mx3(sc[Y][0][q][0], sc[Y][0][q][1], sc[Y][0][q][2]);
          else if constexpr (k == 7) mx[q] = mx2(mx[q], sc[Y][3][q][3]);
          else { constexpr int e = 2 * k + 1; mx[q] = mx3(mx[q], sc[Y][e >> 2][q][e & 3], sc[Y][(e + 1) >> 2][q][(e + 1) & 3]); }
          PINF(mx[q]);
        };
        rdv(IC<0>{}); rdv(IC<1>{});
        SBAR();
        sfor<78>([&](auto Mc) AI {
          constexpr int m = Mc.value, G = m / 3, q = m % 3, t = G / 13, gi = G % 13;
          if constexpr (gi == 0) {
            if constexpr (j == 0 && t == 0) { MFMA_O0(12, q, ones, pf[t][q]); } else { MFMA_O(12, q, ones, pf[t][q]); }
          } else {
            constexpr int d = gi - 1, f = 12 * t + d;
            if constexpr (q == 0 && f + 2 < 24) rdv(IC<f + 2>{});
            if constexpr (j == 0 && t == 0) { MFMA_O0(d, q, vf[f % 3], pf[t][q]); } else { MFMA_O(d, q, vf[f % 3], pf[t][q]); }
          }
          if constexpr (j < 7 && q < 2) {
            if constexpr (G >= 2 && G < 8) { constexpr int i = 4 * (G - 2) + 2 * q; mop(IC<i>{}); mop(IC<i + 1>{}); }
            if constexpr (G == 8 && q == 0) {
              pend = __any(mx3(mx[0], mx[1], mx[2]) > THR);
              if (__builtin_expect(pend, 0)) {
#pragma unroll
                for (int qq_ = 0; qq_ < 3; ++qq_) {
                  const float gr = xmax(mx[qq_]);
                  const unsigned db = (__float_as_uint(fmaxf(gr, 0.f)) + 0xFFFFu) & 0xFFFF0000u;
                  fct[qq_] = __builtin_amdgcn_exp2f(-__uint_as_float(db));
                  u32x4 bw = {g == 0 ? ((db >> 16) | 0x8000u) : 0u, 0u, 0u, 0u};
                  bf16x8 bfr = __builtin_bit_cast(bf16x8, bw);
                  asm volatile("s_nop 1" : "+v"(bfr));
                  mfma_ip_(negm[qq_], ones, bfr);
                  mfma_ip_(sc[Y][0][qq_], ones, bfr); mfma_ip_(sc[Y][1][qq_], ones, bfr); mfma_ip_(sc[Y][2][qq_], ones, bfr); mfma_ip_(sc[Y][3][qq_], ones, bfr);
                }
                NOPS(7);
              }
            }
            if constexpr (G >= 9 && G < 15) { constexpr int e = 2 * (G - 9) + q; EXP1(Y, 0, e >> 2, e & 3); }
          }
          if constexpr (j < 7 && q == 1 && G >= 16 && G < 22) pf_(IC<26 + 2 * j>{}, IC<G - 16>{});
          SBAR();
        });
        if constexpr (j < 7) {
          if (__builtin_expect(pend, 0)) {
            NOPS(7);
            sfor<156>([&](auto N) AI { ASCALE(N.value, fct[(N.value >> 2) % 3]); });
            NOPS(1);
          }
        }
      }
    });
    if (it == 1) break;
    kg += (size_t)4 * NH * SK * DH * 2; vg += (size_t)4 * NH * SK * DH * 2;
    zero_qa();
    sl = 0;
    sfor<10>([&](auto C) AI { p1step(C.value, IC<(C.value >= 8)>{}, IC<C.value>{}); });
    og += (size_t)4 * 192 * 12 * 1024;
  }
  NOPS(7);
  sfor<18>([&](auto U) AI { epi(U, IC<0>{}); epi(U, IC<1>{}); epi(U, IC<2>{}); epi(U, IC<3>{}); });
  asm volatile("s_waitcnt vmcnt(0)" ::: "memory");
}

constexpr int ST2 = 128 * 320;
constexpr int LDS2 = 3 * ST2;
constexpr size_t WS_SLAB = (size_t)64 << 20;
constexpr size_t WS_CNT = (size_t)48 << 20;

DI void gload16(bf16x8& d, unsigned voff, const char* sbase) { asm volatile("global_load_dwordx4 %0, %1, %2" : "=v"(d) : "v"(voff), "s"(sbase) : "memory"); }
template <int N> DI void wait3(bf16x8& a, bf16x8& b, bf16x8& c) { asm volatile("s_waitcnt vmcnt(%3)" : "+v"(a), "+v"(b), "+v"(c) : "i"(N) : "memory"); }

__global__ __launch_bounds__(512, 2) void k_oproj(Ptrs P, char* __restrict__ ws, char* __restrict__ out) {
  extern __shared__ __attribute__((aligned(16))) char smem[];
  lds_ptr lds = (lds_ptr)smem;
  const int tid = threadIdx.x, l = tid & 63, w = __builtin_amdgcn_readfirstlane(tid >> 6);
  const int i16 = l & 15, g = l >> 4, qq = i16 >> 2, pp = i16 & 3;
  const int xcd = blockIdx.x & 7, idx = blockIdx.x >> 3;
  const int br = xcd >> 2, ks = xcd & 3, b = idx >> 2, nq = idx & 3;
  const int kh = w >> 2, qg = w & 3;
  const unsigned lds0 = (unsigned)(uintptr_t)lds;
  const char* wsrc = P.wo[br] + (size_t)ks * 1536 * DM * 2 + nq * 320;
  const char* osrc = ws + (((size_t)(br * NB + b) * 192 + 48 * ks + kh) * 12 + 3 * qg) * 1024;
  const unsigned l16 = l * 16;
  unsigned offW[5];
#pragma unroll
  for (int u = 0; u < 5; ++u) {
    const int o = 1024 * u + 16 * l, r = o / 320, chp = (o % 320) >> 4;
    offW[u] = (16 * w + r) * (DM * 2) + ((chp ^ (((r >> 2) & 1) << 1)) << 4);
  }
  const unsigned o0 = offW[0], o1 = offW[1], o2 = offW[2], o3 = offW[3], o4 = offW[4];
  const int s0 = 0;
  auto wpiece = [&](const char* src, unsigned d, auto Uc) AI {
    constexpr int u = Uc.value;
    glds16(u == 0 ? o0 : u == 1 ? o1 : u == 2 ? o2 : u == 3 ? o3 : o4, src, d + u * 1024);
  };
  auto wsrc_of = [&](int s) AI { s += s0; s = s >= 12 ? s - 12 : s; return wsrc + (size_t)s * 128 * DM * 2; };
  auto osrc_of = [&](int u) AI { u += 2 * s0; u = u >= 24 ? u - 24 : u; return osrc + (size_t)u * 24576; };
  auto issueW = [&](int s, int stg) AI {
    const unsigned d = lds0 + stg * ST2 + w * 5120;
    const char* src = wsrc_of(s);
    sfor<5>([&](auto U) AI { wpiece(src, d, U); });
  };
  bf16x8 of[4][3];
  auto loadO = [&](int u, auto Bc) AI {
    constexpr int B_ = Bc.value;
    const char* src = osrc_of(u);
    gload16(of[B_][0], l16, src); gload16(of[B_][1], l16, src + 1024); gload16(of[B_][2], l16, src + 2048);
  };
  f32x4 acc[10][3];
#pragma unroll
  for (int n = 0; n < 10; ++n)
#pragma unroll
    for (int q = 0; q < 3; ++q) acc[n][q] = f32x4{0.f, 0.f, 0.f, 0.f};
  const unsigned wb0 = lds0 + (32 * kh + 4 * g + qq) * 320 + 8 * pp + ((g & 1) << 5);
  issueW(0, 0); loadO(0, IC<0>{}); loadO(1, IC<1>{}); loadO(2, IC<2>{});
  issueW(1, 1);
  int stg = 0;
  auto kstep = [&](unsigned ae, unsigned ao, auto Vc, auto Bc, auto Lc, const char* lsrc, const char* dsrc, unsigned dd, auto Dc) AI {
    constexpr int v = Vc.value, B_ = Bc.value, LB = Lc.value, DN = Dc.value;
    bf16x8 wf[3];
    auto rdw = [&](auto N) AI { constexpr int n = N.value; const unsigned p = ((n & 1) ? ao : ae) + v * 64 * 320 + (n >> 1) * 64; wf[n % 3] = cat8(ldstr(p), ldstr(p + 16 * 320)); };
    rdw(IC<0>{}); rdw(IC<1>{});
    SBAR();
    sfor<10>([&](auto N) AI {
      constexpr int n = N.value;
      if constexpr (n + 2 < 10) rdw(IC<n + 2>{});
      acc[n][0] = MFMA16(wf[n % 3], of[B_][0], acc[n][0]);
      if constexpr (LB >= 0 && n < 3) { SBAR(); gload16(of[LB][n], l16, lsrc + n * 1024); SBAR(); }
      if constexpr (DN > 0 && n >= 4 && n < 4 + 2 * DN && (n & 1) == 0) { SBAR(); wpiece(dsrc, dd, IC<(v ? 2 : 0) + (n - 4) / 2>{}); SBAR(); }
      acc[n][1] = MFMA16(wf[n % 3], of[B_][1], acc[n][1]);
      acc[n][2] = MFMA16(wf[n % 3], of[B_][2], acc[n][2]);
      SBAR();
    });
  };
  auto stage = [&](int s, auto Pc, auto Mc) AI {
    constexpr int PAR = Pc.value, MODE = Mc.value, B0 = 2 * PAR, B1 = 2 * PAR + 1;
    if constexpr (MODE == 2) { asm volatile("s_waitcnt vmcnt(3) lgkmcnt(0)\n\ts_barrier" ::: "memory"); }
    else { asm volatile("s_waitcnt vmcnt(11) lgkmcnt(0)\n\ts_barrier" ::: "memory"); }
    wait3<63>(of[B0][0], of[B0][1], of[B0][2]);
    const unsigned ae = opq(wb0 + stg * ST2), ao = opq((wb0 ^ 32) + stg * ST2);
    const unsigned dd = lds0 + (stg == 0 ? 2 : stg - 1) * ST2 + w * 5120;
    const char* dsrc = wsrc_of(s + 2);
    kstep(ae, ao, IC<0>{}, IC<B0>{}, IC<(MODE <= 1 ? ((B0 + 3) & 3) : -1)>{}, osrc_of(2 * s + 3), dsrc, dd, IC<(MODE == 0 ? 2 : 0)>{});
    wait3<MODE == 0 ? 13 : MODE == 1 ? 11 : 0>(of[B1][0], of[B1][1], of[B1][2]);
    kstep(ae, ao, IC<1>{}, IC<B1>{}, IC<(MODE == 0 ? B0 : -1)>{}, osrc_of(2 * s + 4), dsrc, dd, IC<(MODE == 0 ? 3 : 0)>{});
    stg = stg == 2 ? 0 : stg + 1;
  };
#pragma unroll 1
  for (int s = 0; s < 10; s += 2) { stage(s, IC<0>{}, IC<0>{}); stage(s + 1, IC<1>{}, IC<0>{}); }
  stage(10, IC<0>{}, IC<1>{}); stage(11, IC<1>{}, IC<2>{});
  asm volatile("s_waitcnt vmcnt(0) lgkmcnt(0)\n\ts_barrier" ::: "memory");
  {
    const unsigned xo = lds0 + ((kh ^ 1) * 4 + qg) * 15360 + l16, xi = lds0 + (kh * 4 + qg) * 15360 + l16;
#pragma unroll
    for (int n = 0; n < 5; ++n)
#pragma unroll
      for (int q = 0; q < 3; ++q) *(__attribute__((address_space(3))) f32x4*)(xo + (n * 3 + q) * 1024) = kh ? acc[n][q] : acc[5 + n][q];
    __syncthreads();
#pragma unroll
    for (int n = 0; n < 5; ++n)
#pragma unroll
      for (int q = 0; q < 3; ++q) {
        const f32x4 o = *(const __attribute__((address_space(3))) f32x4*)(xi + (n * 3 + q) * 1024);
        acc[n][q] = (kh ? acc[5 + n][q] : acc[n][q]) + o;
      }
  }
  const int tile = (br * NB + b) * 4 + nq;
  char* slab = ws + WS_SLAB + (size_t)tile * 4 * 8 * 15360;
  {
    char* mine = slab + (size_t)(ks * 8 + w) * 15360 + l16;
#pragma unroll
    for (int i = 0; i < 15; ++i)
      if ((i & 3) != ks) asm volatile("global_store_dwordx4 %0, %1, off sc0 sc1\n\ts_nop 1" :: "v"(mine + i * 1024), "v"(acc[i / 3][i % 3]) : "memory");
  }
  {
    const char* theirs = slab + (size_t)w * 15360 + l16;
    f32x4 p[4][3];
    for (unsigned spins = 0; spins < (1u << 20); ++spins) {
#pragma unroll
      for (int m = 0; m < 4; ++m) {
        const int i = (4 * m + ks) < 15 ? 4 * m + ks : 11;
#pragma unroll
        for (int r = 0; r < 3; ++r) {
          const int src = (ks + 1 + r) & 3;
          asm volatile("global_load_dwordx4 %0, %1, off sc0 sc1" : "=v"(p[m][r]) : "v"(theirs + (size_t)src * 8 * 15360 + i * 1024) : "memory");
        }
      }
      asm volatile("s_waitcnt vmcnt(0)" : "+v"(p[0][0]), "+v"(p[0][1]), "+v"(p[0][2]), "+v"(p[1][0]), "+v"(p[1][1]), "+v"(p[1][2]),
                                          "+v"(p[2][0]), "+v"(p[2][1]), "+v"(p[2][2]), "+v"(p[3][0]), "+v"(p[3][1]), "+v"(p[3][2]) :: "memory");
      bool ok = true;
#pragma unroll
      for (int m = 0; m < 4; ++m)
#pragma unroll
        for (int r = 0; r < 3; ++r)
#pragma unroll
          for (int e = 0; e < 4; ++e) ok &= __float_as_uint(p[m][r][e]) != 0xFFFFFFFFu;
      if (__all(ok)) break;
      __builtin_amdgcn_s_sleep(2);
    }
#pragma unroll
    for (int i = 0; i < 15; ++i) {
      if ((i & 3) == ks) {
        const f32x4 o = (acc[i / 3][i % 3] + p[i >> 2][0]) + (p[i >> 2][1] + p[i >> 2][2]);
        u32x2 f = {cvtpk(o[0], o[1]), cvtpk(o[2], o[3])};
        char* dst = out + ((size_t)(b * SQ + (3 * qg + i % 3) * 16 + i16) * (2 * DM) + br * DM + nq * 160 + (5 * kh + i / 3) * 16 + 4 * g) * 2;
        *(u32x2*)dst = f;
      }
    }
  }
}

extern "C" void kernel_launch(void* const* d_in, const int* in_sizes, int n_in, void* d_out, int out_size, void* d_ws, size_t ws_size,
                              hipStream_t stream) {
  Ptrs P;
  for (int r = 0; r < 2; ++r) {
    P.x[r] = (const char*)d_in[5 * r + 0];
    P.k[r] = (const char*)d_in[5 * r + 1];
    P.v[r] = (const char*)d_in[5 * r + 2];
    P.wq[r] = (const char*)d_in[5 * r + 3];
    P.wo[r] = (const char*)d_in[5 * r + 4];
  }
  (void)hipFuncSetAttribute((const void*)k_attn, hipFuncAttributeMaxDynamicSharedMemorySize, LDS1);
  (void)hipFuncSetAttribute((const void*)k_oproj, hipFuncAttributeMaxDynamicSharedMemorySize, LDS2);
  hipLaunchKernelGGL(k_attn, dim3(256), dim3(256), LDS1, stream, P, (char*)d_ws);
  hipLaunchKernelGGL(k_oproj, dim3(256), dim3(512), LDS2, stream, P, (char*)d_ws, (char*)d_out);
}
```

```cpp
#include <hip/hip_runtime.h>
#include <hip/hip_bf16.h>
#include <stdint.h>
#include <utility>

using bf16x8 = __attribute__((ext_vector_type(8))) short;
using s16x4 = __attribute__((ext_vector_type(4))) short;
using f32x4 = __attribute__((ext_vector_type(4))) float;
using u32x4 = __attribute__((ext_vector_type(4))) unsigned;
using u32x2 = __attribute__((ext_vector_type(2))) unsigned;
typedef short v4i16_t __attribute__((ext_vector_type(4)));
typedef __attribute__((address_space(3))) char* lds_ptr;
typedef __attribute__((address_space(3))) const char* lds_cptr;

constexpr int NB = 8, SQ = 192, DM = 640, NH = 32, SK = 512, DH = 192;
constexpr int HD = NH * DH;
constexpr int SLOT = 24576;
constexpr int NSLOT = 6;
constexpr int LDS1 = SLOT * NSLOT;
constexpr float QSCALE = 0.07216878364870322f * 1.4426950408889634f;
constexpr float THR = 8.0f;

#define DI __device__ __forceinline__
#define MFMA16(a, b, c) __builtin_amdgcn_mfma_f32_16x16x32_bf16((a), (b), (c), 0, 0, 0)
#define ACLOB "a0","a1","a2","a3","a4","a5","a6","a7","a8","a9","a10","a11","a12","a13","a14","a15","a16","a17","a18","a19","a20","a21","a22","a23","a24","a25","a26","a27","a28","a29","a30","a31","a32","a33","a34","a35","a36","a37","a38","a39","a40","a41","a42","a43","a44","a45","a46","a47","a48","a49","a50","a51","a52","a53","a54","a55","a56","a57","a58","a59","a60","a61","a62","a63","a64","a65","a66","a67","a68","a69","a70","a71","a72","a73","a74","a75","a76","a77","a78","a79","a80","a81","a82","a83","a84","a85","a86","a87","a88","a89","a90","a91","a92","a93","a94","a95","a96","a97","a98","a99","a100","a101","a102","a103","a104","a105","a106","a107","a108","a109","a110","a111","a112","a113","a114","a115","a116","a117","a118","a119","a120","a121","a122","a123","a124","a125","a126","a127","a128","a129","a130","a131","a132","a133","a134","a135","a136","a137","a138","a139","a140","a141","a142","a143","a144","a145","a146","a147","a148","a149","a150","a151","a152","a153","a154","a155","a156","a157","a158","a159","a160","a161","a162","a163","a164","a165","a166","a167","a168","a169","a170","a171","a172","a173","a174","a175","a176","a177","a178","a179","a180","a181","a182","a183","a184","a185","a186","a187","a188","a189","a190","a191","a192","a193","a194","a195","a196","a197","a198","a199","a200","a201","a202","a203","a204","a205","a206","a207","a208","a209","a210","a211","a212","a213","a214","a215","a216","a217","a218","a219","a220","a221","a222","a223","a224","a225","a226","a227"
#define AO(d, q) (4 * (3 * (d) + (q)))
#define AQ(s, q) (156 + 4 * (3 * (s) + (q)))
template <int N> DI void mfma_o_(const bf16x8& a, const bf16x8& b) { asm volatile("v_mfma_f32_16x16x32_bf16 a[%c2:%c3], %0, %1, a[%c2:%c3]" :: "v"(a), "v"(b), "i"(N), "i"(N + 3) : ACLOB); }
template <int N> DI void mfma_o0_(const bf16x8& a, const bf16x8& b) { asm volatile("v_mfma_f32_16x16x32_bf16 a[%c2:%c3], %0, %1, 0" :: "v"(a), "v"(b), "i"(N), "i"(N + 3) : ACLOB); }
template <int N> DI void mfma_s_(f32x4& acc, const bf16x8& a) { asm volatile("v_mfma_f32_16x16x32_bf16 %0, %1, a[%c2:%c3], %0" : "+v"(acc) : "v"(a), "i"(N), "i"(N + 3) : ACLOB); }
template <int N> DI void mfma_sc_(f32x4& acc, const bf16x8& a, const f32x4& c) { asm volatile("v_mfma_f32_16x16x32_bf16 %0, %1, a[%c2:%c3], %4" : "=&v"(acc) : "v"(a), "i"(N), "i"(N + 3), "v"(c) : ACLOB); }
template <int N> DI void mfma_s0_(f32x4& acc, const bf16x8& a) { asm volatile("v_mfma_f32_16x16x32_bf16 %0, %1, a[%c2:%c3], 0" : "=&v"(acc) : "v"(a), "i"(N), "i"(N + 3) : ACLOB); }
template <int N> DI float ard_() { float r; asm volatile("v_accvgpr_read_b32 %0, a[%c1]" : "=v"(r) : "i"(N)); return r; }
template <int N, class T> DI void awr_(T src) { asm volatile("v_accvgpr_write_b32 a[%c1], %0" :: "v"(src), "i"(N)); }
template <int N> DI void ascale_(float f) { float t; asm volatile("v_accvgpr_read_b32 %0, a[%c1]\n\tv_mul_f32 %0, %2, %0\n\tv_accvgpr_write_b32 a[%c1], %0" : "=&v"(t) : "i"(N), "v"(f)); }
DI float subip_(float x, float d) { asm volatile("v_sub_f32 %0, %0, %1" : "+v"(x) : "v"(d)); return x; }
DI void mfma_ip_(f32x4& x, const bf16x8& a, const bf16x8& b) { asm volatile("v_mfma_f32_16x16x32_bf16 %0, %1, %2, %0" : "+v"(x) : "v"(a), "v"(b) : ACLOB); }
template <class T> DI T pin_(T x) { asm volatile("" : "+v"(x)); return x; }
template <class T> DI void pin3n_(T& a, T& b, T& c) { asm volatile("s_nop 1" : "+v"(a), "+v"(b), "+v"(c)); }
#define MFMA_O(d, q, a, b) mfma_o_<AO(d, q)>(a, b)
#define MFMA_O0(d, q, a, b) mfma_o0_<AO(d, q)>(a, b)
#define MFMA_S(acc, a, s, q) mfma_s_<AQ(s, q)>(acc, a)
#define MFMA_SC(acc, a, s, q, c) mfma_sc_<AQ(s, q)>(acc, a, c)
#define ARD(dst, n) dst = ard_<n>()
#define AWR(n, src) awr_<n>(src)
#define ASCALE(n, f) ascale_<n>(f)
#define SUBIP(x, d) x = subip_(x, d)
#define NOPS(n) asm volatile("s_nop " #n ::: "memory")
#define SBAR() __builtin_amdgcn_sched_barrier(0)
#define PINF(x) x = pin_(x)
#define PINU(x) x = pin_(x)
#define WAITV_BAR(N) asm volatile("s_waitcnt vmcnt(" #N ") lgkmcnt(0)\n\ts_barrier" ::: "memory")

DI unsigned cvtpk(float lo, float hi) {
  typedef float f32x2 __attribute__((ext_vector_type(2)));
  typedef __bf16 bf16x2 __attribute__((ext_vector_type(2)));
  f32x2 v = {lo, hi}; bf16x2 b = __builtin_convertvector(v, bf16x2); return __builtin_bit_cast(unsigned, b);
}
DI void glds16(unsigned voff, const char* sbase, unsigned ldsaddr) {
  asm volatile("s_mov_b32 m0, %2\n\ts_nop 0\n\tglobal_load_lds_dwordx4 %0, %1" :: "v"(voff), "s"(sbase), "s"(ldsaddr) : "memory");
}
DI void glds16nt(unsigned voff, const char* sbase, unsigned ldsaddr) {
  asm volatile("s_mov_b32 m0, %2\n\ts_nop 0\n\tglobal_load_lds_dwordx4 %0, %1 nt" :: "v"(voff), "s"(sbase), "s"(ldsaddr) : "memory");
}
DI bf16x8 ldsb128(unsigned p) { return *(const __attribute__((address_space(3))) bf16x8*)p; }
DI s16x4 ldstr(unsigned p) { return __builtin_bit_cast(s16x4, __builtin_amdgcn_ds_read_tr16_b64_v4i16((__attribute__((address_space(3))) v4i16_t*)p)); }
template <int X> DI unsigned adr(unsigned base, unsigned off) {
  unsigned r;
  if constexpr (X == 0) asm volatile("v_add_u32 %0, %2, %1" : "=v"(r) : "v"(base), "s"(off));
  else asm volatile("v_xor_b32 %0, %3, %1\n\tv_add_u32 %0, %2, %0" : "=&v"(r) : "v"(base), "s"(off), "i"(X));
  return r;
}
DI unsigned opq(unsigned x) { asm volatile("" : "+v"(x)); return x; }
DI bf16x8 cat8(s16x4 lo, s16x4 hi) { return __builtin_shufflevector(lo, hi, 0, 1, 2, 3, 4, 5, 6, 7); }
DI float mx3(float a, float b, float c) { float r; asm("v_max3_f32 %0, %1, %2, %3" : "=v"(r) : "v"(a), "v"(b), "v"(c)); return r; }
DI float mx2(float a, float b) { float r; asm("v_max_f32_e32 %0, %1, %2" : "=v"(r) : "v"(a), "v"(b)); return r; }
DI float xmax(float v) {
  auto a = __builtin_amdgcn_permlane32_swap(__float_as_uint(v), __float_as_uint(v), false, false);
  v = mx2(__uint_as_float(a[0]), __uint_as_float(a[1]));
  auto b = __builtin_amdgcn_permlane16_swap(__float_as_uint(v), __float_as_uint(v), false, false);
  return mx2(__uint_as_float(b[0]), __uint_as_float(b[1]));
}
DI float xsum(float v) {
  auto a = __builtin_amdgcn_permlane32_swap(__float_as_uint(v), __float_as_uint(v), false, false);
  v = __uint_as_float(a[0]) + __uint_as_float(a[1]);
  auto b = __builtin_amdgcn_permlane16_swap(__float_as_uint(v), __float_as_uint(v), false, false);
  return __uint_as_float(b[0]) + __uint_as_float(b[1]);
}

template <int N> struct IC { static constexpr int value = N; };
#define AI __attribute__((always_inline))
template <class F, int... Is> DI void sfor_(F&& f, std::integer_sequence<int, Is...>) { (f(IC<Is>{}), ...); }
template <int N, class F> DI void sfor(F&& f) { sfor_(f, std::make_integer_sequence<int, N>{}); }
struct Ptrs { const char* x[2]; const char* k[2]; const char* v[2]; const char* wq[2]; const char* wo[2]; };

__global__ __launch_bounds__(256, 1) void k_attn(Ptrs P, char* __restrict__ ofr) {
  extern __shared__ __attribute__((aligned(16))) char smem[];
  lds_ptr lds = (lds_ptr)smem;
  const int tid = threadIdx.x, l = tid & 63, w = __builtin_amdgcn_readfirstlane(tid >> 6);
  const int i16 = l & 15, g = l >> 4, qq = i16 >> 2, pp = i16 & 3;
  const int xcd = blockIdx.x & 7, idx = blockIdx.x >> 3;
  const int br = xcd >> 2, h = 8 * (xcd & 3) + (idx & 7), b0 = idx >> 3;
  const char* xg = P.x[br] + (size_t)b0 * SQ * DM * 2;
  const char* wg = P.wq[br] + (size_t)h * DH * 2;
  const char* kg = P.k[br] + (size_t)(b0 * NH + h) * SK * DH * 2;
  const char* vg = P.v[br] + (size_t)(b0 * NH + h) * SK * DH * 2;
  char* og = ofr + (((size_t)(br * NB + b0) * 192 + h * 6) * 12 + 3 * w) * 1024 + l * 16;

  if (blockIdx.x == 0 && tid < 64) ((unsigned*)(ofr + ((size_t)48 << 20)))[tid * 16] = 0u;
  unsigned offK[3], offV[3], offW[3], offX;
#pragma unroll
  for (int u = 0; u < 3; ++u) {
    const int o = 1024 * u + 16 * l, r = o / 384, chp = (o % 384) >> 4, row = 8 * w + r;
    const int aK = (row >> 1) & 7;
    offK[u] = row * 384 + (((chp & ~7) | ((chp ^ aK) & 7)) << 4);
    offV[u] = row * 384 + ((chp ^ (((row >> 1) & 3) << 1)) << 4);
    const int sW = ((row >> 1) & 1) | (((row >> 3) & 1) << 1);
    offW[u] = row * (HD * 2) + ((chp ^ (sW << 1)) << 4);
  }
  { const int row = 8 * w + (l >> 3), a = (row >> 1) & 7; offX = row * (DM * 2) + (((l & 7) ^ a) << 4); }

  const unsigned oK0 = offK[0], oK1 = offK[1], oK2 = offK[2], oV0 = offV[0], oV1 = offV[1], oV2 = offV[2], oW0 = offW[0], oW1 = offW[1], oW2 = offW[2];
  const unsigned lds0 = (unsigned)(uintptr_t)lds;
  auto px = [&](int c, int slot, auto N) AI {
    constexpr int n = N.value;
    glds16(offX, xg + c * 128 + (size_t)n * 32 * DM * 2, lds0 + slot * SLOT + w * 1024 + n * 4096);
  };
  auto pw_ = [&](int c, int slot, auto N) AI {
    constexpr int n = N.value, u = n % 3, hf = n / 3;
    glds16(u == 0 ? oW0 : u == 1 ? oW1 : oW2, wg + (size_t)(c * 64 + hf * 32) * HD * 2, lds0 + slot * SLOT + w * 3072 + hf * 12288 + u * 1024);
  };
  auto pk = [&](int j, int slot, auto N) AI {
    constexpr int n = N.value, u = n % 3, hf = n / 3;
    glds16nt(u == 0 ? oK0 : u == 1 ? oK1 : oK2, kg + (size_t)j * SLOT + hf * 12288, lds0 + slot * SLOT + w * 3072 + hf * 12288 + u * 1024);
  };
  auto pv = [&](int j, int slot, auto N) AI {
    constexpr int n = N.value, u = n % 3, hf = n / 3;
    glds16nt(u == 0 ? oV0 : u == 1 ? oV1 : oV2, vg + (size_t)j * SLOT + hf * 12288, lds0 + slot * SLOT + w * 3072 + hf * 12288 + u * 1024);
  };
  auto pf_ = [&](auto F, auto N) AI {
    constexpr int f = F.value;
    if constexpr (f >= 36) { if constexpr ((f & 1) == 0) px((f - 36) >> 1, f % NSLOT, N); else pw_((f - 36) >> 1, f % NSLOT, N); }
    else if constexpr ((f & 1) == 0) pk((f - 20) >> 1, f % NSLOT, N); else pv((f - 20) >> 1, f % NSLOT, N);
  };

  const int a8 = i16 >> 1;
  const unsigned kb0 = lds0 + i16 * 384 + ((g ^ a8) << 4);
  const unsigned xb0 = lds0 + (48 * w + i16) * 128 + ((g ^ a8) << 4);
  const int swz = 2 * (g & 1) + (qq >> 1);
  const unsigned vb0 = lds0 + (4 * g + qq) * 384 + (swz << 5) + 8 * pp;
  const unsigned wb0 = lds0 + (8 * g + qq) * 384 + (swz << 5) + 8 * pp;

  asm volatile("" ::: ACLOB);
  f32x4 qa[12][3];
  float inv = 0.f; f32x4 eX, eY;
  auto epi = [&](auto U, auto Pt) AI {
    constexpr int u = U.value, q = u / 6, t = u % 6, part = Pt.value;
    if constexpr (part == 0) {
      if constexpr (t == 0) { float lq; ARD(lq, AO(12, q)); inv = __builtin_amdgcn_rcpf(lq); }
      sfor<4>([&](auto R) AI { ARD(eX[R.value], AO(2 * t, q) + R.value); });
    } else if constexpr (part == 1) {
      sfor<4>([&](auto R) AI { ARD(eY[R.value], AO(2 * t + 1, q) + R.value); });
    } else if constexpr (part == 2) { eX *= inv; eY *= inv; PINF(eX); PINF(eY); }
    else if constexpr (part == 3) {
      u32x4 f = {cvtpk(eX[0], eX[1]), cvtpk(eX[2], eX[3]), cvtpk(eY[0], eY[1]), cvtpk(eY[2], eY[3])};
      *(u32x4*)(og + q * 1024 + t * 12288) = f;
    }
  };
  int sl = 0;
  auto p1step = [&](int c, auto Tc, auto Ec) AI {
    constexpr int TAIL = Tc.value, EPI = Ec.value;
    WAITV_BAR(12);
    const int sn = sl >= 2 ? sl - 2 : sl + 4;
    unsigned ax[2], aw[4];
    ax[0] = adr<0>(xb0, sl * SLOT); ax[1] = adr<64>(xb0, sl * SLOT);
    aw[0] = adr<0>(wb0, (sl + 1) * SLOT); aw[1] = adr<32>(wb0, (sl + 1) * SLOT); aw[2] = adr<64>(wb0, (sl + 1) * SLOT); aw[3] = adr<96>(wb0, (sl + 1) * SLOT);
    sl = sl == 4 ? 0 : sl + 2;
    bf16x8 xf[2][3], wf[3];
    auto rdw = [&](auto F) AI {
      constexpr int f = F.value, s = f / 12, d = f % 12;
      const unsigned p = aw[d & 3] + s * 32 * 384 + (d >> 2) * 128;
      wf[f % 3] = cat8(ldstr(p), ldstr(p + 4 * 384));
    };
#pragma unroll
    for (int q = 0; q < 3; ++q) xf[0][q] = ldsb128(ax[0] + q * 2048);
    rdw(IC<0>{}); rdw(IC<1>{});
    SBAR();
    sfor<24>([&](auto F) AI {
      constexpr int f = F.value, s = f / 12, d = f % 12;
      if constexpr (f + 2 < 24) rdw(IC<f + 2>{});
      if constexpr (f >= 2 && f < 5) xf[1][f - 2] = ldsb128(ax[1] + (f - 2) * 2048);
      mfma_ip_(qa[d][0], wf[f % 3], xf[s][0]);
      if constexpr (EPI >= 0 && EPI < 9) { constexpr int k = f / 12, part = (f % 12) / 2 - 1;
        if constexpr ((f & 1) == 1 && part >= 0 && part < 4) { epi(IC<2 * EPI + k>{}, IC<part>{}); SBAR(); } }
      mfma_ip_(qa[d][1], wf[f % 3], xf[s][1]);
      if constexpr ((f & 1) == 0) {
        constexpr int n = f >> 1;
        if constexpr (n < 6) { if constexpr (!TAIL) px(c + 2, sn, IC<n>{}); else pk(c - 8, sn, IC<n>{}); }
        else { if constexpr (!TAIL) pw_(c + 2, sn + 1, IC<n - 6>{}); else pv(c - 8, sn + 1, IC<n - 6>{}); }
      }
      mfma_ip_(qa[d][2], wf[f % 3], xf[s][2]);
      SBAR();
    });
  };
  auto zero_qa = [&]() AI {
    float z = 0.f; PINF(z);
#pragma unroll
    for (int d = 0; d < 12; ++d) {
#pragma unroll
      for (int q = 0; q < 3; ++q) qa[d][q] = f32x4{z, z, z, z};
      pin3n_(qa[d][0], qa[d][1], qa[d][2]);
    }
  };

  f32x4 sc[2][4][3];
  f32x4 negm[3];
  unsigned pw[2][3][4];
  bf16x8 ones = {0x3F80, 0x3F80, 0x3F80, 0x3F80, 0x3F80, 0x3F80, 0x3F80, 0x3F80};
  asm volatile("" : "+v"(ones));

#define EXP1(X, kb, q, r) do { sc[X][kb][q][r] = __builtin_amdgcn_exp2f(sc[X][kb][q][r]); PINF(sc[X][kb][q][r]); } while (0)
#define CVT1(X, c) do { constexpr int kb_ = (c) / 6, q_ = ((c) % 6) >> 1, hf_ = (c) & 1; \
    pw[kb_ >> 1][q_][2 * (kb_ & 1) + hf_] = cvtpk(sc[X][kb_][q_][2 * hf_], sc[X][kb_][q_][2 * hf_ + 1]); PINU(pw[kb_ >> 1][q_][2 * (kb_ & 1) + hf_]); } while (0)

  auto conv = [&](auto S, auto Q) AI {
    constexpr int s = S.value, q = Q.value;
    const f32x4 X = qa[2 * s][q] * QSCALE, Y = qa[2 * s + 1][q] * QSCALE;
    unsigned x0 = cvtpk(X[0], X[1]), x1 = cvtpk(X[2], X[3]), y0 = cvtpk(Y[0], Y[1]), y1 = cvtpk(Y[2], Y[3]);
    auto r0 = __builtin_amdgcn_permlane32_swap(x0, y0, false, false);
    auto r1 = __builtin_amdgcn_permlane32_swap(x1, y1, false, false);
    auto t0 = __builtin_amdgcn_permlane16_swap(r0[0], r0[1], false, false);
    auto t1 = __builtin_amdgcn_permlane16_swap(r1[0], r1[1], false, false);
    AWR(AQ(s, q) + 0, t0[0]); AWR(AQ(s, q) + 1, t1[0]); AWR(AQ(s, q) + 2, t0[1]); AWR(AQ(s, q) + 3, t1[1]);
  };

  sfor<6>([&](auto N) AI { px(0, 0, N); }); sfor<6>([&](auto N) AI { pw_(0, 1, N); });
  sfor<6>([&](auto N) AI { px(1, 2, N); }); sfor<6>([&](auto N) AI { pw_(1, 3, N); });
  zero_qa();
#pragma unroll 1
  for (int c = 0; c < 8; ++c) p1step(c, IC<0>{}, IC<-1>{});
  p1step(8, IC<1>{}, IC<-1>{}); p1step(9, IC<1>{}, IC<-1>{});

#pragma unroll 1
  for (int it = 0;; ++it) {
    asm volatile("" : "+s"(wg));
    if (it == 0) xg += (size_t)4 * SQ * DM * 2;
    WAITV_BAR(18);
    {
      unsigned ak[2];
      ak[0] = adr<0>(kb0, 2 * SLOT); ak[1] = adr<64>(kb0, 2 * SLOT);
      bf16x8 kf[3];
      auto rdk = [&](auto F) AI { constexpr int f = F.value, s = f >> 2, kb = f & 3; kf[f % 3] = ldsb128(ak[s & 1] + kb * 6144 + (s >> 1) * 128); };
      NOPS(7);
      SBAR();
      conv(IC<0>{}, IC<0>{}); conv(IC<0>{}, IC<1>{}); conv(IC<0>{}, IC<2>{});
      conv(IC<1>{}, IC<0>{}); conv(IC<1>{}, IC<1>{}); conv(IC<1>{}, IC<2>{});
      SBAR();
      rdk(IC<0>{}); rdk(IC<1>{});
      NOPS(1);
      SBAR();
      sfor<24>([&](auto F) AI {
        constexpr int f = F.value, s = f >> 2, kb = f & 3;
        if constexpr (f + 2 < 24) rdk(IC<f + 2>{});
        sfor<3>([&](auto Q) AI {
          constexpr int q = Q.value;
          if constexpr (s == 0) { mfma_s0_<AQ(s, q)>(sc[0][kb][q], kf[f % 3]); } else { MFMA_S(sc[0][kb][q], kf[f % 3], s, q); }
        });
        if constexpr (s < 4 && kb < 3) conv(IC<s + 2>{}, IC<kb>{});
        if constexpr (kb == 3) { pf_(IC<24 + s / 3>{}, IC<2 * (s % 3)>{}); pf_(IC<24 + s / 3>{}, IC<2 * (s % 3) + 1>{}); }
        SBAR();
      });
      asm volatile("s_nop 7" : "+v"(sc[0][0][0]), "+v"(sc[0][0][1]), "+v"(sc[0][0][2]), "+v"(sc[0][1][0]), "+v"(sc[0][1][1]), "+v"(sc[0][1][2]),
                               "+v"(sc[0][2][0]), "+v"(sc[0][2][1]), "+v"(sc[0][2][2]), "+v"(sc[0][3][0]), "+v"(sc[0][3][1]), "+v"(sc[0][3][2]));
#pragma unroll
      for (int q = 0; q < 3; ++q) {
        float m0 = mx3(sc[0][0][q][0], sc[0][0][q][1], sc[0][0][q][2]);
        m0 = mx2(m0, sc[0][0][q][3]);
#pragma unroll
        for (int kb = 1; kb < 4; ++kb) { m0 = mx3(m0, sc[0][kb][q][0], sc[0][kb][q][1]); m0 = mx3(m0, sc[0][kb][q][2], sc[0][kb][q][3]); }
        m0 = xmax(m0);
        negm[q] = f32x4{-m0, -m0, -m0, -m0};
#pragma unroll
        for (int kb = 0; kb < 4; ++kb) sc[0][kb][q] -= m0;
#pragma unroll
        for (int r = 0; r < 4; ++r) sc[0][0][q][r] = __builtin_amdgcn_exp2f(sc[0][0][q][r]);
      }
    }
    sfor<8>([&](auto Jc) AI {
      constexpr int j = Jc.value;
      constexpr int X = j & 1, Y = X ^ 1;
      if constexpr (j == 0 || j == 7) { WAITV_BAR(18); } else { WAITV_BAR(12); }
      if constexpr (j < 7) {
        unsigned ak[2];
        ak[0] = adr<0>(kb0, ((22 + 2 * j) % NSLOT) * SLOT); ak[1] = adr<64>(kb0, ((22 + 2 * j) % NSLOT) * SLOT);
        pin3n_(negm[0], negm[1], negm[2]);
        bf16x8 kf[3];
        auto rdk = [&](auto F) AI { constexpr int f = F.value, s = f >> 2, kb = f & 3; kf[f % 3] = ldsb128(ak[s & 1] + kb * 6144 + (s >> 1) * 128); };
        rdk(IC<0>{}); rdk(IC<1>{});
        SBAR();
        sfor<24>([&](auto F) AI {
          constexpr int f = F.value, s = f >> 2, kb = f & 3;
          if constexpr (f + 2 < 24) rdk(IC<f + 2>{});
          sfor<3>([&](auto Q) AI {
            constexpr int q = Q.value;
            if constexpr (s == 0) { MFMA_SC(sc[Y][kb][q], kf[f % 3], s, q, negm[q]); } else { MFMA_S(sc[Y][kb][q], kf[f % 3], s, q); }
            if constexpr (q == 0) { CVT1(X, f); }
            else {
              constexpr int e = 2 * f + q - 1;
              if constexpr (e < 36) { EXP1(X, 1 + e / 12, (e % 12) >> 2, e & 3); }
              else if constexpr (q == 1 && j >= 1) pf_(IC<25 + 2 * j>{}, IC<f - 18>{});
            }
            SBAR();
          });
        });
      } else {
        sfor<36>([&](auto E) AI { constexpr int e = E.value; EXP1(X, 1 + e / 12, (e % 12) >> 2, e & 3);
          if constexpr ((e % 6) == 5) pf_(IC<39>{}, IC<e / 6>{}); });
        sfor<24>([&](auto C) AI { CVT1(X, C.value); });
      }
      {
        bf16x8 pf[2][3];
#pragma unroll
        for (int t = 0; t < 2; ++t)
#pragma unroll
          for (int q = 0; q < 3; ++q) { u32x4 f = {pw[t][q][0], pw[t][q][1], pw[t][q][2], pw[t][q][3]}; pf[t][q] = __builtin_bit_cast(bf16x8, f); }
        pin3n_(pf[0][0], pf[0][1], pf[0][2]); pin3n_(pf[1][0], pf[1][1], pf[1][2]);
        unsigned av[4];
        { constexpr unsigned so = ((21 + 2 * j) % NSLOT) * SLOT;
          av[0] = adr<0>(vb0, so); av[1] = adr<32>(vb0, so); av[2] = adr<64>(vb0, so); av[3] = adr<96>(vb0, so); }
        bf16x8 vf[3];
        auto rdv = [&](auto F) AI {
          constexpr int f = F.value, t = f / 12, d = f % 12;
          const unsigned p = av[d & 3] + t * 32 * 384 + (d >> 2) * 128;
          vf[f % 3] = cat8(ldstr(p), ldstr(p + 16 * 384));
        };
        float mx[3], fct[3];
        bool pend = false;
        auto mop = [&](auto I) AI {
          constexpr int q = I.value % 3, k = I.value / 3;
          if constexpr (k == 0) mx[q] = mx3(sc[Y][0][q][0], sc[Y][0][q][1], sc[Y][0][q][2]);
          else if constexpr (k == 7) mx[q] = mx2(mx[q], sc[Y][3][q][3]);
          else { constexpr int e = 2 * k + 1; mx[q] = mx3(mx[q], sc[Y][e >> 2][q][e & 3], sc[Y][(e + 1) >> 2][q][(e + 1) & 3]); }
          PINF(mx[q]);
        };
        rdv(IC<0>{}); rdv(IC<1>{});
        SBAR();
        sfor<78>([&](auto Mc) AI {
          constexpr int m = Mc.value, G = m / 3, q = m % 3, t = G / 13, gi = G % 13;
          if constexpr (gi == 0) {
            if constexpr (j == 0 && t == 0) { MFMA_O0(12, q, ones, pf[t][q]); } else { MFMA_O(12, q, ones, pf[t][q]); }
          } else {
            constexpr int d = gi - 1, f = 12 * t + d;
            if constexpr (q == 0 && f + 2 < 24) rdv(IC<f + 2>{});
            if constexpr (j == 0 && t == 0) { MFMA_O0(d, q, vf[f % 3], pf[t][q]); } else { MFMA_O(d, q, vf[f % 3], pf[t][q]); }
          }
          if constexpr (j < 7 && q < 2) {
            if constexpr (G >= 2 && G < 8) { constexpr int i = 4 * (G - 2) + 2 * q; mop(IC<i>{}); mop(IC<i + 1>{}); }
            if constexpr (G == 8 && q == 0) {
              pend = __any(mx3(mx[0], mx[1], mx[2]) > THR);
              if (__builtin_expect(pend, 0)) {
#pragma unroll
                for (int qq_ = 0; qq_ < 3; ++qq_) {
                  const float gr = xmax(mx[qq_]);
                  const unsigned db = (__float_as_uint(fmaxf(gr, 0.f)) + 0xFFFFu) & 0xFFFF0000u;
                  fct[qq_] = __builtin_amdgcn_exp2f(-__uint_as_float(db));
                  u32x4 bw = {g == 0 ? ((db >> 16) | 0x8000u) : 0u, 0u, 0u, 0u};
                  bf16x8 bfr = __builtin_bit_cast(bf16x8, bw);
                  asm volatile("s_nop 1" : "+v"(bfr));
                  mfma_ip_(negm[qq_], ones, bfr);
                  mfma_ip_(sc[Y][0][qq_], ones, bfr); mfma_ip_(sc[Y][1][qq_], ones, bfr); mfma_ip_(sc[Y][2][qq_], ones, bfr); mfma_ip_(sc[Y][3][qq_], ones, bfr);
                }
                NOPS(7);
              }
            }
            if constexpr (G >= 9 && G < 15) { constexpr int e = 2 * (G - 9) + q; EXP1(Y, 0, e >> 2, e & 3); }
          }
          if constexpr (j < 7 && q == 1 && G >= 16 && G < 22) pf_(IC<26 + 2 * j>{}, IC<G - 16>{});
          SBAR();
        });
        if constexpr (j < 7) {
          if (__builtin_expect(pend, 0)) {
            NOPS(7);
            sfor<156>([&](auto N) AI { ASCALE(N.value, fct[(N.value >> 2) % 3]); });
            NOPS(1);
          }
        }
      }
    });
    if (it == 1) break;
    kg += (size_t)4 * NH * SK * DH * 2; vg += (size_t)4 * NH * SK * DH * 2;
    zero_qa();
    sl = 0;
    sfor<10>([&](auto C) AI { p1step(C.value, IC<(C.value >= 8)>{}, IC<C.value>{}); });
    og += (size_t)4 * 192 * 12 * 1024;
  }
  NOPS(7);
  sfor<18>([&](auto U) AI { epi(U, IC<0>{}); epi(U, IC<1>{}); epi(U, IC<2>{}); epi(U, IC<3>{}); });
  asm volatile("s_waitcnt vmcnt(0)" ::: "memory");
}

constexpr int ST2 = 128 * 320;
constexpr int LDS2 = 3 * ST2;
constexpr size_t WS_SLAB = (size_t)64 << 20;
constexpr size_t WS_CNT = (size_t)48 << 20;

DI void gload16(bf16x8& d, unsigned voff, const char* sbase) { asm volatile("global_load_dwordx4 %0, %1, %2" : "=v"(d) : "v"(voff), "s"(sbase) : "memory"); }
template <int N> DI void wait3(bf16x8& a, bf16x8& b, bf16x8& c) { asm volatile("s_waitcnt vmcnt(%3)" : "+v"(a), "+v"(b), "+v"(c) : "i"(N) : "memory"); }

__global__ __launch_bounds__(512, 2) void k_oproj(Ptrs P, char* __restrict__ ws, char* __restrict__ out) {
  extern __shared__ __attribute__((aligned(16))) char smem[];
  lds_ptr lds = (lds_ptr)smem;
  const int tid = threadIdx.x, l = tid & 63, w = __builtin_amdgcn_readfirstlane(tid >> 6);
  const int i16 = l & 15, g = l >> 4, qq = i16 >> 2, pp = i16 & 3;
  const int xcd = blockIdx.x & 7, idx = blockIdx.x >> 3;
  const int br = xcd >> 2, ks = xcd & 3, b = idx >> 2, nq = idx & 3;
  const int kh = w >> 2, qg = w & 3;
  const unsigned lds0 = (unsigned)(uintptr_t)lds;
  const char* wsrc = P.wo[br] + (size_t)ks * 1536 * DM * 2 + nq * 320;
  const char* osrc = ws + (((size_t)(br * NB + b) * 192 + 48 * ks + kh) * 12 + 3 * qg) * 1024;
  const unsigned l16 = l * 16;
  unsigned offW[5];
#pragma unroll
  for (int u = 0; u < 5; ++u) {
    const int o = 1024 * u + 16 * l, r = o / 320, chp = (o % 320) >> 4;
    offW[u] = (16 * w + r) * (DM * 2) + ((chp ^ (((r >> 2) & 1) << 1)) << 4);
  }
  const unsigned o0 = offW[0], o1 = offW[1], o2 = offW[2], o3 = offW[3], o4 = offW[4];
  const int s0 = 0;
  auto wpiece = [&](const char* src, unsigned d, auto Uc) AI {
    constexpr int u = Uc.value;
    glds16(u == 0 ? o0 : u == 1 ? o1 : u == 2 ? o2 : u == 3 ? o3 : o4, src, d + u * 1024);
  };
  auto wsrc_of = [&](int s) AI { s += s0; s = s >= 12 ? s - 12 : s; return wsrc + (size_t)s * 128 * DM * 2; };
  auto osrc_of = [&](int u) AI { u += 2 * s0; u = u >= 24 ? u - 24 : u; return osrc + (size_t)u * 24576; };
  auto issueW = [&](int s, int stg) AI {
    const unsigned d = lds0 + stg * ST2 + w * 5120;
    const char* src = wsrc_of(s);
    sfor<5>([&](auto U) AI { wpiece(src, d, U); });
  };
  bf16x8 of[4][3];
  auto loadO = [&](int u, auto Bc) AI {
    constexpr int B_ = Bc.value;
    const char* src = osrc_of(u);
    gload16(of[B_][0], l16, src); gload16(of[B_][1], l16, src + 1024); gload16(of[B_][2], l16, src + 2048);
  };
  f32x4 acc[10][3];
#pragma unroll
  for (int n = 0; n < 10; ++n)
#pragma unroll
    for (int q = 0; q < 3; ++q) acc[n][q] = f32x4{0.f, 0.f, 0.f, 0.f};
  const unsigned wb0 = lds0 + (32 * kh + 4 * g + qq) * 320 + 8 * pp + ((g & 1) << 5);
  issueW(0, 0); loadO(0, IC<0>{}); loadO(1, IC<1>{}); loadO(2, IC<2>{});
  issueW(1, 1);
  int stg = 0;
  auto kstep = [&](unsigned ae, unsigned ao, auto Vc, auto Bc, auto Lc, const char* lsrc, const char* dsrc, unsigned dd, auto Dc) AI {
    constexpr int v = Vc.value, B_ = Bc.value, LB = Lc.value, DN = Dc.value;
    bf16x8 wf[3];
    auto rdw = [&](auto N) AI { constexpr int n = N.value; const unsigned p = ((n & 1) ? ao : ae) + v * 64 * 320 + (n >> 1) * 64; wf[n % 3] = cat8(ldstr(p), ldstr(p + 16 * 320)); };
    rdw(IC<0>{}); rdw(IC<1>{});
    SBAR();
    sfor<10>([&](auto N) AI {
      constexpr int n = N.value;
      if constexpr (n + 2 < 10) rdw(IC<n + 2>{});
      acc[n][0] = MFMA16(wf[n % 3], of[B_][0], acc[n][0]);
      if constexpr (LB >= 0 && n < 3) { SBAR(); gload16(of[LB][n], l16, lsrc + n * 1024); SBAR(); }
      if constexpr (DN > 0 && n >= 4 && n < 4 + 2 * DN && (n & 1) == 0) { SBAR(); wpiece(dsrc, dd, IC<(v ? 2 : 0) + (n - 4) / 2>{}); SBAR(); }
      acc[n][1] = MFMA16(wf[n % 3], of[B_][1], acc[n][1]);
      acc[n][2] = MFMA16(wf[n % 3], of[B_][2], acc[n][2]);
      SBAR();
    });
  };
  auto stage = [&](int s, auto Pc, auto Mc) AI {
    constexpr int PAR = Pc.value, MODE = Mc.value, B0 = 2 * PAR, B1 = 2 * PAR + 1;
    if constexpr (MODE == 2) { asm volatile("s_waitcnt vmcnt(3) lgkmcnt(0)\n\ts_barrier" ::: "memory"); }
    else { asm volatile("s_waitcnt vmcnt(11) lgkmcnt(0)\n\ts_barrier" ::: "memory"); }
    wait3<63>(of[B0][0], of[B0][1], of[B0][2]);
    const unsigned ae = opq(wb0 + stg * ST2), ao = opq((wb0 ^ 32) + stg * ST2);
    const unsigned dd = lds0 + (stg == 0 ? 2 : stg - 1) * ST2 + w * 5120;
    const char* dsrc = wsrc_of(s + 2);
    kstep(ae, ao, IC<0>{}, IC<B0>{}, IC<(MODE <= 1 ? ((B0 + 3) & 3) : -1)>{}, osrc_of(2 * s + 3), dsrc, dd, IC<(MODE == 0 ? 2 : 0)>{});
    wait3<MODE == 0 ? 13 : MODE == 1 ? 11 : 0>(of[B1][0], of[B1][1], of[B1][2]);
    kstep(ae, ao, IC<1>{}, IC<B1>{}, IC<(MODE == 0 ? B0 : -1)>{}, osrc_of(2 * s + 4), dsrc, dd, IC<(MODE == 0 ? 3 : 0)>{});
    stg = stg == 2 ? 0 : stg + 1;
  };
#pragma unroll 1
  for (int s = 0; s < 10; s += 2) { stage(s, IC<0>{}, IC<0>{}); stage(s + 1, IC<1>{}, IC<0>{}); }
  stage(10, IC<0>{}, IC<1>{}); stage(11, IC<1>{}, IC<2>{});
  asm volatile("s_waitcnt vmcnt(0) lgkmcnt(0)\n\ts_barrier" ::: "memory");
  {
    const unsigned xo = lds0 + ((kh ^ 1) * 4 + qg) * 15360 + l16, xi = lds0 + (kh * 4 + qg) * 15360 + l16;
#pragma unroll
    for (int n = 0; n < 5; ++n)
#pragma unroll
      for (int q = 0; q < 3; ++q) *(__attribute__((address_space(3))) f32x4*)(xo + (n * 3 + q) * 1024) = kh ? acc[n][q] : acc[5 + n][q];
    __syncthreads();
#pragma unroll
    for (int n = 0; n < 5; ++n)
#pragma unroll
      for (int q = 0; q < 3; ++q) {
        const f32x4 o = *(const __attribute__((address_space(3))) f32x4*)(xi + (n * 3 + q) * 1024);
        acc[n][q] = (kh ? acc[5 + n][q] : acc[n][q]) + o;
      }
  }
  const int tile = (br * NB + b) * 4 + nq;
  char* slab = ws + WS_SLAB + (size_t)tile * 4 * 8 * 15360;
  {
    char* mine = slab + (size_t)(ks * 8 + w) * 15360 + l16;
#pragma unroll
    for (int i = 0; i < 15; ++i)
      if ((i & 3) != ks) asm volatile("global_store_dwordx4 %0, %1, off sc0 sc1\n\ts_nop 1" :: "v"(mine + i * 1024), "v"(acc[i / 3][i % 3]) : "memory");
    asm volatile("s_waitcnt vmcnt(0)" ::: "memory");
    __syncthreads();
    unsigned* cnt = (unsigned*)(ws + WS_CNT) + tile * 16;
    if (tid == 0) {
      __hip_atomic_fetch_add(cnt, 1u, __ATOMIC_RELAXED, __HIP_MEMORY_SCOPE_AGENT);
      unsigned spins = 0;
      while (__hip_atomic_load(cnt, __ATOMIC_RELAXED, __HIP_MEMORY_SCOPE_AGENT) < 4u && ++spins < (1u << 24)) __builtin_amdgcn_s_sleep(1);
    }
    __syncthreads();
  }
  {
    const char* theirs = slab + (size_t)w * 15360 + l16;
    f32x4 p[4][3];
#pragma unroll
    for (int m = 0; m < 4; ++m) {
      const int i = (4 * m + ks) < 15 ? 4 * m + ks : 11;
#pragma unroll
      for (int r = 0; r < 3; ++r) {
        const int src = (ks + 1 + r) & 3;
        asm volatile("global_load_dwordx4 %0, %1, off sc0 sc1" : "=v"(p[m][r]) : "v"(theirs + (size_t)src * 8 * 15360 + i * 1024) : "memory");
      }
    }
    asm volatile("s_waitcnt vmcnt(0)" : "+v"(p[0][0]), "+v"(p[0][1]), "+v"(p[0][2]), "+v"(p[1][0]), "+v"(p[1][1]), "+v"(p[1][2]),
                                        "+v"(p[2][0]), "+v"(p[2][1]), "+v"(p[2][2]), "+v"(p[3][0]), "+v"(p[3][1]), "+v"(p[3][2]) :: "memory");
#pragma unroll
    for (int i = 0; i < 15; ++i) {
      if ((i & 3) == ks) {
        const f32x4 o = (acc[i / 3][i % 3] + p[i >> 2][0]) + (p[i >> 2][1] + p[i >> 2][2]);
        u32x2 f = {cvtpk(o[0], o[1]), cvtpk(o[2], o[3])};
        char* dst = out + ((size_t)(b * SQ + (3 * qg + i % 3) * 16 + i16) * (2 * DM) + br * DM + nq * 160 + (5 * kh + i / 3) * 16 + 4 * g) * 2;
        *(u32x2*)dst = f;
      }
    }
  }
}

extern "C" void kernel_launch(void* const* d_in, const int* in_sizes, int n_in, void* d_out, int out_size, void* d_ws, size_t ws_size,
                              hipStream_t stream) {
  Ptrs P;
  for (int r = 0; r < 2; ++r) {
    P.x[r] = (const char*)d_in[5 * r + 0];
    P.k[r] = (const char*)d_in[5 * r + 1];
    P.v[r] = (const char*)d_in[5 * r + 2];
    P.wq[r] = (const char*)d_in[5 * r + 3];
    P.wo[r] = (const char*)d_in[5 * r + 4];
  }
  (void)hipFuncSetAttribute((const void*)k_attn, hipFuncAttributeMaxDynamicSharedMemorySize, LDS1);
  (void)hipFuncSetAttribute((const void*)k_oproj, hipFuncAttributeMaxDynamicSharedMemorySize, LDS2);
  hipLaunchKernelGGL(k_attn, dim3(256), dim3(256), LDS1, stream, P, (char*)d_ws);
  hipLaunchKernelGGL(k_oproj, dim3(256), dim3(512), LDS2, stream, P, (char*)d_ws, (char*)d_out);
}
```

```cpp
#include <hip/hip_runtime.h>
#include <hip/hip_bf16.h>
#include <stdint.h>
#include <utility>

using bf16x8 = __attribute__((ext_vector_type(8))) short;
using s16x4 = __attribute__((ext_vector_type(4))) short;
using f32x4 = __attribute__((ext_vector_type(4))) float;
using u32x4 = __attribute__((ext_vector_type(4))) unsigned;
using u32x2 = __attribute__((ext_vector_type(2))) unsigned;
typedef short v4i16_t __attribute__((ext_vector_type(4)));
typedef __attribute__((address_space(3))) char* lds_ptr;
typedef __attribute__((address_space(3))) const char* lds_cptr;

constexpr int NB = 8, SQ = 192, DM = 640, NH = 32, SK = 512, DH = 192;
constexpr int HD = NH * DH;
constexpr int SLOT = 24576;
constexpr int NSLOT = 6;
constexpr int LDS1 = SLOT * NSLOT;
constexpr float QSCALE = 0.07216878364870322f * 1.4426950408889634f;
constexpr float THR = 8.0f;

#define DI __device__ __forceinline__
#define MFMA16(a, b, c) __builtin_amdgcn_mfma_f32_16x16x32_bf16((a), (b), (c), 0, 0, 0)
#define ACLOB "a0","a1","a2","a3","a4","a5","a6","a7","a8","a9","a10","a11","a12","a13","a14","a15","a16","a17","a18","a19","a20","a21","a22","a23","a24","a25","a26","a27","a28","a29","a30","a31","a32","a33","a34","a35","a36","a37","a38","a39","a40","a41","a42","a43","a44","a45","a46","a47","a48","a49","a50","a51","a52","a53","a54","a55","a56","a57","a58","a59","a60","a61","a62","a63","a64","a65","a66","a67","a68","a69","a70","a71","a72","a73","a74","a75","a76","a77","a78","a79","a80","a81","a82","a83","a84","a85","a86","a87","a88","a89","a90","a91","a92","a93","a94","a95","a96","a97","a98","a99","a100","a101","a102","a103","a104","a105","a106","a107","a108","a109","a110","a111","a112","a113","a114","a115","a116","a117","a118","a119","a120","a121","a122","a123","a124","a125","a126","a127","a128","a129","a130","a131","a132","a133","a134","a135","a136","a137","a138","a139","a140","a141","a142","a143","a144","a145","a146","a147","a148","a149","a150","a151","a152","a153","a154","a155","a156","a157","a158","a159","a160","a161","a162","a163","a164","a165","a166","a167","a168","a169","a170","a171","a172","a173","a174","a175","a176","a177","a178","a179","a180","a181","a182","a183","a184","a185","a186","a187","a188","a189","a190","a191","a192","a193","a194","a195","a196","a197","a198","a199","a200","a201","a202","a203","a204","a205","a206","a207","a208","a209","a210","a211","a212","a213","a214","a215","a216","a217","a218","a219","a220","a221","a222","a223","a224","a225","a226","a227"
#define AO(d, q) (4 * (3 * (d) + (q)))
#define AQ(s, q) (156 + 4 * (3 * (s) + (q)))
template <int N> DI void mfma_o_(const bf16x8& a, const bf16x8& b) { asm volatile("v_mfma_f32_16x16x32_bf16 a[%c2:%c3], %0, %1, a[%c2:%c3]" :: "v"(a), "v"(b), "i"(N), "i"(N + 3) : ACLOB); }
template <int N> DI void mfma_o0_(const bf16x8& a, const bf16x8& b) { asm volatile("v_mfma_f32_16x16x32_bf16 a[%c2:%c3], %0, %1, 0" :: "v"(a), "v"(b), "i"(N), "i"(N + 3) : ACLOB); }
template <int N> DI void mfma_s_(f32x4& acc, const bf16x8& a) { asm volatile("v_mfma_f32_16x16x32_bf16 %0, %1, a[%c2:%c3], %0" : "+v"(acc) : "v"(a), "i"(N), "i"(N + 3) : ACLOB); }
template <int N> DI void mfma_sc_(f32x4& acc, const bf16x8& a, const f32x4& c) { asm volatile("v_mfma_f32_16x16x32_bf16 %0, %1, a[%c2:%c3], %4" : "=&v"(acc) : "v"(a), "i"(N), "i"(N + 3), "v"(c) : ACLOB); }
template <int N> DI void mfma_s0_(f32x4& acc, const bf16x8& a) { asm volatile("v_mfma_f32_16x16x32_bf16 %0, %1, a[%c2:%c3], 0" : "=&v"(acc) : "v"(a), "i"(N), "i"(N + 3) : ACLOB); }
template <int N> DI float ard_() { float r; asm volatile("v_accvgpr_read_b32 %0, a[%c1]" : "=v"(r) : "i"(N)); return r; }
template <int N, class T> DI void awr_(T src) { asm volatile("v_accvgpr_write_b32 a[%c1], %0" :: "v"(src), "i"(N)); }
template <int N> DI void ascale_(float f) { float t; asm volatile("v_accvgpr_read_b32 %0, a[%c1]\n\tv_mul_f32 %0, %2, %0\n\tv_accvgpr_write_b32 a[%c1], %0" : "=&v"(t) : "i"(N), "v"(f)); }
DI float subip_(float x, float d) { asm volatile("v_sub_f32 %0, %0, %1" : "+v"(x) : "v"(d)); return x; }
DI void mfma_ip_(f32x4& x, const bf16x8& a, const bf16x8& b) { asm volatile("v_mfma_f32_16x16x32_bf16 %0, %1, %2, %0" : "+v"(x) : "v"(a), "v"(b) : ACLOB); }
template <class T> DI T pin_(T x) { asm volatile("" : "+v"(x)); return x; }
template <class T> DI void pin3n_(T& a, T& b, T& c) { asm volatile("s_nop 1" : "+v"(a), "+v"(b), "+v"(c)); }
#define MFMA_O(d, q, a, b) mfma_o_<AO(d, q)>(a, b)
#define MFMA_O0(d, q, a, b) mfma_o0_<AO(d, q)>(a, b)
#define MFMA_S(acc, a, s, q) mfma_s_<AQ(s, q)>(acc, a)
#define MFMA_SC(acc, a, s, q, c) mfma_sc_<AQ(s, q)>(acc, a, c)
#define ARD(dst, n) dst = ard_<n>()
#define AWR(n, src) awr_<n>(src)
#define ASCALE(n, f) ascale_<n>(f)
#define SUBIP(x, d) x = subip_(x, d)
#define NOPS(n) asm volatile("s_nop " #n ::: "memory")
#define SBAR() __builtin_amdgcn_sched_barrier(0)
#define PINF(x) x = pin_(x)
#define PINU(x) x = pin_(x)
#define WAITV_BAR(N) asm volatile("s_waitcnt vmcnt(" #N ") lgkmcnt(0)\n\ts_barrier" ::: "memory")

DI unsigned cvtpk(float lo, float hi) {
  typedef float f32x2 __attribute__((ext_vector_type(2)));
  typedef __bf16 bf16x2 __attribute__((ext_vector_type(2)));
  f32x2 v = {lo, hi}; bf16x2 b = __builtin_convertvector(v, bf16x2); return __builtin_bit_cast(unsigned, b);
}
DI void glds16(unsigned voff, const char* sbase, unsigned ldsaddr) {
  asm volatile("s_mov_b32 m0, %2\n\ts_nop 0\n\tglobal_load_lds_dwordx4 %0, %1" :: "v"(voff), "s"(sbase), "s"(ldsaddr) : "memory");
}
DI void glds16nt(unsigned voff, const char* sbase, unsigned ldsaddr) {
  asm volatile("s_mov_b32 m0, %2\n\ts_nop 0\n\tglobal_load_lds_dwordx4 %0, %1 nt" :: "v"(voff), "s"(sbase), "s"(ldsaddr) : "memory");
}
DI bf16x8 ldsb128(unsigned p) { return *(const __attribute__((address_space(3))) bf16x8*)p; }
DI s16x4 ldstr(unsigned p) { return __builtin_bit_cast(s16x4, __builtin_amdgcn_ds_read_tr16_b64_v4i16((__attribute__((address_space(3))) v4i16_t*)p)); }
template <int X> DI unsigned adr(unsigned base, unsigned off) {
  unsigned r;
  if constexpr (X == 0) asm volatile("v_add_u32 %0, %2, %1" : "=v"(r) : "v"(base), "s"(off));
  else asm volatile("v_xor_b32 %0, %3, %1\n\tv_add_u32 %0, %2, %0" : "=&v"(r) : "v"(base), "s"(off), "i"(X));
  return r;
}
DI unsigned opq(unsigned x) { asm volatile("" : "+v"(x)); return x; }
DI bf16x8 cat8(s16x4 lo, s16x4 hi) { return __builtin_shufflevector(lo, hi, 0, 1, 2, 3, 4, 5, 6, 7); }
DI float mx3(float a, float b, float c) { float r; asm("v_max3_f32 %0, %1, %2, %3" : "=v"(r) : "v"(a), "v"(b), "v"(c)); return r; }
DI float mx3c(float a, float b, float c) { return __builtin_fmaxf(__builtin_fmaxf(a, b), c); }
DI float mx2(float a, float b) { float r; asm("v_max_f32_e32 %0, %1, %2" : "=v"(r) : "v"(a), "v"(b)); return r; }
DI float xmax(float v) {
  auto a = __builtin_amdgcn_permlane32_swap(__float_as_uint(v), __float_as_uint(v), false, false);
  v = mx2(__uint_as_float(a[0]), __uint_as_float(a[1]));
  auto b = __builtin_amdgcn_permlane16_swap(__float_as_uint(v), __float_as_uint(v), false, false);
  return mx2(__uint_as_float(b[0]), __uint_as_float(b[1]));
}
DI float xsum(float v) {
  auto a = __builtin_amdgcn_permlane32_swap(__float_as_uint(v), __float_as_uint(v), false, false);
  v = __uint_as_float(a[0]) + __uint_as_float(a[1]);
  auto b = __builtin_amdgcn_permlane16_swap(__float_as_uint(v), __float_as_uint(v), false, false);
  return __uint_as_float(b[0]) + __uint_as_float(b[1]);
}

template <int N> struct IC { static constexpr int value = N; };
#define AI __attribute__((always_inline))
template <class F, int... Is> DI void sfor_(F&& f, std::integer_sequence<int, Is...>) { (f(IC<Is>{}), ...); }
template <int N, class F> DI void sfor(F&& f) { sfor_(f, std::make_integer_sequence<int, N>{}); }
struct Ptrs { const char* x[2]; const char* k[2]; const char* v[2]; const char* wq[2]; const char* wo[2]; };

__global__ __launch_bounds__(256, 1) void k_attn(Ptrs P, char* __restrict__ ofr) {
  extern __shared__ __attribute__((aligned(16))) char smem[];
  lds_ptr lds = (lds_ptr)smem;
  const int tid = threadIdx.x, l = tid & 63, w = __builtin_amdgcn_readfirstlane(tid >> 6);
  const int i16 = l & 15, g = l >> 4, qq = i16 >> 2, pp = i16 & 3;
  const int xcd = blockIdx.x & 7, idx = blockIdx.x >> 3;
  const int br = xcd >> 2, h = 8 * (xcd & 3) + (idx & 7), b0 = idx >> 3;
  const char* xg = P.x[br] + (size_t)b0 * SQ * DM * 2;
  const char* wg = P.wq[br] + (size_t)h * DH * 2;
  const char* kg = P.k[br] + (size_t)(b0 * NH + h) * SK * DH * 2;
  const char* vg = P.v[br] + (size_t)(b0 * NH + h) * SK * DH * 2;
  char* og = ofr + (((size_t)(br * NB + b0) * 192 + h * 6) * 12 + 3 * w) * 1024 + l * 16;

  if (blockIdx.x == 0 && tid < 64) ((unsigned*)(ofr + ((size_t)48 << 20)))[tid * 16] = 0u;
  unsigned offK[3], offV[3], offW[3], offX;
#pragma unroll
  for (int u = 0; u < 3; ++u) {
    const int o = 1024 * u + 16 * l, r = o / 384, chp = (o % 384) >> 4, row = 8 * w + r;
    const int aK = (row >> 1) & 7;
    offK[u] = row * 384 + (((chp & ~7) | ((chp ^ aK) & 7)) << 4);
    offV[u] = row * 384 + ((chp ^ (((row >> 1) & 3) << 1)) << 4);
    const int sW = ((row >> 1) & 1) | (((row >> 3) & 1) << 1);
    offW[u] = row * (HD * 2) + ((chp ^ (sW << 1)) << 4);
  }
  { const int row = 8 * w + (l >> 3), a = (row >> 1) & 7; offX = row * (DM * 2) + (((l & 7) ^ a) << 4); }

  const unsigned oK0 = offK[0], oK1 = offK[1], oK2 = offK[2], oV0 = offV[0], oV1 = offV[1], oV2 = offV[2], oW0 = offW[0], oW1 = offW[1], oW2 = offW[2];
  const unsigned lds0 = (unsigned)(uintptr_t)lds;
  auto px = [&](int c, int slot, auto N) AI {
    constexpr int n = N.value;
    glds16(offX, xg + c * 128 + (size_t)n * 32 * DM * 2, lds0 + slot * SLOT + w * 1024 + n * 4096);
  };
  auto pw_ = [&](int c, int slot, auto N) AI {
    constexpr int n = N.value, u = n % 3, hf = n / 3;
    glds16(u == 0 ? oW0 : u == 1 ? oW1 : oW2, wg + (size_t)(c * 64 + hf * 32) * HD * 2, lds0 + slot * SLOT + w * 3072 + hf * 12288 + u * 1024);
  };
  auto pk = [&](int j, int slot, auto N) AI {
    constexpr int n = N.value, u = n % 3, hf = n / 3;
    glds16nt(u == 0 ? oK0 : u == 1 ? oK1 : oK2, kg + (size_t)j * SLOT + hf * 12288, lds0 + slot * SLOT + w * 3072 + hf * 12288 + u * 1024);
  };
  auto pv = [&](int j, int slot, auto N) AI {
    constexpr int n = N.value, u = n % 3, hf = n / 3;
    glds16nt(u == 0 ? oV0 : u == 1 ? oV1 : oV2, vg + (size_t)j * SLOT + hf * 12288, lds0 + slot * SLOT + w * 3072 + hf * 12288 + u * 1024);
  };
  auto pf_ = [&](auto F, auto N) AI {
    constexpr int f = F.value;
    if constexpr (f >= 36) { if constexpr ((f & 1) == 0) px((f - 36) >> 1, f % NSLOT, N); else pw_((f - 36) >> 1, f % NSLOT, N); }
    else if constexpr ((f & 1) == 0) pk((f - 20) >> 1, f % NSLOT, N); else pv((f - 20) >> 1, f % NSLOT, N);
  };

  const int a8 = i16 >> 1;
  const unsigned kb0 = lds0 + i16 * 384 + ((g ^ a8) << 4);
  const unsigned xb0 = lds0 + (48 * w + i16) * 128 + ((g ^ a8) << 4);
  const int swz = 2 * (g & 1) + (qq >> 1);
  const unsigned vb0 = lds0 + (4 * g + qq) * 384 + (swz << 5) + 8 * pp;
  const unsigned wb0 = lds0 + (8 * g + qq) * 384 + (swz << 5) + 8 * pp;

  asm volatile("" ::: ACLOB);
  f32x4 qa[12][3];
  float inv = 0.f; f32x4 eX, eY;
  auto epi = [&](auto U, auto Pt) AI {
    constexpr int u = U.value, q = u / 6, t = u % 6, part = Pt.value;
    if constexpr (part == 0) {
      if constexpr (t == 0) { float lq; ARD(lq, AO(12, q)); inv = __builtin_amdgcn_rcpf(lq); }
      sfor<4>([&](auto R) AI { ARD(eX[R.value], AO(2 * t, q) + R.value); });
    } else if constexpr (part == 1) {
      sfor<4>([&](auto R) AI { ARD(eY[R.value], AO(2 * t + 1, q) + R.value); });
    } else if constexpr (part == 2) { eX *= inv; eY *= inv; PINF(eX); PINF(eY); }
    else if constexpr (part == 3) {
      u32x4 f = {cvtpk(eX[0], eX[1]), cvtpk(eX[2], eX[3]), cvtpk(eY[0], eY[1]), cvtpk(eY[2], eY[3])};
      *(u32x4*)(og + q * 1024 + t * 12288) = f;
    }
  };
  int sl = 0;
  auto p1step = [&](int c, auto Tc, auto Ec) AI {
    constexpr int TAIL = Tc.value, EPI = Ec.value;
    WAITV_BAR(12);
    const int sn = sl >= 2 ? sl - 2 : sl + 4;
    unsigned ax[2], aw[4];
    ax[0] = adr<0>(xb0, sl * SLOT); ax[1] = adr<64>(xb0, sl * SLOT);
    aw[0] = adr<0>(wb0, (sl + 1) * SLOT); aw[1] = adr<32>(wb0, (sl + 1) * SLOT); aw[2] = adr<64>(wb0, (sl + 1) * SLOT); aw[3] = adr<96>(wb0, (sl + 1) * SLOT);
    sl = sl == 4 ? 0 : sl + 2;
    bf16x8 xf[2][3], wf[3];
    auto rdw = [&](auto F) AI {
      constexpr int f = F.value, s = f / 12, d = f % 12;
      const unsigned p = aw[d & 3] + s * 32 * 384 + (d >> 2) * 128;
      wf[f % 3] = cat8(ldstr(p), ldstr(p + 4 * 384));
    };
#pragma unroll
    for (int q = 0; q < 3; ++q) xf[0][q] = ldsb128(ax[0] + q * 2048);
    rdw(IC<0>{}); rdw(IC<1>{});
    SBAR();
    sfor<24>([&](auto F) AI {
      constexpr int f = F.value, s = f / 12, d = f % 12;
      if constexpr (f + 2 < 24) rdw(IC<f + 2>{});
      if constexpr (f >= 2 && f < 5) xf[1][f - 2] = ldsb128(ax[1] + (f - 2) * 2048);
      mfma_ip_(qa[d][0], wf[f % 3], xf[s][0]);
      if constexpr (EPI >= 0 && EPI < 9) { constexpr int k = f / 12, part = (f % 12) / 2 - 1;
        if constexpr ((f & 1) == 1 && part >= 0 && part < 4) { epi(IC<2 * EPI + k>{}, IC<part>{}); SBAR(); } }
      mfma_ip_(qa[d][1], wf[f % 3], xf[s][1]);
      if constexpr ((f & 1) == 0) {
        constexpr int n = f >> 1;
        if constexpr (n < 6) { if constexpr (!TAIL) px(c + 2, sn, IC<n>{}); else pk(c - 8, sn, IC<n>{}); }
        else { if constexpr (!TAIL) pw_(c + 2, sn + 1, IC<n - 6>{}); else pv(c - 8, sn + 1, IC<n - 6>{}); }
      }
      mfma_ip_(qa[d][2], wf[f % 3], xf[s][2]);
      SBAR();
    });
  };
  auto zero_qa = [&]() AI {
    float z = 0.f; PINF(z);
#pragma unroll
    for (int d = 0; d < 12; ++d) {
#pragma unroll
      for (int q = 0; q < 3; ++q) qa[d][q] = f32x4{z, z, z, z};
      pin3n_(qa[d][0], qa[d][1], qa[d][2]);
    }
  };

  f32x4 sc[2][4][3];
  f32x4 negm[3];
  unsigned pw[2][3][4];
  bf16x8 ones = {0x3F80, 0x3F80, 0x3F80, 0x3F80, 0x3F80, 0x3F80, 0x3F80, 0x3F80};
  asm volatile("" : "+v"(ones));

#define EXP1(X, kb, q, r) do { sc[X][kb][q][r] = __builtin_amdgcn_exp2f(sc[X][kb][q][r]); PINF(sc[X][kb][q][r]); } while (0)
#define CVT1(X, c) do { constexpr int kb_ = (c) / 6, q_ = ((c) % 6) >> 1, hf_ = (c) & 1; \
    pw[kb_ >> 1][q_][2 * (kb_ & 1) + hf_] = cvtpk(sc[X][kb_][q_][2 * hf_], sc[X][kb_][q_][2 * hf_ + 1]); PINU(pw[kb_ >> 1][q_][2 * (kb_ & 1) + hf_]); } while (0)

  auto conv = [&](auto S, auto Q) AI {
    constexpr int s = S.value, q = Q.value;
    const f32x4 X = qa[2 * s][q] * QSCALE, Y = qa[2 * s + 1][q] * QSCALE;
    unsigned x0 = cvtpk(X[0], X[1]), x1 = cvtpk(X[2], X[3]), y0 = cvtpk(Y[0], Y[1]), y1 = cvtpk(Y[2], Y[3]);
    auto r0 = __builtin_amdgcn_permlane32_swap(x0, y0, false, false);
    auto r1 = __builtin_amdgcn_permlane32_swap(x1, y1, false, false);
    auto t0 = __builtin_amdgcn_permlane16_swap(r0[0], r0[1], false, false);
    auto t1 = __builtin_amdgcn_permlane16_swap(r1[0], r1[1], false, false);
    AWR(AQ(s, q) + 0, t0[0]); AWR(AQ(s, q) + 1, t1[0]); AWR(AQ(s, q) + 2, t0[1]); AWR(AQ(s, q) + 3, t1[1]);
  };

  sfor<6>([&](auto N) AI { px(0, 0, N); }); sfor<6>([&](auto N) AI { pw_(0, 1, N); });
  sfor<6>([&](auto N) AI { px(1, 2, N); }); sfor<6>([&](auto N) AI { pw_(1, 3, N); });
  zero_qa();
#pragma unroll 1
  for (int c = 0; c < 8; ++c) p1step(c, IC<0>{}, IC<-1>{});
  p1step(8, IC<1>{}, IC<-1>{}); p1step(9, IC<1>{}, IC<-1>{});

#pragma unroll 1
  for (int it = 0;; ++it) {
    asm volatile("" : "+s"(wg));
    if (it == 0) xg += (size_t)4 * SQ * DM * 2;
    WAITV_BAR(18);
    {
      unsigned ak[2];
      ak[0] = adr<0>(kb0, 2 * SLOT); ak[1] = adr<64>(kb0, 2 * SLOT);
      bf16x8 kf[3];
      auto rdk = [&](auto F) AI { constexpr int f = F.value, s = f >> 2, kb = f & 3; kf[f % 3] = ldsb128(ak[s & 1] + kb * 6144 + (s >> 1) * 128); };
      NOPS(7);
      SBAR();
      conv(IC<0>{}, IC<0>{}); conv(IC<0>{}, IC<1>{}); conv(IC<0>{}, IC<2>{});
      conv(IC<1>{}, IC<0>{}); conv(IC<1>{}, IC<1>{}); conv(IC<1>{}, IC<2>{});
      SBAR();
      rdk(IC<0>{}); rdk(IC<1>{});
      NOPS(1);
      SBAR();
      sfor<24>([&](auto F) AI {
        constexpr int f = F.value, s = f >> 2, kb = f & 3;
        if constexpr (f + 2 < 24) rdk(IC<f + 2>{});
        sfor<3>([&](auto Q) AI {
          constexpr int q = Q.value;
          if constexpr (s == 0) { mfma_s0_<AQ(s, q)>(sc[0][kb][q], kf[f % 3]); } else { MFMA_S(sc[0][kb][q], kf[f % 3], s, q); }
        });
        if constexpr (s < 4 && kb < 3) conv(IC<s + 2>{}, IC<kb>{});
        if constexpr (kb == 3) { pf_(IC<24 + s / 3>{}, IC<2 * (s % 3)>{}); pf_(IC<24 + s / 3>{}, IC<2 * (s % 3) + 1>{}); }
        SBAR();
      });
      asm volatile("s_nop 7" : "+v"(sc[0][0][0]), "+v"(sc[0][0][1]), "+v"(sc[0][0][2]), "+v"(sc[0][1][0]), "+v"(sc[0][1][1]), "+v"(sc[0][1][2]),
                               "+v"(sc[0][2][0]), "+v"(sc[0][2][1]), "+v"(sc[0][2][2]), "+v"(sc[0][3][0]), "+v"(sc[0][3][1]), "+v"(sc[0][3][2]));
#pragma unroll
      for (int q = 0; q < 3; ++q) {
        float m0 = mx3(sc[0][0][q][0], sc[0][0][q][1], sc[0][0][q][2]);
        m0 = mx2(m0, sc[0][0][q][3]);
#pragma unroll
        for (int kb = 1; kb < 4; ++kb) { m0 = mx3(m0, sc[0][kb][q][0], sc[0][kb][q][1]); m0 = mx3(m0, sc[0][kb][q][2], sc[0][kb][q][3]); }
        m0 = xmax(m0);
        negm[q] = f32x4{-m0, -m0, -m0, -m0};
#pragma unroll
        for (int kb = 0; kb < 4; ++kb) sc[0][kb][q] -= m0;
#pragma unroll
        for (int r = 0; r < 4; ++r) sc[0][0][q][r] = __builtin_amdgcn_exp2f(sc[0][0][q][r]);
      }
    }
    sfor<8>([&](auto Jc) AI {
      constexpr int j = Jc.value;
      constexpr int X = j & 1, Y = X ^ 1;
      if constexpr (j == 0 || j == 7) { WAITV_BAR(18); } else { WAITV_BAR(12); }
      if constexpr (j < 7) {
        unsigned ak[2];
        ak[0] = adr<0>(kb0, ((22 + 2 * j) % NSLOT) * SLOT); ak[1] = adr<64>(kb0, ((22 + 2 * j) % NSLOT) * SLOT);
        pin3n_(negm[0], negm[1], negm[2]);
        bf16x8 kf[3];
        auto rdk = [&](auto F) AI { constexpr int f = F.value, s = f >> 2, kb = f & 3; kf[f % 3] = ldsb128(ak[s & 1] + kb * 6144 + (s >> 1) * 128); };
        rdk(IC<0>{}); rdk(IC<1>{});
        SBAR();
        sfor<24>([&](auto F) AI {
          constexpr int f = F.value, s = f >> 2, kb = f & 3;
          if constexpr (f + 2 < 24) rdk(IC<f + 2>{});
          sfor<3>([&](auto Q) AI {
            constexpr int q = Q.value;
            if constexpr (s == 0) { MFMA_SC(sc[Y][kb][q], kf[f % 3], s, q, negm[q]); } else { MFMA_S(sc[Y][kb][q], kf[f % 3], s, q); }
            if constexpr (q == 0) { CVT1(X, f); }
            else {
              constexpr int e = 2 * f + q - 1;
              if constexpr (e < 36) { EXP1(X, 1 + e / 12, (e % 12) >> 2, e & 3); }
              else if constexpr (q == 1 && j >= 1) pf_(IC<25 + 2 * j>{}, IC<f - 18>{});
            }
            SBAR();
          });
        });
      } else {
        sfor<36>([&](auto E) AI { constexpr int e = E.value; EXP1(X, 1 + e / 12, (e % 12) >> 2, e & 3);
          if constexpr ((e % 6) == 5) pf_(IC<39>{}, IC<e / 6>{}); });
        sfor<24>([&](auto C) AI { CVT1(X, C.value); });
      }
      {
        bf16x8 pf[2][3];
#pragma unroll
        for (int t = 0; t < 2; ++t)
#pragma unroll
          for (int q = 0; q < 3; ++q) { u32x4 f = {pw[t][q][0], pw[t][q][1], pw[t][q][2], pw[t][q][3]}; pf[t][q] = __builtin_bit_cast(bf16x8, f); }
        pin3n_(pf[0][0], pf[0][1], pf[0][2]); pin3n_(pf[1][0], pf[1][1], pf[1][2]);
        unsigned av[4];
        { constexpr unsigned so = ((21 + 2 * j) % NSLOT) * SLOT;
          av[0] = adr<0>(vb0, so); av[1] = adr<32>(vb0, so); av[2] = adr<64>(vb0, so); av[3] = adr<96>(vb0, so); }
        bf16x8 vf[3];
        auto rdv = [&](auto F) AI {
          constexpr int f = F.value, t = f / 12, d = f % 12;
          const unsigned p = av[d & 3] + t * 32 * 384 + (d >> 2) * 128;
          vf[f % 3] = cat8(ldstr(p), ldstr(p + 16 * 384));
        };
        float mx[3], fct[3];
        bool pend = false;
        auto mop = [&](auto I) AI {
          constexpr int q = I.value % 3, k = I.value / 3;
          if constexpr (k == 0) mx[q] = mx2(sc[Y][0][q][0], sc[Y][0][q][1]);
          else { constexpr int e = 2 * k; mx[q] = mx3c(mx[q], sc[Y][e >> 2][q][e & 3], sc[Y][e >> 2][q][(e & 3) + 1]); }
          PINF(mx[q]);
        };
        rdv(IC<0>{}); rdv(IC<1>{});
        SBAR();
        sfor<78>([&](auto Mc) AI {
          constexpr int m = Mc.value, G = m / 3, q = m % 3, t = G / 13, gi = G % 13;
          if constexpr (gi == 0) {
            if constexpr (j == 0 && t == 0) { MFMA_O0(12, q, ones, pf[t][q]); } else { MFMA_O(12, q, ones, pf[t][q]); }
          } else {
            constexpr int d = gi - 1, f = 12 * t + d;
            if constexpr (q == 0 && f + 2 < 24) rdv(IC<f + 2>{});
            if constexpr (j == 0 && t == 0) { MFMA_O0(d, q, vf[f % 3], pf[t][q]); } else { MFMA_O(d, q, vf[f % 3], pf[t][q]); }
          }
          if constexpr (j < 7 && q < 2) {
            if constexpr (G >= 2 && G < 8) { constexpr int i = 4 * (G - 2) + 2 * q; mop(IC<i>{}); mop(IC<i + 1>{}); }
            if constexpr (G == 8 && q == 0) {
              pend = __any(mx3c(mx[0], mx[1], mx[2]) > THR);
              if (__builtin_expect(pend, 0)) {
#pragma unroll
                for (int qq_ = 0; qq_ < 3; ++qq_) {
                  const float gr = xmax(mx[qq_]);
                  const unsigned db = (__float_as_uint(fmaxf(gr, 0.f)) + 0xFFFFu) & 0xFFFF0000u;
                  fct[qq_] = __builtin_amdgcn_exp2f(-__uint_as_float(db));
                  u32x4 bw = {g == 0 ? ((db >> 16) | 0x8000u) : 0u, 0u, 0u, 0u};
                  bf16x8 bfr = __builtin_bit_cast(bf16x8, bw);
                  asm volatile("s_nop 1" : "+v"(bfr));
                  mfma_ip_(negm[qq_], ones, bfr);
                  mfma_ip_(sc[Y][0][qq_], ones, bfr); mfma_ip_(sc[Y][1][qq_], ones, bfr); mfma_ip_(sc[Y][2][qq_], ones, bfr); mfma_ip_(sc[Y][3][qq_], ones, bfr);
                }
                NOPS(7);
              }
            }
            if constexpr (G >= 9 && G < 15) { constexpr int e = 2 * (G - 9) + q; EXP1(Y, 0, e >> 2, e & 3); }
          }
          if constexpr (j < 7 && q == 1 && G >= 16 && G < 22) pf_(IC<26 + 2 * j>{}, IC<G - 16>{});
          SBAR();
        });
        if constexpr (j < 7) {
          if (__builtin_expect(pend, 0)) {
            NOPS(7);
            sfor<156>([&](auto N) AI { ASCALE(N.value, fct[(N.value >> 2) % 3]); });
            NOPS(1);
          }
        }
      }
    });
    if (it == 1) break;
    kg += (size_t)4 * NH * SK * DH * 2; vg += (size_t)4 * NH * SK * DH * 2;
    zero_qa();
    sl = 0;
    sfor<10>([&](auto C) AI { p1step(C.value, IC<(C.value >= 8)>{}, IC<C.value>{}); });
    og += (size_t)4 * 192 * 12 * 1024;
  }
  NOPS(7);
  sfor<18>([&](auto U) AI { epi(U, IC<0>{}); epi(U, IC<1>{}); epi(U, IC<2>{}); epi(U, IC<3>{}); });
  asm volatile("s_waitcnt vmcnt(0)" ::: "memory");
}

constexpr int ST2 = 128 * 320;
constexpr int LDS2 = 3 * ST2;
constexpr size_t WS_SLAB = (size_t)64 << 20;
constexpr size_t WS_CNT = (size_t)48 << 20;

DI void gload16(bf16x8& d, unsigned voff, const char* sbase) { asm volatile("global_load_dwordx4 %0, %1, %2" : "=v"(d) : "v"(voff), "s"(sbase) : "memory"); }
template <int N> DI void wait3(bf16x8& a, bf16x8& b, bf16x8& c) { asm volatile("s_waitcnt vmcnt(%3)" : "+v"(a), "+v"(b), "+v"(c) : "i"(N) : "memory"); }

__global__ __launch_bounds__(512, 2) void k_oproj(Ptrs P, char* __restrict__ ws, char* __restrict__ out) {
  extern __shared__ __attribute__((aligned(16))) char smem[];
  lds_ptr lds = (lds_ptr)smem;
  const int tid = threadIdx.x, l = tid & 63, w = __builtin_amdgcn_readfirstlane(tid >> 6);
  const int i16 = l & 15, g = l >> 4, qq = i16 >> 2, pp = i16 & 3;
  const int xcd = blockIdx.x & 7, idx = blockIdx.x >> 3;
  const int br = xcd >> 2, ks = xcd & 3, b = idx >> 2, nq = idx & 3;
  const int kh = w >> 2, qg = w & 3;
  const unsigned lds0 = (unsigned)(uintptr_t)lds;
  const char* wsrc = P.wo[br] + (size_t)ks * 1536 * DM * 2 + nq * 320;
  const char* osrc = ws + (((size_t)(br * NB + b) * 192 + 48 * ks + kh) * 12 + 3 * qg) * 1024;
  const unsigned l16 = l * 16;
  unsigned offW[5];
#pragma unroll
  for (int u = 0; u < 5; ++u) {
    const int o = 1024 * u + 16 * l, r = o / 320, chp = (o % 320) >> 4;
    offW[u] = (16 * w + r) * (DM * 2) + ((chp ^ (((r >> 2) & 1) << 1)) << 4);
  }
  const unsigned o0 = offW[0], o1 = offW[1], o2 = offW[2], o3 = offW[3], o4 = offW[4];
  const int s0 = 0;
  auto wpiece = [&](const char* src, unsigned d, auto Uc) AI {
    constexpr int u = Uc.value;
    glds16(u == 0 ? o0 : u == 1 ? o1 : u == 2 ? o2 : u == 3 ? o3 : o4, src, d + u * 1024);
  };
  auto wsrc_of = [&](int s) AI { s += s0; s = s >= 12 ? s - 12 : s; return wsrc + (size_t)s * 128 * DM * 2; };
  auto osrc_of = [&](int u) AI { u += 2 * s0; u = u >= 24 ? u - 24 : u; return osrc + (size_t)u * 24576; };
  auto issueW = [&](int s, int stg) AI {
    const unsigned d = lds0 + stg * ST2 + w * 5120;
    const char* src = wsrc_of(s);
    sfor<5>([&](auto U) AI { wpiece(src, d, U); });
  };
  bf16x8 of[4][3];
  auto loadO = [&](int u, auto Bc) AI {
    constexpr int B_ = Bc.value;
    const char* src = osrc_of(u);
    gload16(of[B_][0], l16, src); gload16(of[B_][1], l16, src + 1024); gload16(of[B_][2], l16, src + 2048);
  };
  f32x4 acc[10][3];
#pragma unroll
  for (int n = 0; n < 10; ++n)
#pragma unroll
    for (int q = 0; q < 3; ++q) acc[n][q] = f32x4{0.f, 0.f, 0.f, 0.f};
  const unsigned wb0 = lds0 + (32 * kh + 4 * g + qq) * 320 + 8 * pp + ((g & 1) << 5);
  issueW(0, 0); loadO(0, IC<0>{}); loadO(1, IC<1>{}); loadO(2, IC<2>{});
  issueW(1, 1);
  int stg = 0;
  auto kstep = [&](unsigned ae, unsigned ao, auto Vc, auto Bc, auto Lc, const char* lsrc, const char* dsrc, unsigned dd, auto Dc) AI {
    constexpr int v = Vc.value, B_ = Bc.value, LB = Lc.value, DN = Dc.value;
    bf16x8 wf[3];
    auto rdw = [&](auto N) AI { constexpr int n = N.value; const unsigned p = ((n & 1) ? ao : ae) + v * 64 * 320 + (n >> 1) * 64; wf[n % 3] = cat8(ldstr(p), ldstr(p + 16 * 320)); };
    rdw(IC<0>{}); rdw(IC<1>{});
    SBAR();
    sfor<10>([&](auto N) AI {
      constexpr int n = N.value;
      if constexpr (n + 2 < 10) rdw(IC<n + 2>{});
      acc[n][0] = MFMA16(wf[n % 3], of[B_][0], acc[n][0]);
      if constexpr (LB >= 0 && n < 3) { SBAR(); gload16(of[LB][n], l16, lsrc + n * 1024); SBAR(); }
      if constexpr (DN > 0 && n >= 4 && n < 4 + 2 * DN && (n & 1) == 0) { SBAR(); wpiece(dsrc, dd, IC<(v ? 2 : 0) + (n - 4) / 2>{}); SBAR(); }
      acc[n][1] = MFMA16(wf[n % 3], of[B_][1], acc[n][1]);
      acc[n][2] = MFMA16(wf[n % 3], of[B_][2], acc[n][2]);
      SBAR();
    });
  };
  auto stage = [&](int s, auto Pc, auto Mc) AI {
    constexpr int PAR = Pc.value, MODE = Mc.value, B0 = 2 * PAR, B1 = 2 * PAR + 1;
    if constexpr (MODE == 2) { asm volatile("s_waitcnt vmcnt(3) lgkmcnt(0)\n\ts_barrier" ::: "memory"); }
    else { asm volatile("s_waitcnt vmcnt(11) lgkmcnt(0)\n\ts_barrier" ::: "memory"); }
    wait3<63>(of[B0][0], of[B0][1], of[B0][2]);
    const unsigned ae = opq(wb0 + stg * ST2), ao = opq((wb0 ^ 32) + stg * ST2);
    const unsigned dd = lds0 + (stg == 0 ? 2 : stg - 1) * ST2 + w * 5120;
    const char* dsrc = wsrc_of(s + 2);
    kstep(ae, ao, IC<0>{}, IC<B0>{}, IC<(MODE <= 1 ? ((B0 + 3) & 3) : -1)>{}, osrc_of(2 * s + 3), dsrc, dd, IC<(MODE == 0 ? 2 : 0)>{});
    wait3<MODE == 0 ? 13 : MODE == 1 ? 11 : 0>(of[B1][0], of[B1][1], of[B1][2]);
    kstep(ae, ao, IC<1>{}, IC<B1>{}, IC<(MODE == 0 ? B0 : -1)>{}, osrc_of(2 * s + 4), dsrc, dd, IC<(MODE == 0 ? 3 : 0)>{});
    stg = stg == 2 ? 0 : stg + 1;
  };
#pragma unroll 1
  for (int s = 0; s < 10; s += 2) { stage(s, IC<0>{}, IC<0>{}); stage(s + 1, IC<1>{}, IC<0>{}); }
  stage(10, IC<0>{}, IC<1>{}); stage(11, IC<1>{}, IC<2>{});
  asm volatile("s_waitcnt vmcnt(0) lgkmcnt(0)\n\ts_barrier" ::: "memory");
  {
    const unsigned xo = lds0 + ((kh ^ 1) * 4 + qg) * 15360 + l16, xi = lds0 + (kh * 4 + qg) * 15360 + l16;
#pragma unroll
    for (int n = 0; n < 5; ++n)
#pragma unroll
      for (int q = 0; q < 3; ++q) *(__attribute__((address_space(3))) f32x4*)(xo + (n * 3 + q) * 1024) = kh ? acc[n][q] : acc[5 + n][q];
    __syncthreads();
#pragma unroll
    for (int n = 0; n < 5; ++n)
#pragma unroll
      for (int q = 0; q < 3; ++q) {
        const f32x4 o = *(const __attribute__((address_space(3))) f32x4*)(xi + (n * 3 + q) * 1024);
        acc[n][q] = (kh ? acc[5 + n][q] : acc[n][q]) + o;
      }
  }
  const int tile = (br * NB + b) * 4 + nq;
  char* slab = ws + WS_SLAB + (size_t)tile * 4 * 8 * 15360;
  {
    char* mine = slab + (size_t)(ks * 8 + w) * 15360 + l16;
#pragma unroll
    for (int i = 0; i < 15; ++i)
      if ((i & 3) != ks) asm volatile("global_store_dwordx4 %0, %1, off sc0 sc1\n\ts_nop 1" :: "v"(mine + i * 1024), "v"(acc[i / 3][i % 3]) : "memory");
    asm volatile("s_waitcnt vmcnt(0)" ::: "memory");
    __syncthreads();
    unsigned* cnt = (unsigned*)(ws + WS_CNT) + tile * 16;
    if (tid == 0) {
      __hip_atomic_fetch_add(cnt, 1u, __ATOMIC_RELAXED, __HIP_MEMORY_SCOPE_AGENT);
      unsigned spins = 0;
      while (__hip_atomic_load(cnt, __ATOMIC_RELAXED, __HIP_MEMORY_SCOPE_AGENT) < 4u && ++spins < (1u << 24)) __builtin_amdgcn_s_sleep(1);
    }
    __syncthreads();
  }
  {
    const char* theirs = slab + (size_t)w * 15360 + l16;
    f32x4 p[4][3];
#pragma unroll
    for (int m = 0; m < 4; ++m) {
      const int i = (4 * m + ks) < 15 ? 4 * m + ks : 11;
#pragma unroll
      for (int r = 0; r < 3; ++r) {
        const int src = (ks + 1 + r) & 3;
        asm volatile("global_load_dwordx4 %0, %1, off sc0 sc1" : "=v"(p[m][r]) : "v"(theirs + (size_t)src * 8 * 15360 + i * 1024) : "memory");
      }
    }
    asm volatile("s_waitcnt vmcnt(0)" : "+v"(p[0][0]), "+v"(p[0][1]), "+v"(p[0][2]), "+v"(p[1][0]), "+v"(p[1][1]), "+v"(p[1][2]),
                                        "+v"(p[2][0]), "+v"(p[2][1]), "+v"(p[2][2]), "+v"(p[3][0]), "+v"(p[3][1]), "+v"(p[3][2]) :: "memory");
#pragma unroll
    for (int i = 0; i < 15; ++i) {
      if ((i & 3) == ks) {
        const f32x4 o = (acc[i / 3][i % 3] + p[i >> 2][0]) + (p[i >> 2][1] + p[i >> 2][2]);
        u32x2 f = {cvtpk(o[0], o[1]), cvtpk(o[2], o[3])};
        char* dst = out + ((size_t)(b * SQ + (3 * qg + i % 3) * 16 + i16) * (2 * DM) + br * DM + nq * 160 + (5 * kh + i / 3) * 16 + 4 * g) * 2;
        *(u32x2*)dst = f;
      }
    }
  }
}

extern "C" void kernel_launch(void* const* d_in, const int* in_sizes, int n_in, void* d_out, int out_size, void* d_ws, size_t ws_size,
                              hipStream_t stream) {
  Ptrs P;
  for (int r = 0; r < 2; ++r) {
    P.x[r] = (const char*)d_in[5 * r + 0];
    P.k[r] = (const char*)d_in[5 * r + 1];
    P.v[r] = (const char*)d_in[5 * r + 2];
    P.wq[r] = (const char*)d_in[5 * r + 3];
    P.wo[r] = (const char*)d_in[5 * r + 4];
  }
  (void)hipFuncSetAttribute((const void*)k_attn, hipFuncAttributeMaxDynamicSharedMemorySize, LDS1);
  (void)hipFuncSetAttribute((const void*)k_oproj, hipFuncAttributeMaxDynamicSharedMemorySize, LDS2);
  hipLaunchKernelGGL(k_attn, dim3(256), dim3(256), LDS1, stream, P, (char*)d_ws);
  hipLaunchKernelGGL(k_oproj, dim3(256), dim3(512), LDS2, stream, P, (char*)d_ws, (char*)d_out);
}
```

```cpp
#include <hip/hip_runtime.h>
#include <hip/hip_bf16.h>
#include <stdint.h>
#include <utility>

using bf16x8 = __attribute__((ext_vector_type(8))) short;
using s16x4 = __attribute__((ext_vector_type(4))) short;
using f32x4 = __attribute__((ext_vector_type(4))) float;
using u32x4 = __attribute__((ext_vector_type(4))) unsigned;
using u32x2 = __attribute__((ext_vector_type(2))) unsigned;
typedef short v4i16_t __attribute__((ext_vector_type(4)));
typedef __attribute__((address_space(3))) char* lds_ptr;
typedef __attribute__((address_space(3))) const char* lds_cptr;

constexpr int NB = 8, SQ = 192, DM = 640, NH = 32, SK = 512, DH = 192;
constexpr int HD = NH * DH;
constexpr int SLOT = 24576;
constexpr int NSLOT = 6;
constexpr int LDS1 = SLOT * NSLOT;
constexpr float QSCALE = 0.07216878364870322f * 1.4426950408889634f;
constexpr float THR = 8.0f;

#define DI __device__ __forceinline__
#define MFMA16(a, b, c) __builtin_amdgcn_mfma_f32_16x16x32_bf16((a), (b), (c), 0, 0, 0)
#define ACLOB "a0","a1","a2","a3","a4","a5","a6","a7","a8","a9","a10","a11","a12","a13","a14","a15","a16","a17","a18","a19","a20","a21","a22","a23","a24","a25","a26","a27","a28","a29","a30","a31","a32","a33","a34","a35","a36","a37","a38","a39","a40","a41","a42","a43","a44","a45","a46","a47","a48","a49","a50","a51","a52","a53","a54","a55","a56","a57","a58","a59","a60","a61","a62","a63","a64","a65","a66","a67","a68","a69","a70","a71","a72","a73","a74","a75","a76","a77","a78","a79","a80","a81","a82","a83","a84","a85","a86","a87","a88","a89","a90","a91","a92","a93","a94","a95","a96","a97","a98","a99","a100","a101","a102","a103","a104","a105","a106","a107","a108","a109","a110","a111","a112","a113","a114","a115","a116","a117","a118","a119","a120","a121","a122","a123","a124","a125","a126","a127","a128","a129","a130","a131","a132","a133","a134","a135","a136","a137","a138","a139","a140","a141","a142","a143","a144","a145","a146","a147","a148","a149","a150","a151","a152","a153","a154","a155","a156","a157","a158","a159","a160","a161","a162","a163","a164","a165","a166","a167","a168","a169","a170","a171","a172","a173","a174","a175","a176","a177","a178","a179","a180","a181","a182","a183","a184","a185","a186","a187","a188","a189","a190","a191","a192","a193","a194","a195","a196","a197","a198","a199","a200","a201","a202","a203","a204","a205","a206","a207","a208","a209","a210","a211","a212","a213","a214","a215","a216","a217","a218","a219","a220","a221","a222","a223","a224","a225","a226","a227"
#define AO(d, q) (4 * (3 * (d) + (q)))
#define AQ(s, q) (156 + 4 * (3 * (s) + (q)))
template <int N> DI void mfma_o_(const bf16x8& a, const bf16x8& b) { asm volatile("v_mfma_f32_16x16x32_bf16 a[%c2:%c3], %0, %1, a[%c2:%c3]" :: "v"(a), "v"(b), "i"(N), "i"(N + 3) : ACLOB); }
template <int N> DI void mfma_o0_(const bf16x8& a, const bf16x8& b) { asm volatile("v_mfma_f32_16x16x32_bf16 a[%c2:%c3], %0, %1, 0" :: "v"(a), "v"(b), "i"(N), "i"(N + 3) : ACLOB); }
template <int N> DI void mfma_s_(f32x4& acc, const bf16x8& a) { asm volatile("v_mfma_f32_16x16x32_bf16 %0, %1, a[%c2:%c3], %0" : "+v"(acc) : "v"(a), "i"(N), "i"(N + 3) : ACLOB); }
template <int N> DI void mfma_sc_(f32x4& acc, const bf16x8& a, const f32x4& c) { asm volatile("v_mfma_f32_16x16x32_bf16 %0, %1, a[%c2:%c3], %4" : "=&v"(acc) : "v"(a), "i"(N), "i"(N + 3), "v"(c) : ACLOB); }
template <int N> DI void mfma_s0_(f32x4& acc, const bf16x8& a) { asm volatile("v_mfma_f32_16x16x32_bf16 %0, %1, a[%c2:%c3], 0" : "=&v"(acc) : "v"(a), "i"(N), "i"(N + 3) : ACLOB); }
template <int N> DI float ard_() { float r; asm volatile("v_accvgpr_read_b32 %0, a[%c1]" : "=v"(r) : "i"(N)); return r; }
template <int N, class T> DI void awr_(T src) { asm volatile("v_accvgpr_write_b32 a[%c1], %0" :: "v"(src), "i"(N)); }
template <int N> DI void ascale_(float f) { float t; asm volatile("v_accvgpr_read_b32 %0, a[%c1]\n\tv_mul_f32 %0, %2, %0\n\tv_accvgpr_write_b32 a[%c1], %0" : "=&v"(t) : "i"(N), "v"(f)); }
DI float subip_(float x, float d) { asm volatile("v_sub_f32 %0, %0, %1" : "+v"(x) : "v"(d)); return x; }
DI void mfma_ip_(f32x4& x, const bf16x8& a, const bf16x8& b) { asm volatile("v_mfma_f32_16x16x32_bf16 %0, %1, %2, %0" : "+v"(x) : "v"(a), "v"(b) : ACLOB); }
template <class T> DI T pin_(T x) { asm volatile("" : "+v"(x)); return x; }
template <class T> DI void pin3n_(T& a, T& b, T& c) { asm volatile("s_nop 1" : "+v"(a), "+v"(b), "+v"(c)); }
#define MFMA_O(d, q, a, b) mfma_o_<AO(d, q)>(a, b)
#define MFMA_O0(d, q, a, b) mfma_o0_<AO(d, q)>(a, b)
#define MFMA_S(acc, a, s, q) mfma_s_<AQ(s, q)>(acc, a)
#define MFMA_SC(acc, a, s, q, c) mfma_sc_<AQ(s, q)>(acc, a, c)
#define ARD(dst, n) dst = ard_<n>()
#define AWR(n, src) awr_<n>(src)
#define ASCALE(n, f) ascale_<n>(f)
#define SUBIP(x, d) x = subip_(x, d)
#define NOPS(n) asm volatile("s_nop " #n ::: "memory")
#define SBAR() __builtin_amdgcn_sched_barrier(0)
#define PINF(x) x = pin_(x)
#define PINU(x) x = pin_(x)
#define WAITV_BAR(N) asm volatile("s_waitcnt vmcnt(" #N ") lgkmcnt(0)\n\ts_barrier" ::: "memory")

DI unsigned cvtpk(float lo, float hi) {
  typedef float f32x2 __attribute__((ext_vector_type(2)));
  typedef __bf16 bf16x2 __attribute__((ext_vector_type(2)));
  f32x2 v = {lo, hi}; bf16x2 b = __builtin_convertvector(v, bf16x2); return __builtin_bit_cast(unsigned, b);
}
DI void glds16(unsigned voff, const char* sbase, unsigned ldsaddr) {
  asm volatile("s_mov_b32 m0, %2\n\ts_nop 0\n\tglobal_load_lds_dwordx4 %0, %1" :: "v"(voff), "s"(sbase), "s"(ldsaddr) : "memory");
}
DI void glds16nt(unsigned voff, const char* sbase, unsigned ldsaddr) {
  asm volatile("s_mov_b32 m0, %2\n\ts_nop 0\n\tglobal_load_lds_dwordx4 %0, %1 nt" :: "v"(voff), "s"(sbase), "s"(ldsaddr) : "memory");
}
DI bf16x8 ldsb128(unsigned p) { return *(const __attribute__((address_space(3))) bf16x8*)p; }
DI s16x4 ldstr(unsigned p) { return __builtin_bit_cast(s16x4, __builtin_amdgcn_ds_read_tr16_b64_v4i16((__attribute__((address_space(3))) v4i16_t*)p)); }
template <int X> DI unsigned adr(unsigned base, unsigned off) {
  unsigned r;
  if constexpr (X == 0) asm volatile("v_add_u32 %0, %2, %1" : "=v"(r) : "v"(base), "s"(off));
  else asm volatile("v_xor_b32 %0, %3, %1\n\tv_add_u32 %0, %2, %0" : "=&v"(r) : "v"(base), "s"(off), "i"(X));
  return r;
}
DI unsigned opq(unsigned x) { asm volatile("" : "+v"(x)); return x; }
DI bf16x8 cat8(s16x4 lo, s16x4 hi) { return __builtin_shufflevector(lo, hi, 0, 1, 2, 3, 4, 5, 6, 7); }
DI float mx3(float a, float b, float c) { float r; asm("v_max3_f32 %0, %1, %2, %3" : "=v"(r) : "v"(a), "v"(b), "v"(c)); return r; }
DI float mx3c(float a, float b, float c) { return __builtin_fmaxf(__builtin_fmaxf(a, b), c); }
DI float mx2(float a, float b) { float r; asm("v_max_f32_e32 %0, %1, %2" : "=v"(r) : "v"(a), "v"(b)); return r; }
DI float xmax(float v) {
  auto a = __builtin_amdgcn_permlane32_swap(__float_as_uint(v), __float_as_uint(v), false, false);
  v = mx2(__uint_as_float(a[0]), __uint_as_float(a[1]));
  auto b = __builtin_amdgcn_permlane16_swap(__float_as_uint(v), __float_as_uint(v), false, false);
  return mx2(__uint_as_float(b[0]), __uint_as_float(b[1]));
}
DI float xsum(float v) {
  auto a = __builtin_amdgcn_permlane32_swap(__float_as_uint(v), __float_as_uint(v), false, false);
  v = __uint_as_float(a[0]) + __uint_as_float(a[1]);
  auto b = __builtin_amdgcn_permlane16_swap(__float_as_uint(v), __float_as_uint(v), false, false);
  return __uint_as_float(b[0]) + __uint_as_float(b[1]);
}

template <int N> struct IC { static constexpr int value = N; };
#define AI __attribute__((always_inline))
template <class F, int... Is> DI void sfor_(F&& f, std::integer_sequence<int, Is...>) { (f(IC<Is>{}), ...); }
template <int N, class F> DI void sfor(F&& f) { sfor_(f, std::make_integer_sequence<int, N>{}); }
struct Ptrs { const char* x[2]; const char* k[2]; const char* v[2]; const char* wq[2]; const char* wo[2]; };

__global__ __launch_bounds__(256, 1) void k_attn(Ptrs P, char* __restrict__ ofr) {
  extern __shared__ __attribute__((aligned(16))) char smem[];
  lds_ptr lds = (lds_ptr)smem;
  const int tid = threadIdx.x, l = tid & 63, w = __builtin_amdgcn_readfirstlane(tid >> 6);
  const int i16 = l & 15, g = l >> 4, qq = i16 >> 2, pp = i16 & 3;
  const int xcd = blockIdx.x & 7, idx = blockIdx.x >> 3;
  const int br = xcd >> 2, h = 8 * (xcd & 3) + (idx & 7), b0 = idx >> 3;
  const char* xg = P.x[br] + (size_t)b0 * SQ * DM * 2;
  const char* wg = P.wq[br] + (size_t)h * DH * 2;
  const char* kg = P.k[br] + (size_t)(b0 * NH + h) * SK * DH * 2;
  const char* vg = P.v[br] + (size_t)(b0 * NH + h) * SK * DH * 2;
  char* og = ofr + (((size_t)(br * NB + b0) * 192 + h * 6) * 12 + 3 * w) * 1024 + l * 16;

  if (blockIdx.x == 0 && tid < 64) ((unsigned*)(ofr + ((size_t)48 << 20)))[tid * 16] = 0u;
  unsigned offK[3], offV[3], offW[3], offX;
#pragma unroll
  for (int u = 0; u < 3; ++u) {
    const int o = 1024 * u + 16 * l, r = o / 384, chp = (o % 384) >> 4, row = 8 * w + r;
    const int aK = (row >> 1) & 7;
    offK[u] = row * 384 + (((chp & ~7) | ((chp ^ aK) & 7)) << 4);
    offV[u] = row * 384 + ((chp ^ (((row >> 1) & 3) << 1)) << 4);
    const int sW = ((row >> 1) & 1) | (((row >> 3) & 1) << 1);
    offW[u] = row * (HD * 2) + ((chp ^ (sW << 1)) << 4);
  }
  { const int row = 8 * w + (l >> 3), a = (row >> 1) & 7; offX = row * (DM * 2) + (((l & 7) ^ a) << 4); }

  const unsigned oK0 = offK[0], oK1 = offK[1], oK2 = offK[2], oV0 = offV[0], oV1 = offV[1], oV2 = offV[2], oW0 = offW[0], oW1 = offW[1], oW2 = offW[2];
  const unsigned lds0 = (unsigned)(uintptr_t)lds;
  auto px = [&](int c, int slot, auto N) AI {
    constexpr int n = N.value;
    glds16(offX, xg + c * 128 + (size_t)n * 32 * DM * 2, lds0 + slot * SLOT + w * 1024 + n * 4096);
  };
  auto pw_ = [&](int c, int slot, auto N) AI {
    constexpr int n = N.value, u = n % 3, hf = n / 3;
    glds16(u == 0 ? oW0 : u == 1 ? oW1 : oW2, wg + (size_t)(c * 64 + hf * 32) * HD * 2, lds0 + slot * SLOT + w * 3072 + hf * 12288 + u * 1024);
  };
  auto pk = [&](int j, int slot, auto N) AI {
    constexpr int n = N.value, u = n % 3, hf = n / 3;
    glds16nt(u == 0 ? oK0 : u == 1 ? oK1 : oK2, kg + (size_t)j * SLOT + hf * 12288, lds0 + slot * SLOT + w * 3072 + hf * 12288 + u * 1024);
  };
  auto pv = [&](int j, int slot, auto N) AI {
    constexpr int n = N.value, u = n % 3, hf = n / 3;
    glds16nt(u == 0 ? oV0 : u == 1 ? oV1 : oV2, vg + (size_t)j * SLOT + hf * 12288, lds0 + slot * SLOT + w * 3072 + hf * 12288 + u * 1024);
  };
  auto pf_ = [&](auto F, auto N) AI {
    constexpr int f = F.value;
    if constexpr (f >= 36) { if constexpr ((f & 1) == 0) px((f - 36) >> 1, f % NSLOT, N); else pw_((f - 36) >> 1, f % NSLOT, N); }
    else if constexpr ((f & 1) == 0) pk((f - 20) >> 1, f % NSLOT, N); else pv((f - 20) >> 1, f % NSLOT, N);
  };

  const int a8 = i16 >> 1;
  const unsigned kb0 = lds0 + i16 * 384 + ((g ^ a8) << 4);
  const unsigned xb0 = lds0 + (48 * w + i16) * 128 + ((g ^ a8) << 4);
  const int swz = 2 * (g & 1) + (qq >> 1);
  const unsigned vb0 = lds0 + (4 * g + qq) * 384 + (swz << 5) + 8 * pp;
  const unsigned wb0 = lds0 + (8 * g + qq) * 384 + (swz << 5) + 8 * pp;

  asm volatile("" ::: ACLOB);
  f32x4 qa[12][3];
  float inv = 0.f; f32x4 eX, eY;
  auto epi = [&](auto U, auto Pt) AI {
    constexpr int u = U.value, q = u / 6, t = u % 6, part = Pt.value;
    if constexpr (part == 0) {
      if constexpr (t == 0) { float lq; ARD(lq, AO(12, q)); inv = __builtin_amdgcn_rcpf(lq); }
      sfor<4>([&](auto R) AI { ARD(eX[R.value], AO(2 * t, q) + R.value); });
    } else if constexpr (part == 1) {
      sfor<4>([&](auto R) AI { ARD(eY[R.value], AO(2 * t + 1, q) + R.value); });
    } else if constexpr (part == 2) { eX *= inv; eY *= inv; PINF(eX); PINF(eY); }
    else if constexpr (part == 3) {
      u32x4 f = {cvtpk(eX[0], eX[1]), cvtpk(eX[2], eX[3]), cvtpk(eY[0], eY[1]), cvtpk(eY[2], eY[3])};
      __builtin_nontemporal_store(f, (u32x4*)(og + q * 1024 + t * 12288));
    }
  };
  int sl = 0;
  auto p1step = [&](int c, auto Tc, auto Ec) AI {
    constexpr int TAIL = Tc.value, EPI = Ec.value;
    WAITV_BAR(12);
    const int sn = sl >= 2 ? sl - 2 : sl + 4;
    unsigned ax[2], aw[4];
    ax[0] = adr<0>(xb0, sl * SLOT); ax[1] = adr<64>(xb0, sl * SLOT);
    aw[0] = adr<0>(wb0, (sl + 1) * SLOT); aw[1] = adr<32>(wb0, (sl + 1) * SLOT); aw[2] = adr<64>(wb0, (sl + 1) * SLOT); aw[3] = adr<96>(wb0, (sl + 1) * SLOT);
    sl = sl == 4 ? 0 : sl + 2;
    bf16x8 xf[2][3], wf[3];
    auto rdw = [&](auto F) AI {
      constexpr int f = F.value, s = f / 12, d = f % 12;
      const unsigned p = aw[d & 3] + s * 32 * 384 + (d >> 2) * 128;
      wf[f % 3] = cat8(ldstr(p), ldstr(p + 4 * 384));
    };
#pragma unroll
    for (int q = 0; q < 3; ++q) xf[0][q] = ldsb128(ax[0] + q * 2048);
    rdw(IC<0>{}); rdw(IC<1>{});
    SBAR();
    sfor<24>([&](auto F) AI {
      constexpr int f = F.value, s = f / 12, d = f % 12;
      if constexpr (f + 2 < 24) rdw(IC<f + 2>{});
      if constexpr (f >= 2 && f < 5) xf[1][f - 2] = ldsb128(ax[1] + (f - 2) * 2048);
      mfma_ip_(qa[d][0], wf[f % 3], xf[s][0]);
      if constexpr (EPI >= 0 && EPI < 9) { constexpr int k = f / 12, part = (f % 12) / 2 - 1;
        if constexpr ((f & 1) == 1 && part >= 0 && part < 4) { epi(IC<2 * EPI + k>{}, IC<part>{}); SBAR(); } }
      mfma_ip_(qa[d][1], wf[f % 3], xf[s][1]);
      if constexpr ((f & 1) == 0) {
        constexpr int n = f >> 1;
        if constexpr (n < 6) { if constexpr (!TAIL) px(c + 2, sn, IC<n>{}); else pk(c - 8, sn, IC<n>{}); }
        else { if constexpr (!TAIL) pw_(c + 2, sn + 1, IC<n - 6>{}); else pv(c - 8, sn + 1, IC<n - 6>{}); }
      }
      mfma_ip_(qa[d][2], wf[f % 3], xf[s][2]);
      SBAR();
    });
  };
  auto zero_qa = [&]() AI {
    float z = 0.f; PINF(z);
#pragma unroll
    for (int d = 0; d < 12; ++d) {
#pragma unroll
      for (int q = 0; q < 3; ++q) qa[d][q] = f32x4{z, z, z, z};
      pin3n_(qa[d][0], qa[d][1], qa[d][2]);
    }
  };

  f32x4 sc[2][4][3];
  f32x4 negm[3];
  unsigned pw[2][3][4];
  bf16x8 ones = {0x3F80, 0x3F80, 0x3F80, 0x3F80, 0x3F80, 0x3F80, 0x3F80, 0x3F80};
  asm volatile("" : "+v"(ones));

#define EXP1(X, kb, q, r) do { sc[X][kb][q][r] = __builtin_amdgcn_exp2f(sc[X][kb][q][r]); PINF(sc[X][kb][q][r]); } while (0)
#define CVT1(X, c) do { constexpr int kb_ = (c) / 6, q_ = ((c) % 6) >> 1, hf_ = (c) & 1; \
    pw[kb_ >> 1][q_][2 * (kb_ & 1) + hf_] = cvtpk(sc[X][kb_][q_][2 * hf_], sc[X][kb_][q_][2 * hf_ + 1]); PINU(pw[kb_ >> 1][q_][2 * (kb_ & 1) + hf_]); } while (0)

  auto conv = [&](auto S, auto Q) AI {
    constexpr int s = S.value, q = Q.value;
    const f32x4 X = qa[2 * s][q] * QSCALE, Y = qa[2 * s + 1][q] * QSCALE;
    unsigned x0 = cvtpk(X[0], X[1]), x1 = cvtpk(X[2], X[3]), y0 = cvtpk(Y[0], Y[1]), y1 = cvtpk(Y[2], Y[3]);
    auto r0 = __builtin_amdgcn_permlane32_swap(x0, y0, false, false);
    auto r1 = __builtin_amdgcn_permlane32_swap(x1, y1, false, false);
    auto t0 = __builtin_amdgcn_permlane16_swap(r0[0], r0[1], false, false);
    auto t1 = __builtin_amdgcn_permlane16_swap(r1[0], r1[1], false, false);
    AWR(AQ(s, q) + 0, t0[0]); AWR(AQ(s, q) + 1, t1[0]); AWR(AQ(s, q) + 2, t0[1]); AWR(AQ(s, q) + 3, t1[1]);
  };

  sfor<6>([&](auto N) AI { px(0, 0, N); }); sfor<6>([&](auto N) AI { pw_(0, 1, N); });
  sfor<6>([&](auto N) AI { px(1, 2, N); }); sfor<6>([&](auto N) AI { pw_(1, 3, N); });
  zero_qa();
#pragma unroll 1
  for (int c = 0; c < 8; ++c) p1step(c, IC<0>{}, IC<-1>{});
  p1step(8, IC<1>{}, IC<-1>{}); p1step(9, IC<1>{}, IC<-1>{});

#pragma unroll 1
  for (int it = 0;; ++it) {
    asm volatile("" : "+s"(wg));
    if (it == 0) xg += (size_t)4 * SQ * DM * 2;
    WAITV_BAR(18);
    {
      unsigned ak[2];
      ak[0] = adr<0>(kb0, 2 * SLOT); ak[1] = adr<64>(kb0, 2 * SLOT);
      bf16x8 kf[3];
      auto rdk = [&](auto F) AI { constexpr int f = F.value, s = f >> 2, kb = f & 3; kf[f % 3] = ldsb128(ak[s & 1] + kb * 6144 + (s >> 1) * 128); };
      NOPS(7);
      SBAR();
      conv(IC<0>{}, IC<0>{}); conv(IC<0>{}, IC<1>{}); conv(IC<0>{}, IC<2>{});
      conv(IC<1>{}, IC<0>{}); conv(IC<1>{}, IC<1>{}); conv(IC<1>{}, IC<2>{});
      SBAR();
      rdk(IC<0>{}); rdk(IC<1>{});
      NOPS(1);
      SBAR();
      sfor<24>([&](auto F) AI {
        constexpr int f = F.value, s = f >> 2, kb = f & 3;
        if constexpr (f + 2 < 24) rdk(IC<f + 2>{});
        sfor<3>([&](auto Q) AI {
          constexpr int q = Q.value;
          if constexpr (s == 0) { mfma_s0_<AQ(s, q)>(sc[0][kb][q], kf[f % 3]); } else { MFMA_S(sc[0][kb][q], kf[f % 3], s, q); }
        });
        if constexpr (s < 4 && kb < 3) conv(IC<s + 2>{}, IC<kb>{});
        if constexpr (kb == 3) { pf_(IC<24 + s / 3>{}, IC<2 * (s % 3)>{}); pf_(IC<24 + s / 3>{}, IC<2 * (s % 3) + 1>{}); }
        SBAR();
      });
      asm volatile("s_nop 7" : "+v"(sc[0][0][0]), "+v"(sc[0][0][1]), "+v"(sc[0][0][2]), "+v"(sc[0][1][0]), "+v"(sc[0][1][1]), "+v"(sc[0][1][2]),
                               "+v"(sc[0][2][0]), "+v"(sc[0][2][1]), "+v"(sc[0][2][2]), "+v"(sc[0][3][0]), "+v"(sc[0][3][1]), "+v"(sc[0][3][2]));
#pragma unroll
      for (int q = 0; q < 3; ++q) {
        float m0 = mx3(sc[0][0][q][0], sc[0][0][q][1], sc[0][0][q][2]);
        m0 = mx2(m0, sc[0][0][q][3]);
#pragma unroll
        for (int kb = 1; kb < 4; ++kb) { m0 = mx3(m0, sc[0][kb][q][0], sc[0][kb][q][1]); m0 = mx3(m0, sc[0][kb][q][2], sc[0][kb][q][3]); }
        m0 = xmax(m0);
        negm[q] = f32x4{-m0, -m0, -m0, -m0};
#pragma unroll
        for (int kb = 0; kb < 4; ++kb) sc[0][kb][q] -= m0;
#pragma unroll
        for (int r = 0; r < 4; ++r) sc[0][0][q][r] = __builtin_amdgcn_exp2f(sc[0][0][q][r]);
      }
    }
    sfor<8>([&](auto Jc) AI {
      constexpr int j = Jc.value;
      constexpr int X = j & 1, Y = X ^ 1;
      if constexpr (j == 0 || j == 7) { WAITV_BAR(18); } else { WAITV_BAR(12); }
      if constexpr (j < 7) {
        unsigned ak[2];
        ak[0] = adr<0>(kb0, ((22 + 2 * j) % NSLOT) * SLOT); ak[1] = adr<64>(kb0, ((22 + 2 * j) % NSLOT) * SLOT);
        pin3n_(negm[0], negm[1], negm[2]);
        bf16x8 kf[3];
        auto rdk = [&](auto F) AI { constexpr int f = F.value, s = f >> 2, kb = f & 3; kf[f % 3] = ldsb128(ak[s & 1] + kb * 6144 + (s >> 1) * 128); };
        rdk(IC<0>{}); rdk(IC<1>{});
        SBAR();
        sfor<24>([&](auto F) AI {
          constexpr int f = F.value, s = f >> 2, kb = f & 3;
          if constexpr (f + 2 < 24) rdk(IC<f + 2>{});
          sfor<3>([&](auto Q) AI {
            constexpr int q = Q.value;
            if constexpr (s == 0) { MFMA_SC(sc[Y][kb][q], kf[f % 3], s, q, negm[q]); } else { MFMA_S(sc[Y][kb][q], kf[f % 3], s, q); }
            if constexpr (q == 0) { CVT1(X, f); }
            else {
              constexpr int e = 2 * f + q - 1;
              if constexpr (e < 36) { EXP1(X, 1 + e / 12, (e % 12) >> 2, e & 3); }
              else if constexpr (q == 1 && j >= 1) pf_(IC<25 + 2 * j>{}, IC<f - 18>{});
            }
            SBAR();
          });
        });
      } else {
        sfor<36>([&](auto E) AI { constexpr int e = E.value; EXP1(X, 1 + e / 12, (e % 12) >> 2, e & 3);
          if constexpr ((e % 6) == 5) pf_(IC<39>{}, IC<e / 6>{}); });
        sfor<24>([&](auto C) AI { CVT1(X, C.value); });
      }
      {
        bf16x8 pf[2][3];
#pragma unroll
        for (int t = 0; t < 2; ++t)
#pragma unroll
          for (int q = 0; q < 3; ++q) { u32x4 f = {pw[t][q][0], pw[t][q][1], pw[t][q][2], pw[t][q][3]}; pf[t][q] = __builtin_bit_cast(bf16x8, f); }
        pin3n_(pf[0][0], pf[0][1], pf[0][2]); pin3n_(pf[1][0], pf[1][1], pf[1][2]);
        unsigned av[4];
        { constexpr unsigned so = ((21 + 2 * j) % NSLOT) * SLOT;
          av[0] = adr<0>(vb0, so); av[1] = adr<32>(vb0, so); av[2] = adr<64>(vb0, so); av[3] = adr<96>(vb0, so); }
        bf16x8 vf[3];
        auto rdv = [&](auto F) AI {
          constexpr int f = F.value, t = f / 12, d = f % 12;
          const unsigned p = av[d & 3] + t * 32 * 384 + (d >> 2) * 128;
          vf[f % 3] = cat8(ldstr(p), ldstr(p + 16 * 384));
        };
        float mx[3], fct[3];
        bool pend = false;
        auto mop = [&](auto I) AI {
          constexpr int q = I.value % 3, k = I.value / 3;
          if constexpr (k == 0) mx[q] = mx2(sc[Y][0][q][0], sc[Y][0][q][1]);
          else { constexpr int e = 2 * k; mx[q] = mx3c(mx[q], sc[Y][e >> 2][q][e & 3], sc[Y][e >> 2][q][(e & 3) + 1]); }
          PINF(mx[q]);
        };
        rdv(IC<0>{}); rdv(IC<1>{});
        SBAR();
        sfor<78>([&](auto Mc) AI {
          constexpr int m = Mc.value, G = m / 3, q = m % 3, t = G / 13, gi = G % 13;
          if constexpr (gi == 0) {
            if constexpr (j == 0 && t == 0) { MFMA_O0(12, q, ones, pf[t][q]); } else { MFMA_O(12, q, ones, pf[t][q]); }
          } else {
            constexpr int d = gi - 1, f = 12 * t + d;
            if constexpr (q == 0 && f + 2 < 24) rdv(IC<f + 2>{});
            if constexpr (j == 0 && t == 0) { MFMA_O0(d, q, vf[f % 3], pf[t][q]); } else { MFMA_O(d, q, vf[f % 3], pf[t][q]); }
          }
          if constexpr (j < 7 && q < 2) {
            if constexpr (G >= 2 && G < 8) { constexpr int i = 4 * (G - 2) + 2 * q; mop(IC<i>{}); mop(IC<i + 1>{}); }
            if constexpr (G == 8 && q == 0) {
              pend = __any(mx3c(mx[0], mx[1], mx[2]) > THR);
              if (__builtin_expect(pend, 0)) {
#pragma unroll
                for (int qq_ = 0; qq_ < 3; ++qq_) {
                  const float gr = xmax(mx[qq_]);
                  const unsigned db = (__float_as_uint(fmaxf(gr, 0.f)) + 0xFFFFu) & 0xFFFF0000u;
                  fct[qq_] = __builtin_amdgcn_exp2f(-__uint_as_float(db));
                  u32x4 bw = {g == 0 ? ((db >> 16) | 0x8000u) : 0u, 0u, 0u, 0u};
                  bf16x8 bfr = __builtin_bit_cast(bf16x8, bw);
                  asm volatile("s_nop 1" : "+v"(bfr));
                  mfma_ip_(negm[qq_], ones, bfr);
                  mfma_ip_(sc[Y][0][qq_], ones, bfr); mfma_ip_(sc[Y][1][qq_], ones, bfr); mfma_ip_(sc[Y][2][qq_], ones, bfr); mfma_ip_(sc[Y][3][qq_], ones, bfr);
                }
                NOPS(7);
              }
            }
            if constexpr (G >= 9 && G < 15) { constexpr int e = 2 * (G - 9) + q; EXP1(Y, 0, e >> 2, e & 3); }
          }
          if constexpr (j < 7 && q == 1 && G >= 16 && G < 22) pf_(IC<26 + 2 * j>{}, IC<G - 16>{});
          SBAR();
        });
        if constexpr (j < 7) {
          if (__builtin_expect(pend, 0)) {
            NOPS(7);
            sfor<156>([&](auto N) AI { ASCALE(N.value, fct[(N.value >> 2) % 3]); });
            NOPS(1);
          }
        }
      }
    });
    if (it == 1) break;
    kg += (size_t)4 * NH * SK * DH * 2; vg += (size_t)4 * NH * SK * DH * 2;
    zero_qa();
    sl = 0;
    sfor<10>([&](auto C) AI { p1step(C.value, IC<(C.value >= 8)>{}, IC<C.value>{}); });
    og += (size_t)4 * 192 * 12 * 1024;
  }
  NOPS(7);
  sfor<18>([&](auto U) AI { epi(U, IC<0>{}); epi(U, IC<1>{}); epi(U, IC<2>{}); epi(U, IC<3>{}); });
  asm volatile("s_waitcnt vmcnt(0)" ::: "memory");
}

constexpr int ST2 = 128 * 320;
constexpr int LDS2 = 3 * ST2;
constexpr size_t WS_SLAB = (size_t)64 << 20;
constexpr size_t WS_CNT = (size_t)48 << 20;

DI void gload16(bf16x8& d, unsigned voff, const char* sbase) { asm volatile("global_load_dwordx4 %0, %1, %2 nt" : "=v"(d) : "v"(voff), "s"(sbase) : "memory"); }
template <int N> DI void wait3(bf16x8& a, bf16x8& b, bf16x8& c) { asm volatile("s_waitcnt vmcnt(%3)" : "+v"(a), "+v"(b), "+v"(c) : "i"(N) : "memory"); }

__global__ __launch_bounds__(512, 2) void k_oproj(Ptrs P, char* __restrict__ ws, char* __restrict__ out) {
  extern __shared__ __attribute__((aligned(16))) char smem[];
  lds_ptr lds = (lds_ptr)smem;
  const int tid = threadIdx.x, l = tid & 63, w = __builtin_amdgcn_readfirstlane(tid >> 6);
  const int i16 = l & 15, g = l >> 4, qq = i16 >> 2, pp = i16 & 3;
  const int xcd = blockIdx.x & 7, idx = blockIdx.x >> 3;
  const int br = xcd >> 2, ks = xcd & 3, b = idx >> 2, nq = idx & 3;
  const int kh = w >> 2, qg = w & 3;
  const unsigned lds0 = (unsigned)(uintptr_t)lds;
  const char* wsrc = P.wo[br] + (size_t)ks * 1536 * DM * 2 + nq * 320;
  const char* osrc = ws + (((size_t)(br * NB + b) * 192 + 48 * ks + kh) * 12 + 3 * qg) * 1024;
  const unsigned l16 = l * 16;
  unsigned offW[5];
#pragma unroll
  for (int u = 0; u < 5; ++u) {
    const int o = 1024 * u + 16 * l, r = o / 320, chp = (o % 320) >> 4;
    offW[u] = (16 * w + r) * (DM * 2) + ((chp ^ (((r >> 2) & 1) << 1)) << 4);
  }
  const unsigned o0 = offW[0], o1 = offW[1], o2 = offW[2], o3 = offW[3], o4 = offW[4];
  const int s0 = 0;
  auto wpiece = [&](const char* src, unsigned d, auto Uc) AI {
    constexpr int u = Uc.value;
    glds16(u == 0 ? o0 : u == 1 ? o1 : u == 2 ? o2 : u == 3 ? o3 : o4, src, d + u * 1024);
  };
  auto wsrc_of = [&](int s) AI { s += s0; s = s >= 12 ? s - 12 : s; return wsrc + (size_t)s * 128 * DM * 2; };
  auto osrc_of = [&](int u) AI { u += 2 * s0; u = u >= 24 ? u - 24 : u; return osrc + (size_t)u * 24576; };
  auto issueW = [&](int s, int stg) AI {
    const unsigned d = lds0 + stg * ST2 + w * 5120;
    const char* src = wsrc_of(s);
    sfor<5>([&](auto U) AI { wpiece(src, d, U); });
  };
  bf16x8 of[4][3];
  auto loadO = [&](int u, auto Bc) AI {
    constexpr int B_ = Bc.value;
    const char* src = osrc_of(u);
    gload16(of[B_][0], l16, src); gload16(of[B_][1], l16, src + 1024); gload16(of[B_][2], l16, src + 2048);
  };
  f32x4 acc[10][3];
#pragma unroll
  for (int n = 0; n < 10; ++n)
#pragma unroll
    for (int q = 0; q < 3; ++q) acc[n][q] = f32x4{0.f, 0.f, 0.f, 0.f};
  const unsigned wb0 = lds0 + (32 * kh + 4 * g + qq) * 320 + 8 * pp + ((g & 1) << 5);
  {
    unsigned tch;
    const char* wo = P.wo[br] + (size_t)ks * 1536 * DM * 2 + (size_t)(idx * 8 + w) * 60 * 128;
    asm volatile("global_load_dword %0, %1, %2" : "=v"(tch) : "v"((l < 60 ? l : 59) * 128), "s"(wo) : "memory");
    issueW(0, 0); loadO(0, IC<0>{}); loadO(1, IC<1>{}); loadO(2, IC<2>{});
    issueW(1, 1);
    asm volatile("s_waitcnt vmcnt(19)" : "+v"(tch) :: "memory");
  }
  int stg = 0;
  auto kstep = [&](unsigned ae, unsigned ao, auto Vc, auto Bc, auto Lc, const char* lsrc, const char* dsrc, unsigned dd, auto Dc) AI {
    constexpr int v = Vc.value, B_ = Bc.value, LB = Lc.value, DN = Dc.value;
    bf16x8 wf[3];
    auto rdw = [&](auto N) AI { constexpr int n = N.value; const unsigned p = ((n & 1) ? ao : ae) + v * 64 * 320 + (n >> 1) * 64; wf[n % 3] = cat8(ldstr(p), ldstr(p + 16 * 320)); };
    rdw(IC<0>{}); rdw(IC<1>{});
    SBAR();
    sfor<10>([&](auto N) AI {
      constexpr int n = N.value;
      if constexpr (n + 2 < 10) rdw(IC<n + 2>{});
      acc[n][0] = MFMA16(wf[n % 3], of[B_][0], acc[n][0]);
      if constexpr (LB >= 0 && n < 3) { SBAR(); gload16(of[LB][n], l16, lsrc + n * 1024); SBAR(); }
      if constexpr (DN > 0 && n >= 4 && n < 4 + 2 * DN && (n & 1) == 0) { SBAR(); wpiece(dsrc, dd, IC<(v ? 2 : 0) + (n - 4) / 2>{}); SBAR(); }
      acc[n][1] = MFMA16(wf[n % 3], of[B_][1], acc[n][1]);
      acc[n][2] = MFMA16(wf[n % 3], of[B_][2], acc[n][2]);
      SBAR();
    });
  };
  auto stage = [&](int s, auto Pc, auto Mc) AI {
    constexpr int PAR = Pc.value, MODE = Mc.value, B0 = 2 * PAR, B1 = 2 * PAR + 1;
    if constexpr (MODE == 2) { asm volatile("s_waitcnt vmcnt(3) lgkmcnt(0)\n\ts_barrier" ::: "memory"); }
    else { asm volatile("s_waitcnt vmcnt(11) lgkmcnt(0)\n\ts_barrier" ::: "memory"); }
    wait3<63>(of[B0][0], of[B0][1], of[B0][2]);
    const unsigned ae = opq(wb0 + stg * ST2), ao = opq((wb0 ^ 32) + stg * ST2);
    const unsigned dd = lds0 + (stg == 0 ? 2 : stg - 1) * ST2 + w * 5120;
    const char* dsrc = wsrc_of(s + 2);
    kstep(ae, ao, IC<0>{}, IC<B0>{}, IC<(MODE <= 1 ? ((B0 + 3) & 3) : -1)>{}, osrc_of(2 * s + 3), dsrc, dd, IC<(MODE == 0 ? 2 : 0)>{});
    wait3<MODE == 0 ? 13 : MODE == 1 ? 11 : 0>(of[B1][0], of[B1][1], of[B1][2]);
    kstep(ae, ao, IC<1>{}, IC<B1>{}, IC<(MODE == 0 ? B0 : -1)>{}, osrc_of(2 * s + 4), dsrc, dd, IC<(MODE == 0 ? 3 : 0)>{});
    stg = stg == 2 ? 0 : stg + 1;
  };
#pragma unroll 1
  for (int s = 0; s < 10; s += 2) { stage(s, IC<0>{}, IC<0>{}); stage(s + 1, IC<1>{}, IC<0>{}); }
  stage(10, IC<0>{}, IC<1>{}); stage(11, IC<1>{}, IC<2>{});
  asm volatile("s_waitcnt vmcnt(0) lgkmcnt(0)\n\ts_barrier" ::: "memory");
  {
    const unsigned xo = lds0 + ((kh ^ 1) * 4 + qg) * 15360 + l16, xi = lds0 + (kh * 4 + qg) * 15360 + l16;
#pragma unroll
    for (int n = 0; n < 5; ++n)
#pragma unroll
      for (int q = 0; q < 3; ++q) *(__attribute__((address_space(3))) f32x4*)(xo + (n * 3 + q) * 1024) = kh ? acc[n][q] : acc[5 + n][q];
    __syncthreads();
#pragma unroll
    for (int n = 0; n < 5; ++n)
#pragma unroll
      for (int q = 0; q < 3; ++q) {
        const f32x4 o = *(const __attribute__((address_space(3))) f32x4*)(xi + (n * 3 + q) * 1024);
        acc[n][q] = (kh ? acc[5 + n][q] : acc[n][q]) + o;
      }
  }
  const int tile = (br * NB + b) * 4 + nq;
  char* slab = ws + WS_SLAB + (size_t)tile * 4 * 8 * 15360;
  {
    char* mine = slab + (size_t)(ks * 8 + w) * 15360 + l16;
#pragma unroll
    for (int i = 0; i < 15; ++i)
      if ((i & 3) != ks) asm volatile("global_store_dwordx4 %0, %1, off sc0 sc1\n\ts_nop 1" :: "v"(mine + i * 1024), "v"(acc[i / 3][i % 3]) : "memory");
    asm volatile("s_waitcnt vmcnt(0)" ::: "memory");
    __syncthreads();
    unsigned* cnt = (unsigned*)(ws + WS_CNT) + tile * 16;
    if (tid == 0) {
      __hip_atomic_fetch_add(cnt, 1u, __ATOMIC_RELAXED, __HIP_MEMORY_SCOPE_AGENT);
      unsigned spins = 0;
      while (__hip_atomic_load(cnt, __ATOMIC_RELAXED, __HIP_MEMORY_SCOPE_AGENT) < 4u && ++spins < (1u << 24)) __builtin_amdgcn_s_sleep(1);
    }
    __syncthreads();
  }
  {
    const char* theirs = slab + (size_t)w * 15360 + l16;
    f32x4 p[4][3];
#pragma unroll
    for (int m = 0; m < 4; ++m) {
      const int i = (4 * m + ks) < 15 ? 4 * m + ks : 11;
#pragma unroll
      for (int r = 0; r < 3; ++r) {
        const int src = (ks + 1 + r) & 3;
        asm volatile("global_load_dwordx4 %0, %1, off sc0 sc1" : "=v"(p[m][r]) : "v"(theirs + (size_t)src * 8 * 15360 + i * 1024) : "memory");
      }
    }
    asm volatile("s_waitcnt vmcnt(0)" : "+v"(p[0][0]), "+v"(p[0][1]), "+v"(p[0][2]), "+v"(p[1][0]), "+v"(p[1][1]), "+v"(p[1][2]),
                                        "+v"(p[2][0]), "+v"(p[2][1]), "+v"(p[2][2]), "+v"(p[3][0]), "+v"(p[3][1]), "+v"(p[3][2]) :: "memory");
#pragma unroll
    for (int i = 0; i < 15; ++i) {
      if ((i & 3) == ks) {
        const f32x4 o = (acc[i / 3][i % 3] + p[i >> 2][0]) + (p[i >> 2][1] + p[i >> 2][2]);
        u32x2 f = {cvtpk(o[0], o[1]), cvtpk(o[2], o[3])};
        char* dst = out + ((size_t)(b * SQ + (3 * qg + i % 3) * 16 + i16) * (2 * DM) + br * DM + nq * 160 + (5 * kh + i / 3) * 16 + 4 * g) * 2;
        *(u32x2*)dst = f;
      }
    }
  }
}

extern "C" void kernel_launch(void* const* d_in, const int* in_sizes, int n_in, void* d_out, int out_size, void* d_ws, size_t ws_size,
                              hipStream_t stream) {
  Ptrs P;
  for (int r = 0; r < 2; ++r) {
    P.x[r] = (const char*)d_in[5 * r + 0];
    P.k[r] = (const char*)d_in[5 * r + 1];
    P.v[r] = (const char*)d_in[5 * r + 2];
    P.wq[r] = (const char*)d_in[5 * r + 3];
    P.wo[r] = (const char*)d_in[5 * r + 4];
  }
  (void)hipFuncSetAttribute((const void*)k_attn, hipFuncAttributeMaxDynamicSharedMemorySize, LDS1);
  (void)hipFuncSetAttribute((const void*)k_oproj, hipFuncAttributeMaxDynamicSharedMemorySize, LDS2);
  hipLaunchKernelGGL(k_attn, dim3(256), dim3(256), LDS1, stream, P, (char*)d_ws);
  hipLaunchKernelGGL(k_oproj, dim3(256), dim3(512), LDS2, stream, P, (char*)d_ws, (char*)d_out);
}
```

```cpp
#include <hip/hip_runtime.h>
#include <hip/hip_bf16.h>
#include <stdint.h>
#include <utility>

using bf16x8 = __attribute__((ext_vector_type(8))) short;
using s16x4 = __attribute__((ext_vector_type(4))) short;
using f32x4 = __attribute__((ext_vector_type(4))) float;
using u32x4 = __attribute__((ext_vector_type(4))) unsigned;
using u32x2 = __attribute__((ext_vector_type(2))) unsigned;
typedef short v4i16_t __attribute__((ext_vector_type(4)));
typedef __attribute__((address_space(3))) char* lds_ptr;
typedef __attribute__((address_space(3))) const char* lds_cptr;

constexpr int NB = 8, SQ = 192, DM = 640, NH = 32, SK = 512, DH = 192;
constexpr int HD = NH * DH;
constexpr int SLOT = 24576;
constexpr int NSLOT = 6;
constexpr int LDS1 = SLOT * NSLOT;
constexpr float QSCALE = 0.07216878364870322f * 1.4426950408889634f;
constexpr float THR = 8.0f;

#define DI __device__ __forceinline__
#define MFMA16(a, b, c) __builtin_amdgcn_mfma_f32_16x16x32_bf16((a), (b), (c), 0, 0, 0)
#define ACLOB "a0","a1","a2","a3","a4","a5","a6","a7","a8","a9","a10","a11","a12","a13","a14","a15","a16","a17","a18","a19","a20","a21","a22","a23","a24","a25","a26","a27","a28","a29","a30","a31","a32","a33","a34","a35","a36","a37","a38","a39","a40","a41","a42","a43","a44","a45","a46","a47","a48","a49","a50","a51","a52","a53","a54","a55","a56","a57","a58","a59","a60","a61","a62","a63","a64","a65","a66","a67","a68","a69","a70","a71","a72","a73","a74","a75","a76","a77","a78","a79","a80","a81","a82","a83","a84","a85","a86","a87","a88","a89","a90","a91","a92","a93","a94","a95","a96","a97","a98","a99","a100","a101","a102","a103","a104","a105","a106","a107","a108","a109","a110","a111","a112","a113","a114","a115","a116","a117","a118","a119","a120","a121","a122","a123","a124","a125","a126","a127","a128","a129","a130","a131","a132","a133","a134","a135","a136","a137","a138","a139","a140","a141","a142","a143","a144","a145","a146","a147","a148","a149","a150","a151","a152","a153","a154","a155","a156","a157","a158","a159","a160","a161","a162","a163","a164","a165","a166","a167","a168","a169","a170","a171","a172","a173","a174","a175","a176","a177","a178","a179","a180","a181","a182","a183","a184","a185","a186","a187","a188","a189","a190","a191","a192","a193","a194","a195","a196","a197","a198","a199","a200","a201","a202","a203","a204","a205","a206","a207","a208","a209","a210","a211","a212","a213","a214","a215","a216","a217","a218","a219","a220","a221","a222","a223","a224","a225","a226","a227"
#define AO(d, q) (4 * (3 * (d) + (q)))
#define AQ(s, q) (156 + 4 * (3 * (s) + (q)))
template <int N> DI void mfma_o_(const bf16x8& a, const bf16x8& b) { asm volatile("v_mfma_f32_16x16x32_bf16 a[%c2:%c3], %0, %1, a[%c2:%c3]" :: "v"(a), "v"(b), "i"(N), "i"(N + 3) : ACLOB); }
template <int N> DI void mfma_o0_(const bf16x8& a, const bf16x8& b) { asm volatile("v_mfma_f32_16x16x32_bf16 a[%c2:%c3], %0, %1, 0" :: "v"(a), "v"(b), "i"(N), "i"(N + 3) : ACLOB); }
template <int N> DI void mfma_s_(f32x4& acc, const bf16x8& a) { asm volatile("v_mfma_f32_16x16x32_bf16 %0, %1, a[%c2:%c3], %0" : "+v"(acc) : "v"(a), "i"(N), "i"(N + 3) : ACLOB); }
template <int N> DI void mfma_sc_(f32x4& acc, const bf16x8& a, const f32x4& c) { asm volatile("v_mfma_f32_16x16x32_bf16 %0, %1, a[%c2:%c3], %4" : "=&v"(acc) : "v"(a), "i"(N), "i"(N + 3), "v"(c) : ACLOB); }
template <int N> DI void mfma_s0_(f32x4& acc, const bf16x8& a) { asm volatile("v_mfma_f32_16x16x32_bf16 %0, %1, a[%c2:%c3], 0" : "=&v"(acc) : "v"(a), "i"(N), "i"(N + 3) : ACLOB); }
template <int N> DI float ard_() { float r; asm volatile("v_accvgpr_read_b32 %0, a[%c1]" : "=v"(r) : "i"(N)); return r; }
template <int N, class T> DI void awr_(T src) { asm volatile("v_accvgpr_write_b32 a[%c1], %0" :: "v"(src), "i"(N)); }
template <int N> DI void ascale_(float f) { float t; asm volatile("v_accvgpr_read_b32 %0, a[%c1]\n\tv_mul_f32 %0, %2, %0\n\tv_accvgpr_write_b32 a[%c1], %0" : "=&v"(t) : "i"(N), "v"(f)); }
DI float subip_(float x, float d) { asm volatile("v_sub_f32 %0, %0, %1" : "+v"(x) : "v"(d)); return x; }
DI void mfma_ip_(f32x4& x, const bf16x8& a, const bf16x8& b) { asm volatile("v_mfma_f32_16x16x32_bf16 %0, %1, %2, %0" : "+v"(x) : "v"(a), "v"(b) : ACLOB); }
template <class T> DI T pin_(T x) { asm volatile("" : "+v"(x)); return x; }
template <class T> DI void pin3n_(T& a, T& b, T& c) { asm volatile("s_nop 1" : "+v"(a), "+v"(b), "+v"(c)); }
#define MFMA_O(d, q, a, b) mfma_o_<AO(d, q)>(a, b)
#define MFMA_O0(d, q, a, b) mfma_o0_<AO(d, q)>(a, b)
#define MFMA_S(acc, a, s, q) mfma_s_<AQ(s, q)>(acc, a)
#define MFMA_SC(acc, a, s, q, c) mfma_sc_<AQ(s, q)>(acc, a, c)
#define ARD(dst, n) dst = ard_<n>()
#define AWR(n, src) awr_<n>(src)
#define ASCALE(n, f) ascale_<n>(f)
#define SUBIP(x, d) x = subip_(x, d)
#define NOPS(n) asm volatile("s_nop " #n ::: "memory")
#define SBAR() __builtin_amdgcn_sched_barrier(0)
#define PINF(x) x = pin_(x)
#define PINU(x) x = pin_(x)
#define WAITV_BAR(N) asm volatile("s_waitcnt vmcnt(" #N ") lgkmcnt(0)\n\ts_barrier" ::: "memory")

DI unsigned cvtpk(float lo, float hi) {
  typedef float f32x2 __attribute__((ext_vector_type(2)));
  typedef __bf16 bf16x2 __attribute__((ext_vector_type(2)));
  f32x2 v = {lo, hi}; bf16x2 b = __builtin_convertvector(v, bf16x2); return __builtin_bit_cast(unsigned, b);
}
DI void glds16(unsigned voff, const char* sbase, unsigned ldsaddr) {
  asm volatile("s_mov_b32 m0, %2\n\ts_nop 0\n\tglobal_load_lds_dwordx4 %0, %1" :: "v"(voff), "s"(sbase), "s"(ldsaddr) : "memory");
}
DI void glds16nt(unsigned voff, const char* sbase, unsigned ldsaddr) {
  asm volatile("s_mov_b32 m0, %2\n\ts_nop 0\n\tglobal_load_lds_dwordx4 %0, %1 nt" :: "v"(voff), "s"(sbase), "s"(ldsaddr) : "memory");
}
DI bf16x8 ldsb128(unsigned p) { return *(const __attribute__((address_space(3))) bf16x8*)p; }
DI s16x4 ldstr(unsigned p) { return __builtin_bit_cast(s16x4, __builtin_amdgcn_ds_read_tr16_b64_v4i16((__attribute__((address_space(3))) v4i16_t*)p)); }
template <int X> DI unsigned adr(unsigned base, unsigned off) {
  unsigned r;
  if constexpr (X == 0) asm volatile("v_add_u32 %0, %2, %1" : "=v"(r) : "v"(base), "s"(off));
  else asm volatile("v_xor_b32 %0, %3, %1\n\tv_add_u32 %0, %2, %0" : "=&v"(r) : "v"(base), "s"(off), "i"(X));
  return r;
}
DI unsigned opq(unsigned x) { asm volatile("" : "+v"(x)); return x; }
DI bf16x8 cat8(s16x4 lo, s16x4 hi) { return __builtin_shufflevector(lo, hi, 0, 1, 2, 3, 4, 5, 6, 7); }
DI float mx3(float a, float b, float c) { float r; asm("v_max3_f32 %0, %1, %2, %3" : "=v"(r) : "v"(a), "v"(b), "v"(c)); return r; }
DI float mx3c(float a, float b, float c) { return __builtin_fmaxf(__builtin_fmaxf(a, b), c); }
DI float mx2(float a, float b) { float r; asm("v_max_f32_e32 %0, %1, %2" : "=v"(r) : "v"(a), "v"(b)); return r; }
DI float xmax(float v) {
  auto a = __builtin_amdgcn_permlane32_swap(__float_as_uint(v), __float_as_uint(v), false, false);
  v = mx2(__uint_as_float(a[0]), __uint_as_float(a[1]));
  auto b = __builtin_amdgcn_permlane16_swap(__float_as_uint(v), __float_as_uint(v), false, false);
  return mx2(__uint_as_float(b[0]), __uint_as_float(b[1]));
}
DI float xsum(float v) {
  auto a = __builtin_amdgcn_permlane32_swap(__float_as_uint(v), __float_as_uint(v), false, false);
  v = __uint_as_float(a[0]) + __uint_as_float(a[1]);
  auto b = __builtin_amdgcn_permlane16_swap(__float_as_uint(v), __float_as_uint(v), false, false);
  return __uint_as_float(b[0]) + __uint_as_float(b[1]);
}

template <int N> struct IC { static constexpr int value = N; };
#define AI __attribute__((always_inline))
template <class F, int... Is> DI void sfor_(F&& f, std::integer_sequence<int, Is...>) { (f(IC<Is>{}), ...); }
template <int N, class F> DI void sfor(F&& f) { sfor_(f, std::make_integer_sequence<int, N>{}); }
struct Ptrs { const char* x[2]; const char* k[2]; const char* v[2]; const char* wq[2]; const char* wo[2]; };

__global__ __launch_bounds__(256, 1) void k_attn(Ptrs P, char* __restrict__ ofr) {
  extern __shared__ __attribute__((aligned(16))) char smem[];
  lds_ptr lds = (lds_ptr)smem;
  const int tid = threadIdx.x, l = tid & 63, w = __builtin_amdgcn_readfirstlane(tid >> 6);
  const int i16 = l & 15, g = l >> 4, qq = i16 >> 2, pp = i16 & 3;
  const int xcd = blockIdx.x & 7, idx = blockIdx.x >> 3;
  const int br = xcd >> 2, h = 8 * (xcd & 3) + (idx & 7), b0 = idx >> 3;
  const char* xg = P.x[br] + (size_t)b0 * SQ * DM * 2;
  const char* wg = P.wq[br] + (size_t)h * DH * 2;
  const char* kg = P.k[br] + (size_t)(b0 * NH + h) * SK * DH * 2;
  const char* vg = P.v[br] + (size_t)(b0 * NH + h) * SK * DH * 2;
  char* og = ofr + (((size_t)(br * NB + b0) * 192 + h * 6) * 12 + 3 * w) * 1024 + l * 16;

  if (blockIdx.x == 0 && tid < 64) ((unsigned*)(ofr + ((size_t)48 << 20)))[tid * 16] = 0u;
  unsigned offK[3], offV[3], offW[3], offX;
#pragma unroll
  for (int u = 0; u < 3; ++u) {
    const int o = 1024 * u + 16 * l, r = o / 384, chp = (o % 384) >> 4, row = 8 * w + r;
    const int aK = (row >> 1) & 7;
    offK[u] = row * 384 + (((chp & ~7) | ((chp ^ aK) & 7)) << 4);
    offV[u] = row * 384 + ((chp ^ (((row >> 1) & 3) << 1)) << 4);
    const int sW = ((row >> 1) & 1) | (((row >> 3) & 1) << 1);
    offW[u] = row * (HD * 2) + ((chp ^ (sW << 1)) << 4);
  }
  { const int row = 8 * w + (l >> 3), a = (row >> 1) & 7; offX = row * (DM * 2) + (((l & 7) ^ a) << 4); }

  const unsigned oK0 = offK[0], oK1 = offK[1], oK2 = offK[2], oV0 = offV[0], oV1 = offV[1], oV2 = offV[2], oW0 = offW[0], oW1 = offW[1], oW2 = offW[2];
  const unsigned lds0 = (unsigned)(uintptr_t)lds;
  auto px = [&](int c, int slot, auto N) AI {
    constexpr int n = N.value;
    glds16(offX, xg + c * 128 + (size_t)n * 32 * DM * 2, lds0 + slot * SLOT + w * 1024 + n * 4096);
  };
  auto pw_ = [&](int c, int slot, auto N) AI {
    constexpr int n = N.value, u = n % 3, hf = n / 3;
    glds16(u == 0 ? oW0 : u == 1 ? oW1 : oW2, wg + (size_t)(c * 64 + hf * 32) * HD * 2, lds0 + slot * SLOT + w * 3072 + hf * 12288 + u * 1024);
  };
  auto pk = [&](int j, int slot, auto N) AI {
    constexpr int n = N.value, u = n % 3, hf = n / 3;
    glds16nt(u == 0 ? oK0 : u == 1 ? oK1 : oK2, kg + (size_t)j * SLOT + hf * 12288, lds0 + slot * SLOT + w * 3072 + hf * 12288 + u * 1024);
  };
  auto pv = [&](int j, int slot, auto N) AI {
    constexpr int n = N.value, u = n % 3, hf = n / 3;
    glds16nt(u == 0 ? oV0 : u == 1 ? oV1 : oV2, vg + (size_t)j * SLOT + hf * 12288, lds0 + slot * SLOT + w * 3072 + hf * 12288 + u * 1024);
  };
  auto pf_ = [&](auto F, auto N) AI {
    constexpr int f = F.value;
    if constexpr (f >= 36) { if constexpr ((f & 1) == 0) px((f - 36) >> 1, f % NSLOT, N); else pw_((f - 36) >> 1, f % NSLOT, N); }
    else if constexpr ((f & 1) == 0) pk((f - 20) >> 1, f % NSLOT, N); else pv((f - 20) >> 1, f % NSLOT, N);
  };

  const int a8 = i16 >> 1;
  const unsigned kb0 = lds0 + i16 * 384 + ((g ^ a8) << 4);
  const unsigned xb0 = lds0 + (48 * w + i16) * 128 + ((g ^ a8) << 4);
  const int swz = 2 * (g & 1) + (qq >> 1);
  const unsigned vb0 = lds0 + (4 * g + qq) * 384 + (swz << 5) + 8 * pp;
  const unsigned wb0 = lds0 + (8 * g + qq) * 384 + (swz << 5) + 8 * pp;

  asm volatile("" ::: ACLOB);
  f32x4 qa[12][3];
  float inv = 0.f; f32x4 eX, eY;
  auto epi = [&](auto U, auto Pt) AI {
    constexpr int u = U.value, q = u / 6, t = u % 6, part = Pt.value;
    if constexpr (part == 0) {
      if constexpr (t == 0) { float lq; ARD(lq, AO(12, q)); inv = __builtin_amdgcn_rcpf(lq); }
      sfor<4>([&](auto R) AI { ARD(eX[R.value], AO(2 * t, q) + R.value); });
    } else if constexpr (part == 1) {
      sfor<4>([&](auto R) AI { ARD(eY[R.value], AO(2 * t + 1, q) + R.value); });
    } else if constexpr (part == 2) { eX *= inv; eY *= inv; PINF(eX); PINF(eY); }
    else if constexpr (part == 3) {
      u32x4 f = {cvtpk(eX[0], eX[1]), cvtpk(eX[2], eX[3]), cvtpk(eY[0], eY[1]), cvtpk(eY[2], eY[3])};
      *(u32x4*)(og + q * 1024 + t * 12288) = f;
    }
  };
  int sl = 0;
  unsigned ax[2], aw[4], ak[2];
  bf16x8 xf[2][3], wf[4], kf[3];
  auto rdw = [&](auto F) AI {
    constexpr int f = F.value, s = f / 12, d = f % 12;
    const unsigned p = aw[d & 3] + s * 32 * 384 + (d >> 2) * 128;
    wf[f & 3] = cat8(ldstr(p), ldstr(p + 4 * 384));
  };
  auto rdk = [&](auto F) AI { constexpr int f = F.value, s = f >> 2, kb = f & 3; kf[f % 3] = ldsb128(ak[s & 1] + kb * 6144 + (s >> 1) * 128); };
  auto p1head = [&]() AI {
    ax[0] = adr<0>(xb0, sl * SLOT); ax[1] = adr<64>(xb0, sl * SLOT);
    aw[0] = adr<0>(wb0, (sl + 1) * SLOT); aw[1] = adr<32>(wb0, (sl + 1) * SLOT); aw[2] = adr<64>(wb0, (sl + 1) * SLOT); aw[3] = adr<96>(wb0, (sl + 1) * SLOT);
#pragma unroll
    for (int q = 0; q < 3; ++q) xf[0][q] = ldsb128(ax[0] + q * 2048);
    rdw(IC<0>{}); rdw(IC<1>{});
  };
  auto khead = [&](auto Sc) AI { ak[0] = adr<0>(kb0, Sc.value * SLOT); ak[1] = adr<64>(kb0, Sc.value * SLOT); rdk(IC<0>{}); rdk(IC<1>{}); };
  auto p1step = [&](int c, auto Tc, auto Ec, auto Nc) AI {
    constexpr int TAIL = Tc.value, EPI = Ec.value, NEXT = Nc.value;
    const int sn = sl >= 2 ? sl - 2 : sl + 4;
    SBAR();
    sfor<24>([&](auto F) AI {
      constexpr int f = F.value, s = f / 12, d = f % 12;
      if constexpr (f == 22) {
        sl = sl == 4 ? 0 : sl + 2;
        if constexpr (NEXT == 0) { WAITV_BAR(12); p1head(); } else { WAITV_BAR(18); khead(IC<2>{}); }
        SBAR();
      }
      if constexpr (f + 2 < 24) rdw(IC<f + 2>{});
      if constexpr (f >= 2 && f < 5) xf[1][f - 2] = ldsb128(ax[1] + (f - 2) * 2048);
      mfma_ip_(qa[d][0], wf[f & 3], xf[s][0]);
      if constexpr (EPI >= 0 && EPI < 9) { constexpr int k = f / 12, part = (f % 12) / 2 - 1;
        if constexpr ((f & 1) == 1 && part >= 0 && part < 4) { epi(IC<2 * EPI + k>{}, IC<part>{}); SBAR(); } }
      mfma_ip_(qa[d][1], wf[f & 3], xf[s][1]);
      if constexpr (((f & 1) == 0 && f <= 20) || f == 21) {
        constexpr int n = f == 21 ? 11 : f >> 1;
        if constexpr (n < 6) { if constexpr (!TAIL) px(c + 2, sn, IC<n>{}); else pk(c - 8, sn, IC<n>{}); }
        else { if constexpr (!TAIL) pw_(c + 2, sn + 1, IC<n - 6>{}); else pv(c - 8, sn + 1, IC<n - 6>{}); }
      }
      mfma_ip_(qa[d][2], wf[f & 3], xf[s][2]);
      SBAR();
    });
  };
  auto zero_qa = [&]() AI {
    float z = 0.f; PINF(z);
#pragma unroll
    for (int d = 0; d < 12; ++d) {
#pragma unroll
      for (int q = 0; q < 3; ++q) qa[d][q] = f32x4{z, z, z, z};
      pin3n_(qa[d][0], qa[d][1], qa[d][2]);
    }
  };

  f32x4 sc[2][4][3];
  f32x4 negm[3];
  unsigned pw[2][3][4];
  bf16x8 ones = {0x3F80, 0x3F80, 0x3F80, 0x3F80, 0x3F80, 0x3F80, 0x3F80, 0x3F80};
  asm volatile("" : "+v"(ones));

#define EXP1(X, kb, q, r) do { sc[X][kb][q][r] = __builtin_amdgcn_exp2f(sc[X][kb][q][r]); PINF(sc[X][kb][q][r]); } while (0)
#define CVT1(X, c) do { constexpr int kb_ = (c) / 6, q_ = ((c) % 6) >> 1, hf_ = (c) & 1; \
    pw[kb_ >> 1][q_][2 * (kb_ & 1) + hf_] = cvtpk(sc[X][kb_][q_][2 * hf_], sc[X][kb_][q_][2 * hf_ + 1]); PINU(pw[kb_ >> 1][q_][2 * (kb_ & 1) + hf_]); } while (0)

  auto conv = [&](auto S, auto Q) AI {
    constexpr int s = S.value, q = Q.value;
    const f32x4 X = qa[2 * s][q] * QSCALE, Y = qa[2 * s + 1][q] * QSCALE;
    unsigned x0 = cvtpk(X[0], X[1]), x1 = cvtpk(X[2], X[3]), y0 = cvtpk(Y[0], Y[1]), y1 = cvtpk(Y[2], Y[3]);
    auto r0 = __builtin_amdgcn_permlane32_swap(x0, y0, false, false);
    auto r1 = __builtin_amdgcn_permlane32_swap(x1, y1, false, false);
    auto t0 = __builtin_amdgcn_permlane16_swap(r0[0], r0[1], false, false);
    auto t1 = __builtin_amdgcn_permlane16_swap(r1[0], r1[1], false, false);
    AWR(AQ(s, q) + 0, t0[0]); AWR(AQ(s, q) + 1, t1[0]); AWR(AQ(s, q) + 2, t0[1]); AWR(AQ(s, q) + 3, t1[1]);
  };

  sfor<6>([&](auto N) AI { px(0, 0, N); }); sfor<6>([&](auto N) AI { pw_(0, 1, N); });
  sfor<6>([&](auto N) AI { px(1, 2, N); }); sfor<6>([&](auto N) AI { pw_(1, 3, N); });
  zero_qa();
  WAITV_BAR(12); p1head();
#pragma unroll 1
  for (int c = 0; c < 8; ++c) p1step(c, IC<0>{}, IC<-1>{}, IC<0>{});
  p1step(8, IC<1>{}, IC<-1>{}, IC<0>{}); p1step(9, IC<1>{}, IC<-1>{}, IC<1>{});

#pragma unroll 1
  for (int it = 0;; ++it) {
    asm volatile("" : "+s"(wg));
    if (it == 0) xg += (size_t)4 * SQ * DM * 2;
    {
      NOPS(7);
      SBAR();
      conv(IC<0>{}, IC<0>{}); conv(IC<0>{}, IC<1>{}); conv(IC<0>{}, IC<2>{});
      conv(IC<1>{}, IC<0>{}); conv(IC<1>{}, IC<1>{}); conv(IC<1>{}, IC<2>{});
      NOPS(1);
      SBAR();
      sfor<24>([&](auto F) AI {
        constexpr int f = F.value, s = f >> 2, kb = f & 3;
        if constexpr (f + 2 < 24) rdk(IC<f + 2>{});
        sfor<3>([&](auto Q) AI {
          constexpr int q = Q.value;
          if constexpr (s == 0) { mfma_s0_<AQ(s, q)>(sc[0][kb][q], kf[f % 3]); } else { MFMA_S(sc[0][kb][q], kf[f % 3], s, q); }
        });
        if constexpr (s < 4 && kb < 3) conv(IC<s + 2>{}, IC<kb>{});
        if constexpr (kb == 3) { pf_(IC<24 + s / 3>{}, IC<2 * (s % 3)>{}); pf_(IC<24 + s / 3>{}, IC<2 * (s % 3) + 1>{}); }
        SBAR();
      });
      WAITV_BAR(18); khead(IC<22 % NSLOT>{});
      asm volatile("s_nop 7" : "+v"(sc[0][0][0]), "+v"(sc[0][0][1]), "+v"(sc[0][0][2]), "+v"(sc[0][1][0]), "+v"(sc[0][1][1]), "+v"(sc[0][1][2]),
                               "+v"(sc[0][2][0]), "+v"(sc[0][2][1]), "+v"(sc[0][2][2]), "+v"(sc[0][3][0]), "+v"(sc[0][3][1]), "+v"(sc[0][3][2]));
#pragma unroll
      for (int q = 0; q < 3; ++q) {
        float m0 = mx3(sc[0][0][q][0], sc[0][0][q][1], sc[0][0][q][2]);
        m0 = mx2(m0, sc[0][0][q][3]);
#pragma unroll
        for (int kb = 1; kb < 4; ++kb) { m0 = mx3(m0, sc[0][kb][q][0], sc[0][kb][q][1]); m0 = mx3(m0, sc[0][kb][q][2], sc[0][kb][q][3]); }
        m0 = xmax(m0);
        negm[q] = f32x4{-m0, -m0, -m0, -m0};
#pragma unroll
        for (int kb = 0; kb < 4; ++kb) sc[0][kb][q] -= m0;
#pragma unroll
        for (int r = 0; r < 4; ++r) sc[0][0][q][r] = __builtin_amdgcn_exp2f(sc[0][0][q][r]);
      }
    }
    sfor<8>([&](auto Jc) AI {
      constexpr int j = Jc.value;
      constexpr int X = j & 1, Y = X ^ 1;
      if constexpr (j < 7) {
        pin3n_(negm[0], negm[1], negm[2]);
        SBAR();
        sfor<24>([&](auto F) AI {
          constexpr int f = F.value, s = f >> 2, kb = f & 3;
          if constexpr (f + 2 < 24) rdk(IC<f + 2>{});
          sfor<3>([&](auto Q) AI {
            constexpr int q = Q.value;
            if constexpr (s == 0) { MFMA_SC(sc[Y][kb][q], kf[f % 3], s, q, negm[q]); } else { MFMA_S(sc[Y][kb][q], kf[f % 3], s, q); }
            if constexpr (q == 0) { CVT1(X, f); }
            else {
              constexpr int e = 2 * f + q - 1;
              if constexpr (e < 36) { EXP1(X, 1 + e / 12, (e % 12) >> 2, e & 3); }
              else if constexpr (q == 1 && j >= 1) pf_(IC<25 + 2 * j>{}, IC<f - 18>{});
            }
            SBAR();
          });
        });
      } else {
        sfor<36>([&](auto E) AI { constexpr int e = E.value; EXP1(X, 1 + e / 12, (e % 12) >> 2, e & 3);
          if constexpr ((e % 6) == 5) pf_(IC<39>{}, IC<e / 6>{}); });
        sfor<24>([&](auto C) AI { CVT1(X, C.value); });
      }
      {
        bf16x8 pf[2][3];
#pragma unroll
        for (int t = 0; t < 2; ++t)
#pragma unroll
          for (int q = 0; q < 3; ++q) { u32x4 f = {pw[t][q][0], pw[t][q][1], pw[t][q][2], pw[t][q][3]}; pf[t][q] = __builtin_bit_cast(bf16x8, f); }
        pin3n_(pf[0][0], pf[0][1], pf[0][2]); pin3n_(pf[1][0], pf[1][1], pf[1][2]);
        unsigned av[4];
        { constexpr unsigned so = ((21 + 2 * j) % NSLOT) * SLOT;
          av[0] = adr<0>(vb0, so); av[1] = adr<32>(vb0, so); av[2] = adr<64>(vb0, so); av[3] = adr<96>(vb0, so); }
        bf16x8 vf[3];
        auto rdv = [&](auto F) AI {
          constexpr int f = F.value, t = f / 12, d = f % 12;
          const unsigned p = av[d & 3] + t * 32 * 384 + (d >> 2) * 128;
          vf[f % 3] = cat8(ldstr(p), ldstr(p + 16 * 384));
        };
        float mx[3], fct[3];
        bool pend = false;
        auto mop = [&](auto I) AI {
          constexpr int q = I.value % 3, k = I.value / 3;
          if constexpr (k == 0) mx[q] = mx2(sc[Y][0][q][0], sc[Y][0][q][1]);
          else { constexpr int e = 2 * k; mx[q] = mx3c(mx[q], sc[Y][e >> 2][q][e & 3], sc[Y][e >> 2][q][(e & 3) + 1]); }
          PINF(mx[q]);
        };
        rdv(IC<0>{}); rdv(IC<1>{});
        SBAR();
        sfor<78>([&](auto Mc) AI {
          constexpr int m = Mc.value, G = m / 3, q = m % 3, t = G / 13, gi = G % 13;
          if constexpr (m == 72) {
            if constexpr (j < 6) { WAITV_BAR(12); khead(IC<(24 + 2 * j) % NSLOT>{}); }
            else if constexpr (j == 6) { WAITV_BAR(18); }
            else { WAITV_BAR(12); sl = 0; p1head(); }
            SBAR();
          }
          if constexpr (gi == 0) {
            if constexpr (j == 0 && t == 0) { MFMA_O0(12, q, ones, pf[t][q]); } else { MFMA_O(12, q, ones, pf[t][q]); }
          } else {
            constexpr int d = gi - 1, f = 12 * t + d;
            if constexpr (q == 0 && f + 2 < 24) rdv(IC<f + 2>{});
            if constexpr (j == 0 && t == 0) { MFMA_O0(d, q, vf[f % 3], pf[t][q]); } else { MFMA_O(d, q, vf[f % 3], pf[t][q]); }
          }
          if constexpr (j < 7 && q < 2) {
            if constexpr (G >= 2 && G < 8) { constexpr int i = 4 * (G - 2) + 2 * q; mop(IC<i>{}); mop(IC<i + 1>{}); }
            if constexpr (G == 8 && q == 0) {
              pend = __any(mx3c(mx[0], mx[1], mx[2]) > THR);
              if (__builtin_expect(pend, 0)) {
#pragma unroll
                for (int qq_ = 0; qq_ < 3; ++qq_) {
                  const float gr = xmax(mx[qq_]);
                  const unsigned db = (__float_as_uint(fmaxf(gr, 0.f)) + 0xFFFFu) & 0xFFFF0000u;
                  fct[qq_] = __builtin_amdgcn_exp2f(-__uint_as_float(db));
                  u32x4 bw = {g == 0 ? ((db >> 16) | 0x8000u) : 0u, 0u, 0u, 0u};
                  bf16x8 bfr = __builtin_bit_cast(bf16x8, bw);
                  asm volatile("s_nop 1" : "+v"(bfr));
                  mfma_ip_(negm[qq_], ones, bfr);
                  mfma_ip_(sc[Y][0][qq_], ones, bfr); mfma_ip_(sc[Y][1][qq_], ones, bfr); mfma_ip_(sc[Y][2][qq_], ones, bfr); mfma_ip_(sc[Y][3][qq_], ones, bfr);
                }
                NOPS(7);
              }
            }
            if constexpr (G >= 9 && G < 15) { constexpr int e = 2 * (G - 9) + q; EXP1(Y, 0, e >> 2, e & 3); }
          }
          if constexpr (j < 7 && q == 1 && G >= 16 && G < 22) pf_(IC<26 + 2 * j>{}, IC<G - 16>{});
          SBAR();
        });
        if constexpr (j < 7) {
          if (__builtin_expect(pend, 0)) {
            NOPS(7);
            sfor<156>([&](auto N) AI { ASCALE(N.value, fct[(N.value >> 2) % 3]); });
            NOPS(1);
          }
        }
      }
    });
    if (it == 1) break;
    kg += (size_t)4 * NH * SK * DH * 2; vg += (size_t)4 * NH * SK * DH * 2;
    zero_qa();
    sfor<10>([&](auto C) AI { p1step(C.value, IC<(C.value >= 8)>{}, IC<C.value>{}, IC<(C.value == 9)>{}); });
    og += (size_t)4 * 192 * 12 * 1024;
  }
  NOPS(7);
  sfor<18>([&](auto U) AI { epi(U, IC<0>{}); epi(U, IC<1>{}); epi(U, IC<2>{}); epi(U, IC<3>{}); });
  asm volatile("s_waitcnt vmcnt(0)" ::: "memory");
}

constexpr int ST2 = 128 * 320;
constexpr int LDS2 = 3 * ST2;
constexpr size_t WS_SLAB = (size_t)64 << 20;
constexpr size_t WS_CNT = (size_t)48 << 20;

DI void gload16(bf16x8& d, unsigned voff, const char* sbase) { asm volatile("global_load_dwordx4 %0, %1, %2" : "=v"(d) : "v"(voff), "s"(sbase) : "memory"); }
template <int N> DI void wait3(bf16x8& a, bf16x8& b, bf16x8& c) { asm volatile("s_waitcnt vmcnt(%3)" : "+v"(a), "+v"(b), "+v"(c) : "i"(N) : "memory"); }

__global__ __launch_bounds__(512, 2) void k_oproj(Ptrs P, char* __restrict__ ws, char* __restrict__ out) {
  extern __shared__ __attribute__((aligned(16))) char smem[];
  lds_ptr lds = (lds_ptr)smem;
  const int tid = threadIdx.x, l = tid & 63, w = __builtin_amdgcn_readfirstlane(tid >> 6);
  const int i16 = l & 15, g = l >> 4, qq = i16 >> 2, pp = i16 & 3;
  const int xcd = blockIdx.x & 7, idx = blockIdx.x >> 3;
  const int br = xcd >> 2, ks = xcd & 3, b = idx >> 2, nq = idx & 3;
  const int kh = w >> 2, qg = w & 3;
  const unsigned lds0 = (unsigned)(uintptr_t)lds;
  const char* wsrc = P.wo[br] + (size_t)ks * 1536 * DM * 2 + nq * 320;
  const char* osrc = ws + (((size_t)(br * NB + b) * 192 + 48 * ks + kh) * 12 + 3 * qg) * 1024;
  const unsigned l16 = l * 16;
  unsigned offW[5];
#pragma unroll
  for (int u = 0; u < 5; ++u) {
    const int o = 1024 * u + 16 * l, r = o / 320, chp = (o % 320) >> 4;
    offW[u] = (16 * w + r) * (DM * 2) + ((chp ^ (((r >> 2) & 1) << 1)) << 4);
  }
  const unsigned o0 = offW[0], o1 = offW[1], o2 = offW[2], o3 = offW[3], o4 = offW[4];
  const int s0 = 0;
  auto wpiece = [&](const char* src, unsigned d, auto Uc) AI {
    constexpr int u = Uc.value;
    glds16(u == 0 ? o0 : u == 1 ? o1 : u == 2 ? o2 : u == 3 ? o3 : o4, src, d + u * 1024);
  };
  auto wsrc_of = [&](int s) AI { s += s0; s = s >= 12 ? s - 12 : s; return wsrc + (size_t)s * 128 * DM * 2; };
  auto osrc_of = [&](int u) AI { u += 2 * s0; u = u >= 24 ? u - 24 : u; return osrc + (size_t)u * 24576; };
  auto issueW = [&](int s, int stg) AI {
    const unsigned d = lds0 + stg * ST2 + w * 5120;
    const char* src = wsrc_of(s);
    sfor<5>([&](auto U) AI { wpiece(src, d, U); });
  };
  bf16x8 of[4][3];
  auto loadO = [&](int u, auto Bc) AI {
    constexpr int B_ = Bc.value;
    const char* src = osrc_of(u);
    gload16(of[B_][0], l16, src); gload16(of[B_][1], l16, src + 1024); gload16(of[B_][2], l16, src + 2048);
  };
  f32x4 acc[10][3];
#pragma unroll
  for (int n = 0; n < 10; ++n)
#pragma unroll
    for (int q = 0; q < 3; ++q) acc[n][q] = f32x4{0.f, 0.f, 0.f, 0.f};
  const unsigned wb0 = lds0 + (32 * kh + 4 * g + qq) * 320 + 8 * pp + ((g & 1) << 5);
  {
    unsigned tch;
    const char* wo = P.wo[br] + (size_t)ks * 1536 * DM * 2 + (size_t)(idx * 8 + w) * 60 * 128;
    asm volatile("global_load_dword %0, %1, %2" : "=v"(tch) : "v"((l < 60 ? l : 59) * 128), "s"(wo) : "memory");
    issueW(0, 0); loadO(0, IC<0>{}); loadO(1, IC<1>{}); loadO(2, IC<2>{});
    issueW(1, 1);
    asm volatile("s_waitcnt vmcnt(19)" : "+v"(tch) :: "memory");
  }
  int stg = 0;
  auto kstep = [&](unsigned ae, unsigned ao, auto Vc, auto Bc, auto Lc, const char* lsrc, const char* dsrc, unsigned dd, auto Dc) AI {
    constexpr int v = Vc.value, B_ = Bc.value, LB = Lc.value, DN = Dc.value;
    bf16x8 wf[3];
    auto rdw = [&](auto N) AI { constexpr int n = N.value; const unsigned p = ((n & 1) ? ao : ae) + v * 64 * 320 + (n >> 1) * 64; wf[n % 3] = cat8(ldstr(p), ldstr(p + 16 * 320)); };
    rdw(IC<0>{}); rdw(IC<1>{});
    SBAR();
    sfor<10>([&](auto N) AI {
      constexpr int n = N.value;
      if constexpr (n + 2 < 10) rdw(IC<n + 2>{});
      acc[n][0] = MFMA16(wf[n % 3], of[B_][0], acc[n][0]);
      if constexpr (LB >= 0 && n < 3) { SBAR(); gload16(of[LB][n], l16, lsrc + n * 1024); SBAR(); }
      if constexpr (DN > 0 && n >= 4 && n < 4 + 2 * DN && (n & 1) == 0) { SBAR(); wpiece(dsrc, dd, IC<(v ? 2 : 0) + (n - 4) / 2>{}); SBAR(); }
      acc[n][1] = MFMA16(wf[n % 3], of[B_][1], acc[n][1]);
      acc[n][2] = MFMA16(wf[n % 3], of[B_][2], acc[n][2]);
      SBAR();
    });
  };
  auto stage = [&](int s, auto Pc, auto Mc) AI {
    constexpr int PAR = Pc.value, MODE = Mc.value, B0 = 2 * PAR, B1 = 2 * PAR + 1;
    if constexpr (MODE == 2) { asm volatile("s_waitcnt vmcnt(3) lgkmcnt(0)\n\ts_barrier" ::: "memory"); }
    else { asm volatile("s_waitcnt vmcnt(11) lgkmcnt(0)\n\ts_barrier" ::: "memory"); }
    wait3<63>(of[B0][0], of[B0][1], of[B0][2]);
    const unsigned ae = opq(wb0 + stg * ST2), ao = opq((wb0 ^ 32) + stg * ST2);
    const unsigned dd = lds0 + (stg == 0 ? 2 : stg - 1) * ST2 + w * 5120;
    const char* dsrc = wsrc_of(s + 2);
    kstep(ae, ao, IC<0>{}, IC<B0>{}, IC<(MODE <= 1 ? ((B0 + 3) & 3) : -1)>{}, osrc_of(2 * s + 3), dsrc, dd, IC<(MODE == 0 ? 2 : 0)>{});
    wait3<MODE == 0 ? 13 : MODE == 1 ? 11 : 0>(of[B1][0], of[B1][1], of[B1][2]);
    kstep(ae, ao, IC<1>{}, IC<B1>{}, IC<(MODE == 0 ? B0 : -1)>{}, osrc_of(2 * s + 4), dsrc, dd, IC<(MODE == 0 ? 3 : 0)>{});
    stg = stg == 2 ? 0 : stg + 1;
  };
#pragma unroll 1
  for (int s = 0; s < 10; s += 2) { stage(s, IC<0>{}, IC<0>{}); stage(s + 1, IC<1>{}, IC<0>{}); }
  stage(10, IC<0>{}, IC<1>{}); stage(11, IC<1>{}, IC<2>{});
  asm volatile("s_waitcnt vmcnt(0) lgkmcnt(0)\n\ts_barrier" ::: "memory");
  {
    const unsigned xo = lds0 + ((kh ^ 1) * 4 + qg) * 15360 + l16, xi = lds0 + (kh * 4 + qg) * 15360 + l16;
#pragma unroll
    for (int n = 0; n < 5; ++n)
#pragma unroll
      for (int q = 0; q < 3; ++q) *(__attribute__((address_space(3))) f32x4*)(xo + (n * 3 + q) * 1024) = kh ? acc[n][q] : acc[5 + n][q];
    __syncthreads();
#pragma unroll
    for (int n = 0; n < 5; ++n)
#pragma unroll
      for (int q = 0; q < 3; ++q) {
        const f32x4 o = *(const __attribute__((address_space(3))) f32x4*)(xi + (n * 3 + q) * 1024);
        acc[n][q] = (kh ? acc[5 + n][q] : acc[n][q]) + o;
      }
  }
  const int tile = (br * NB + b) * 4 + nq;
  char* slab = ws + WS_SLAB + (size_t)tile * 4 * 8 * 15360;
  {
    char* mine = slab + (size_t)(ks * 8 + w) * 15360 + l16;
#pragma unroll
    for (int i = 0; i < 15; ++i)
      if ((i & 3) != ks) asm volatile("global_store_dwordx4 %0, %1, off sc0 sc1\n\ts_nop 1" :: "v"(mine + i * 1024), "v"(acc[i / 3][i % 3]) : "memory");
    asm volatile("s_waitcnt vmcnt(0)" ::: "memory");
    __syncthreads();
    unsigned* cnt = (unsigned*)(ws + WS_CNT) + tile * 16;
    if (tid == 0) {
      __hip_atomic_fetch_add(cnt, 1u, __ATOMIC_RELAXED, __HIP_MEMORY_SCOPE_AGENT);
      unsigned spins = 0;
      while (__hip_atomic_load(cnt, __ATOMIC_RELAXED, __HIP_MEMORY_SCOPE_AGENT) < 4u && ++spins < (1u << 24)) __builtin_amdgcn_s_sleep(1);
    }
    __syncthreads();
  }
  {
    const char* theirs = slab + (size_t)w * 15360 + l16;
    f32x4 p[4][3];
#pragma unroll
    for (int m = 0; m < 4; ++m) {
      const int i = (4 * m + ks) < 15 ? 4 * m + ks : 11;
#pragma unroll
      for (int r = 0; r < 3; ++r) {
        const int src = (ks + 1 + r) & 3;
        asm volatile("global_load_dwordx4 %0, %1, off sc0 sc1" : "=v"(p[m][r]) : "v"(theirs + (size_t)src * 8 * 15360 + i * 1024) : "memory");
      }
    }
    asm volatile("s_waitcnt vmcnt(0)" : "+v"(p[0][0]), "+v"(p[0][1]), "+v"(p[0][2]), "+v"(p[1][0]), "+v"(p[1][1]), "+v"(p[1][2]),
                                        "+v"(p[2][0]), "+v"(p[2][1]), "+v"(p[2][2]), "+v"(p[3][0]), "+v"(p[3][1]), "+v"(p[3][2]) :: "memory");
#pragma unroll
    for (int i = 0; i < 15; ++i) {
      if ((i & 3) == ks) {
        const f32x4 o = (acc[i / 3][i % 3] + p[i >> 2][0]) + (p[i >> 2][1] + p[i >> 2][2]);
        u32x2 f = {cvtpk(o[0], o[1]), cvtpk(o[2], o[3])};
        char* dst = out + ((size_t)(b * SQ + (3 * qg + i % 3) * 16 + i16) * (2 * DM) + br * DM + nq * 160 + (5 * kh + i / 3) * 16 + 4 * g) * 2;
        *(u32x2*)dst = f;
      }
    }
  }
}

extern "C" void kernel_launch(void* const* d_in, const int* in_sizes, int n_in, void* d_out, int out_size, void* d_ws, size_t ws_size,
                              hipStream_t stream) {
  Ptrs P;
  for (int r = 0; r < 2; ++r) {
    P.x[r] = (const char*)d_in[5 * r + 0];
    P.k[r] = (const char*)d_in[5 * r + 1];
    P.v[r] = (const char*)d_in[5 * r + 2];
    P.wq[r] = (const char*)d_in[5 * r + 3];
    P.wo[r] = (const char*)d_in[5 * r + 4];
  }
  (void)hipFuncSetAttribute((const void*)k_attn, hipFuncAttributeMaxDynamicSharedMemorySize, LDS1);
  (void)hipFuncSetAttribute((const void*)k_oproj, hipFuncAttributeMaxDynamicSharedMemorySize, LDS2);
  hipLaunchKernelGGL(k_attn, dim3(256), dim3(256), LDS1, stream, P, (char*)d_ws);
  hipLaunchKernelGGL(k_oproj, dim3(256), dim3(512), LDS2, stream, P, (char*)d_ws, (char*)d_out);
}
```

```cpp
#include <hip/hip_runtime.h>
#include <hip/hip_bf16.h>
#include <stdint.h>
#include <utility>

using bf16x8 = __attribute__((ext_vector_type(8))) short;
using s16x4 = __attribute__((ext_vector_type(4))) short;
using f32x4 = __attribute__((ext_vector_type(4))) float;
using u32x4 = __attribute__((ext_vector_type(4))) unsigned;
using u32x2 = __attribute__((ext_vector_type(2))) unsigned;
typedef short v4i16_t __attribute__((ext_vector_type(4)));
typedef __attribute__((address_space(3))) char* lds_ptr;
typedef __attribute__((address_space(3))) const char* lds_cptr;

constexpr int NB = 8, SQ = 192, DM = 640, NH = 32, SK = 512, DH = 192;
constexpr int HD = NH * DH;
constexpr int SLOT = 24576;
constexpr int NSLOT = 6;
constexpr int LDS1 = SLOT * NSLOT;
constexpr float QSCALE = 0.07216878364870322f * 1.4426950408889634f;
constexpr float THR = 8.0f;

#define DI __device__ __forceinline__
#define MFMA16(a, b, c) __builtin_amdgcn_mfma_f32_16x16x32_bf16((a), (b), (c), 0, 0, 0)
#define ACLOB "a0","a1","a2","a3","a4","a5","a6","a7","a8","a9","a10","a11","a12","a13","a14","a15","a16","a17","a18","a19","a20","a21","a22","a23","a24","a25","a26","a27","a28","a29","a30","a31","a32","a33","a34","a35","a36","a37","a38","a39","a40","a41","a42","a43","a44","a45","a46","a47","a48","a49","a50","a51","a52","a53","a54","a55","a56","a57","a58","a59","a60","a61","a62","a63","a64","a65","a66","a67","a68","a69","a70","a71","a72","a73","a74","a75","a76","a77","a78","a79","a80","a81","a82","a83","a84","a85","a86","a87","a88","a89","a90","a91","a92","a93","a94","a95","a96","a97","a98","a99","a100","a101","a102","a103","a104","a105","a106","a107","a108","a109","a110","a111","a112","a113","a114","a115","a116","a117","a118","a119","a120","a121","a122","a123","a124","a125","a126","a127","a128","a129","a130","a131","a132","a133","a134","a135","a136","a137","a138","a139","a140","a141","a142","a143","a144","a145","a146","a147","a148","a149","a150","a151","a152","a153","a154","a155","a156","a157","a158","a159","a160","a161","a162","a163","a164","a165","a166","a167","a168","a169","a170","a171","a172","a173","a174","a175","a176","a177","a178","a179","a180","a181","a182","a183","a184","a185","a186","a187","a188","a189","a190","a191","a192","a193","a194","a195","a196","a197","a198","a199","a200","a201","a202","a203","a204","a205","a206","a207","a208","a209","a210","a211","a212","a213","a214","a215","a216","a217","a218","a219","a220","a221","a222","a223","a224","a225","a226","a227"
#define AO(d, q) (4 * (3 * (d) + (q)))
#define AQ(s, q) (156 + 4 * (3 * (s) + (q)))
template <int N> DI void mfma_o_(const bf16x8& a, const bf16x8& b) { asm volatile("v_mfma_f32_16x16x32_bf16 a[%c2:%c3], %0, %1, a[%c2:%c3]" :: "v"(a), "v"(b), "i"(N), "i"(N + 3) : ACLOB); }
template <int N> DI void mfma_o0_(const bf16x8& a, const bf16x8& b) { asm volatile("v_mfma_f32_16x16x32_bf16 a[%c2:%c3], %0, %1, 0" :: "v"(a), "v"(b), "i"(N), "i"(N + 3) : ACLOB); }
template <int N> DI void mfma_s_(f32x4& acc, const bf16x8& a) { asm volatile("v_mfma_f32_16x16x32_bf16 %0, %1, a[%c2:%c3], %0" : "+v"(acc) : "v"(a), "i"(N), "i"(N + 3) : ACLOB); }
template <int N> DI void mfma_sc_(f32x4& acc, const bf16x8& a, const f32x4& c) { asm volatile("v_mfma_f32_16x16x32_bf16 %0, %1, a[%c2:%c3], %4" : "=&v"(acc) : "v"(a), "i"(N), "i"(N + 3), "v"(c) : ACLOB); }
template <int N> DI void mfma_s0_(f32x4& acc, const bf16x8& a) { asm volatile("v_mfma_f32_16x16x32_bf16 %0, %1, a[%c2:%c3], 0" : "=&v"(acc) : "v"(a), "i"(N), "i"(N + 3) : ACLOB); }
template <int N> DI float ard_() { float r; asm volatile("v_accvgpr_read_b32 %0, a[%c1]" : "=v"(r) : "i"(N)); return r; }
template <int N, class T> DI void awr_(T src) { asm volatile("v_accvgpr_write_b32 a[%c1], %0" :: "v"(src), "i"(N)); }
template <int N> DI void ascale_(float f) { float t; asm volatile("v_accvgpr_read_b32 %0, a[%c1]\n\tv_mul_f32 %0, %2, %0\n\tv_accvgpr_write_b32 a[%c1], %0" : "=&v"(t) : "i"(N), "v"(f)); }
DI float subip_(float x, float d) { asm volatile("v_sub_f32 %0, %0, %1" : "+v"(x) : "v"(d)); return x; }
DI void mfma_ip_(f32x4& x, const bf16x8& a, const bf16x8& b) { asm volatile("v_mfma_f32_16x16x32_bf16 %0, %1, %2, %0" : "+v"(x) : "v"(a), "v"(b) : ACLOB); }
template <class T> DI T pin_(T x) { asm volatile("" : "+v"(x)); return x; }
template <class T> DI void pin3n_(T& a, T& b, T& c) { asm volatile("s_nop 1" : "+v"(a), "+v"(b), "+v"(c)); }
#define MFMA_O(d, q, a, b) mfma_o_<AO(d, q)>(a, b)
#define MFMA_O0(d, q, a, b) mfma_o0_<AO(d, q)>(a, b)
#define MFMA_S(acc, a, s, q) mfma_s_<AQ(s, q)>(acc, a)
#define MFMA_SC(acc, a, s, q, c) mfma_sc_<AQ(s, q)>(acc, a, c)
#define ARD(dst, n) dst = ard_<n>()
#define AWR(n, src) awr_<n>(src)
#define ASCALE(n, f) ascale_<n>(f)
#define SUBIP(x, d) x = subip_(x, d)
#define NOPS(n) asm volatile("s_nop " #n ::: "memory")
#define SBAR() __builtin_amdgcn_sched_barrier(0)
#define PINF(x) x = pin_(x)
#define PINU(x) x = pin_(x)
#define WAITV_BAR(N) asm volatile("s_waitcnt vmcnt(" #N ") lgkmcnt(0)\n\ts_barrier" ::: "memory")

DI unsigned cvtpk(float lo, float hi) {
  typedef float f32x2 __attribute__((ext_vector_type(2)));
  typedef __bf16 bf16x2 __attribute__((ext_vector_type(2)));
  f32x2 v = {lo, hi}; bf16x2 b = __builtin_convertvector(v, bf16x2); return __builtin_bit_cast(unsigned, b);
}
DI void glds16(unsigned voff, const char* sbase, unsigned ldsaddr) {
  asm volatile("s_mov_b32 m0, %2\n\ts_nop 0\n\tglobal_load_lds_dwordx4 %0, %1" :: "v"(voff), "s"(sbase), "s"(ldsaddr) : "memory");
}
DI void glds16nt(unsigned voff, const char* sbase, unsigned ldsaddr) {
  asm volatile("s_mov_b32 m0, %2\n\ts_nop 0\n\tglobal_load_lds_dwordx4 %0, %1 nt" :: "v"(voff), "s"(sbase), "s"(ldsaddr) : "memory");
}
DI bf16x8 ldsb128(unsigned p) { return *(const __attribute__((address_space(3))) bf16x8*)p; }
DI s16x4 ldstr(unsigned p) { return __builtin_bit_cast(s16x4, __builtin_amdgcn_ds_read_tr16_b64_v4i16((__attribute__((address_space(3))) v4i16_t*)p)); }
template <int X> DI unsigned adr(unsigned base, unsigned off) {
  unsigned r;
  if constexpr (X == 0) asm volatile("v_add_u32 %0, %2, %1" : "=v"(r) : "v"(base), "s"(off));
  else asm volatile("v_xor_b32 %0, %3, %1\n\tv_add_u32 %0, %2, %0" : "=&v"(r) : "v"(base), "s"(off), "i"(X));
  return r;
}
DI unsigned opq(unsigned x) { asm volatile("" : "+v"(x)); return x; }
DI bf16x8 cat8(s16x4 lo, s16x4 hi) { return __builtin_shufflevector(lo, hi, 0, 1, 2, 3, 4, 5, 6, 7); }
DI float mx3(float a, float b, float c) { float r; asm("v_max3_f32 %0, %1, %2, %3" : "=v"(r) : "v"(a), "v"(b), "v"(c)); return r; }
DI float mx3c(float a, float b, float c) { return __builtin_fmaxf(__builtin_fmaxf(a, b), c); }
DI float mx2(float a, float b) { float r; asm("v_max_f32_e32 %0, %1, %2" : "=v"(r) : "v"(a), "v"(b)); return r; }
DI float xmax(float v) {
  auto a = __builtin_amdgcn_permlane32_swap(__float_as_uint(v), __float_as_uint(v), false, false);
  v = mx2(__uint_as_float(a[0]), __uint_as_float(a[1]));
  auto b = __builtin_amdgcn_permlane16_swap(__float_as_uint(v), __float_as_uint(v), false, false);
  return mx2(__uint_as_float(b[0]), __uint_as_float(b[1]));
}
DI float xsum(float v) {
  auto a = __builtin_amdgcn_permlane32_swap(__float_as_uint(v), __float_as_uint(v), false, false);
  v = __uint_as_float(a[0]) + __uint_as_float(a[1]);
  auto b = __builtin_amdgcn_permlane16_swap(__float_as_uint(v), __float_as_uint(v), false, false);
  return __uint_as_float(b[0]) + __uint_as_float(b[1]);
}

template <int N> struct IC { static constexpr int value = N; };
#define AI __attribute__((always_inline))
template <class F, int... Is> DI void sfor_(F&& f, std::integer_sequence<int, Is...>) { (f(IC<Is>{}), ...); }
template <int N, class F> DI void sfor(F&& f) { sfor_(f, std::make_integer_sequence<int, N>{}); }
struct Ptrs { const char* x[2]; const char* k[2]; const char* v[2]; const char* wq[2]; const char* wo[2]; };

__global__ __launch_bounds__(256, 1) void k_attn(Ptrs P, char* __restrict__ ofr) {
  extern __shared__ __attribute__((aligned(16))) char smem[];
  lds_ptr lds = (lds_ptr)smem;
  const int tid = threadIdx.x, l = tid & 63, w = __builtin_amdgcn_readfirstlane(tid >> 6);
  const int i16 = l & 15, g = l >> 4, qq = i16 >> 2, pp = i16 & 3;
  const int xcd = blockIdx.x & 7, idx = blockIdx.x >> 3;
  const int br = xcd >> 2, h = 8 * (xcd & 3) + (idx & 7), b0 = idx >> 3;
  const char* xg = P.x[br] + (size_t)b0 * SQ * DM * 2;
  const char* wg = P.wq[br] + (size_t)h * DH * 2;
  const char* kg = P.k[br] + (size_t)(b0 * NH + h) * SK * DH * 2;
  const char* vg = P.v[br] + (size_t)(b0 * NH + h) * SK * DH * 2;
  char* og = ofr + (((size_t)(br * NB + b0) * 192 + h * 6) * 12 + 3 * w) * 1024 + l * 16;

  if (blockIdx.x == 0 && tid < 64) ((unsigned*)(ofr + ((size_t)48 << 20)))[tid * 16] = 0u;
  unsigned offK[3], offV[3], offW[3], offX;
#pragma unroll
  for (int u = 0; u < 3; ++u) {
    const int o = 1024 * u + 16 * l, r = o / 384, chp = (o % 384) >> 4, row = 8 * w + r;
    const int aK = (row >> 1) & 7;
    offK[u] = row * 384 + (((chp & ~7) | ((chp ^ aK) & 7)) << 4);
    offV[u] = row * 384 + ((chp ^ (((row >> 1) & 3) << 1)) << 4);
    const int sW = ((row >> 1) & 1) | (((row >> 3) & 1) << 1);
    offW[u] = row * (HD * 2) + ((chp ^ (sW << 1)) << 4);
  }
  { const int row = 8 * w + (l >> 3), a = (row >> 1) & 7; offX = row * (DM * 2) + (((l & 7) ^ a) << 4); }

  const unsigned oK0 = offK[0], oK1 = offK[1], oK2 = offK[2], oV0 = offV[0], oV1 = offV[1], oV2 = offV[2], oW0 = offW[0], oW1 = offW[1], oW2 = offW[2];
  const unsigned lds0 = (unsigned)(uintptr_t)lds;
  auto px = [&](int c, int slot, auto N) AI {
    constexpr int n = N.value;
    glds16(offX, xg + c * 128 + (size_t)n * 32 * DM * 2, lds0 + slot * SLOT + w * 1024 + n * 4096);
  };
  auto pw_ = [&](int c, int slot, auto N) AI {
    constexpr int n = N.value, u = n % 3, hf = n / 3;
    glds16(u == 0 ? oW0 : u == 1 ? oW1 : oW2, wg + (size_t)(c * 64 + hf * 32) * HD * 2, lds0 + slot * SLOT + w * 3072 + hf * 12288 + u * 1024);
  };
  auto pk = [&](int j, int slot, auto N) AI {
    constexpr int n = N.value, u = n % 3, hf = n / 3;
    glds16nt(u == 0 ? oK0 : u == 1 ? oK1 : oK2, kg + (size_t)j * SLOT + hf * 12288, lds0 + slot * SLOT + w * 3072 + hf * 12288 + u * 1024);
  };
  auto pv = [&](int j, int slot, auto N) AI {
    constexpr int n = N.value, u = n % 3, hf = n / 3;
    glds16nt(u == 0 ? oV0 : u == 1 ? oV1 : oV2, vg + (size_t)j * SLOT + hf * 12288, lds0 + slot * SLOT + w * 3072 + hf * 12288 + u * 1024);
  };
  auto pf_ = [&](auto F, auto N) AI {
    constexpr int f = F.value;
    if constexpr (f >= 36) { if constexpr ((f & 1) == 0) px((f - 36) >> 1, f % NSLOT, N); else pw_((f - 36) >> 1, f % NSLOT, N); }
    else if constexpr ((f & 1) == 0) pk((f - 20) >> 1, f % NSLOT, N); else pv((f - 20) >> 1, f % NSLOT, N);
  };

  const int a8 = i16 >> 1;
  const unsigned kb0 = lds0 + i16 * 384 + ((g ^ a8) << 4);
  const unsigned xb0 = lds0 + (48 * w + i16) * 128 + ((g ^ a8) << 4);
  const int swz = 2 * (g & 1) + (qq >> 1);
  const unsigned vb0 = lds0 + (4 * g + qq) * 384 + (swz << 5) + 8 * pp;
  const unsigned wb0 = lds0 + (8 * g + qq) * 384 + (swz << 5) + 8 * pp;

  asm volatile("" ::: ACLOB);
  f32x4 qa[12][3];
  float inv = 0.f; f32x4 eX, eY;
  auto epi = [&](auto U, auto Pt) AI {
    constexpr int u = U.value, q = u / 6, t = u % 6, part = Pt.value;
    if constexpr (part == 0) {
      if constexpr (t == 0) { float lq; ARD(lq, AO(12, q)); inv = __builtin_amdgcn_rcpf(lq); }
      sfor<4>([&](auto R) AI { ARD(eX[R.value], AO(2 * t, q) + R.value); });
    } else if constexpr (part == 1) {
      sfor<4>([&](auto R) AI { ARD(eY[R.value], AO(2 * t + 1, q) + R.value); });
    } else if constexpr (part == 2) { eX *= inv; eY *= inv; PINF(eX); PINF(eY); }
    else if constexpr (part == 3) {
      u32x4 f = {cvtpk(eX[0], eX[1]), cvtpk(eX[2], eX[3]), cvtpk(eY[0], eY[1]), cvtpk(eY[2], eY[3])};
      *(u32x4*)(og + q * 1024 + t * 12288) = f;
    }
  };
  int sl = 0;
  unsigned ax[2], aw[4], ak[2];
  bf16x8 xf[2][3], wf[4], kf[3];
  auto rdw = [&](auto F) AI {
    constexpr int f = F.value, s = f / 12, d = f % 12;
    const unsigned p = aw[d & 3] + s * 32 * 384 + (d >> 2) * 128;
    wf[f & 3] = cat8(ldstr(p), ldstr(p + 4 * 384));
  };
  auto rdk = [&](auto F) AI { constexpr int f = F.value, s = f >> 2, kb = f & 3; kf[f % 3] = ldsb128(ak[s & 1] + kb * 6144 + (s >> 1) * 128); };
  auto p1head = [&]() AI {
    ax[0] = adr<0>(xb0, sl * SLOT); ax[1] = adr<64>(xb0, sl * SLOT);
    aw[0] = adr<0>(wb0, (sl + 1) * SLOT); aw[1] = adr<32>(wb0, (sl + 1) * SLOT); aw[2] = adr<64>(wb0, (sl + 1) * SLOT); aw[3] = adr<96>(wb0, (sl + 1) * SLOT);
#pragma unroll
    for (int q = 0; q < 3; ++q) xf[0][q] = ldsb128(ax[0] + q * 2048);
    rdw(IC<0>{}); rdw(IC<1>{});
  };
  auto khead = [&](auto Sc) AI { ak[0] = adr<0>(kb0, Sc.value * SLOT); ak[1] = adr<64>(kb0, Sc.value * SLOT); rdk(IC<0>{}); rdk(IC<1>{}); };
  auto p1step = [&](int c, auto Tc, auto Ec, auto Nc) AI {
    constexpr int TAIL = Tc.value, EPI = Ec.value, NEXT = Nc.value;
    const int sn = sl >= 2 ? sl - 2 : sl + 4;
    SBAR();
    sfor<24>([&](auto F) AI {
      constexpr int f = F.value, s = f / 12, d = f % 12;
      if constexpr (f == 22) {
        sl = sl == 4 ? 0 : sl + 2;
        if constexpr (NEXT == 0) { WAITV_BAR(12); p1head(); } else { WAITV_BAR(18); khead(IC<2>{}); }
        SBAR();
      }
      if constexpr (f + 2 < 24) rdw(IC<f + 2>{});
      if constexpr (f >= 2 && f < 5) xf[1][f - 2] = ldsb128(ax[1] + (f - 2) * 2048);
      mfma_ip_(qa[d][0], wf[f & 3], xf[s][0]);
      if constexpr (EPI >= 0 && EPI < 9) { constexpr int k = f / 12, part = (f % 12) / 2 - 1;
        if constexpr ((f & 1) == 1 && part >= 0 && part < 4) { epi(IC<2 * EPI + k>{}, IC<part>{}); SBAR(); } }
      mfma_ip_(qa[d][1], wf[f & 3], xf[s][1]);
      if constexpr (((f & 1) == 0 && f <= 20) || f == 21) {
        constexpr int n = f == 21 ? 11 : f >> 1;
        if constexpr (n < 6) { if constexpr (!TAIL) px(c + 2, sn, IC<n>{}); else pk(c - 8, sn, IC<n>{}); }
        else { if constexpr (!TAIL) pw_(c + 2, sn + 1, IC<n - 6>{}); else pv(c - 8, sn + 1, IC<n - 6>{}); }
      }
      mfma_ip_(qa[d][2], wf[f & 3], xf[s][2]);
      SBAR();
    });
  };
  auto zero_qa = [&]() AI {
    float z = 0.f; PINF(z);
#pragma unroll
    for (int d = 0; d < 12; ++d) {
#pragma unroll
      for (int q = 0; q < 3; ++q) qa[d][q] = f32x4{z, z, z, z};
      pin3n_(qa[d][0], qa[d][1], qa[d][2]);
    }
  };

  f32x4 sc[2][4][3];
  f32x4 negm[3];
  unsigned pw[2][3][4];
  bf16x8 ones = {0x3F80, 0x3F80, 0x3F80, 0x3F80, 0x3F80, 0x3F80, 0x3F80, 0x3F80};
  asm volatile("" : "+v"(ones));

#define EXP1(X, kb, q, r) do { sc[X][kb][q][r] = __builtin_amdgcn_exp2f(sc[X][kb][q][r]); PINF(sc[X][kb][q][r]); } while (0)
#define CVT1(X, c) do { constexpr int kb_ = (c) / 6, q_ = ((c) % 6) >> 1, hf_ = (c) & 1; \
    pw[kb_ >> 1][q_][2 * (kb_ & 1) + hf_] = cvtpk(sc[X][kb_][q_][2 * hf_], sc[X][kb_][q_][2 * hf_ + 1]); PINU(pw[kb_ >> 1][q_][2 * (kb_ & 1) + hf_]); } while (0)

  auto conv = [&](auto S, auto Q) AI {
    constexpr int s = S.value, q = Q.value;
    const f32x4 X = qa[2 * s][q] * QSCALE, Y = qa[2 * s + 1][q] * QSCALE;
    unsigned x0 = cvtpk(X[0], X[1]), x1 = cvtpk(X[2], X[3]), y0 = cvtpk(Y[0], Y[1]), y1 = cvtpk(Y[2], Y[3]);
    auto r0 = __builtin_amdgcn_permlane32_swap(x0, y0, false, false);
    auto r1 = __builtin_amdgcn_permlane32_swap(x1, y1, false, false);
    auto t0 = __builtin_amdgcn_permlane16_swap(r0[0], r0[1], false, false);
    auto t1 = __builtin_amdgcn_permlane16_swap(r1[0], r1[1], false, false);
    AWR(AQ(s, q) + 0, t0[0]); AWR(AQ(s, q) + 1, t1[0]); AWR(AQ(s, q) + 2, t0[1]); AWR(AQ(s, q) + 3, t1[1]);
  };

  sfor<6>([&](auto N) AI { px(0, 0, N); }); sfor<6>([&](auto N) AI { pw_(0, 1, N); });
  sfor<6>([&](auto N) AI { px(1, 2, N); }); sfor<6>([&](auto N) AI { pw_(1, 3, N); });
  zero_qa();
  WAITV_BAR(12); p1head();
#pragma unroll 1
  for (int c = 0; c < 8; ++c) p1step(c, IC<0>{}, IC<-1>{}, IC<0>{});
  p1step(8, IC<1>{}, IC<-1>{}, IC<0>{}); p1step(9, IC<1>{}, IC<-1>{}, IC<1>{});

#pragma unroll 1
  for (int it = 0;; ++it) {
    asm volatile("" : "+s"(wg));
    if (it == 0) xg += (size_t)4 * SQ * DM * 2;
    {
      NOPS(7);
      SBAR();
      conv(IC<0>{}, IC<0>{}); conv(IC<0>{}, IC<1>{}); conv(IC<0>{}, IC<2>{});
      conv(IC<1>{}, IC<0>{}); conv(IC<1>{}, IC<1>{}); conv(IC<1>{}, IC<2>{});
      NOPS(1);
      SBAR();
      sfor<24>([&](auto F) AI {
        constexpr int f = F.value, s = f >> 2, kb = f & 3;
        if constexpr (f + 2 < 24) rdk(IC<f + 2>{});
        sfor<3>([&](auto Q) AI {
          constexpr int q = Q.value;
          if constexpr (s == 0) { mfma_s0_<AQ(s, q)>(sc[0][kb][q], kf[f % 3]); } else { MFMA_S(sc[0][kb][q], kf[f % 3], s, q); }
        });
        if constexpr (s < 4 && kb < 3) conv(IC<s + 2>{}, IC<kb>{});
        if constexpr (kb == 3) { pf_(IC<24 + s / 3>{}, IC<2 * (s % 3)>{}); pf_(IC<24 + s / 3>{}, IC<2 * (s % 3) + 1>{}); }
        SBAR();
      });
      WAITV_BAR(18); khead(IC<22 % NSLOT>{});
      asm volatile("s_nop 7" : "+v"(sc[0][0][0]), "+v"(sc[0][0][1]), "+v"(sc[0][0][2]), "+v"(sc[0][1][0]), "+v"(sc[0][1][1]), "+v"(sc[0][1][2]),
                               "+v"(sc[0][2][0]), "+v"(sc[0][2][1]), "+v"(sc[0][2][2]), "+v"(sc[0][3][0]), "+v"(sc[0][3][1]), "+v"(sc[0][3][2]));
#pragma unroll
      for (int q = 0; q < 3; ++q) {
        float m0 = mx3(sc[0][0][q][0], sc[0][0][q][1], sc[0][0][q][2]);
        m0 = mx2(m0, sc[0][0][q][3]);
#pragma unroll
        for (int kb = 1; kb < 4; ++kb) { m0 = mx3(m0, sc[0][kb][q][0], sc[0][kb][q][1]); m0 = mx3(m0, sc[0][kb][q][2], sc[0][kb][q][3]); }
        m0 = xmax(m0);
        negm[q] = f32x4{-m0, -m0, -m0, -m0};
#pragma unroll
        for (int kb = 0; kb < 4; ++kb) sc[0][kb][q] -= m0;
#pragma unroll
        for (int r = 0; r < 4; ++r) sc[0][0][q][r] = __builtin_amdgcn_exp2f(sc[0][0][q][r]);
      }
    }
    sfor<8>([&](auto Jc) AI {
      constexpr int j = Jc.value;
      constexpr int X = j & 1, Y = X ^ 1;
      if constexpr (j < 7) {
        pin3n_(negm[0], negm[1], negm[2]);
        SBAR();
        sfor<24>([&](auto F) AI {
          constexpr int f = F.value, s = f >> 2, kb = f & 3;
          if constexpr (f + 2 < 24) rdk(IC<f + 2>{});
          sfor<3>([&](auto Q) AI {
            constexpr int q = Q.value;
            if constexpr (s == 0) { MFMA_SC(sc[Y][kb][q], kf[f % 3], s, q, negm[q]); } else { MFMA_S(sc[Y][kb][q], kf[f % 3], s, q); }
            if constexpr (q == 0) { CVT1(X, f); }
            else {
              constexpr int e = 2 * f + q - 1;
              if constexpr (e < 36) { EXP1(X, 1 + e / 12, (e % 12) >> 2, e & 3); }
              else if constexpr (q == 1 && j >= 1) pf_(IC<25 + 2 * j>{}, IC<f - 18>{});
            }
            SBAR();
          });
        });
      } else {
        sfor<36>([&](auto E) AI { constexpr int e = E.value; EXP1(X, 1 + e / 12, (e % 12) >> 2, e & 3);
          if constexpr ((e % 6) == 5) pf_(IC<39>{}, IC<e / 6>{}); });
        sfor<24>([&](auto C) AI { CVT1(X, C.value); });
      }
      {
        bf16x8 pf[2][3];
#pragma unroll
        for (int t = 0; t < 2; ++t)
#pragma unroll
          for (int q = 0; q < 3; ++q) { u32x4 f = {pw[t][q][0], pw[t][q][1], pw[t][q][2], pw[t][q][3]}; pf[t][q] = __builtin_bit_cast(bf16x8, f); }
        pin3n_(pf[0][0], pf[0][1], pf[0][2]); pin3n_(pf[1][0], pf[1][1], pf[1][2]);
        unsigned av[4];
        { constexpr unsigned so = ((21 + 2 * j) % NSLOT) * SLOT;
          av[0] = adr<0>(vb0, so); av[1] = adr<32>(vb0, so); av[2] = adr<64>(vb0, so); av[3] = adr<96>(vb0, so); }
        bf16x8 vf[3];
        auto rdv = [&](auto F) AI {
          constexpr int f = F.value, t = f / 12, d = f % 12;
          const unsigned p = av[d & 3] + t * 32 * 384 + (d >> 2) * 128;
          vf[f % 3] = cat8(ldstr(p), ldstr(p + 16 * 384));
        };
        float mx[3], fct[3];
        bool pend = false;
        auto mop = [&](auto I) AI {
          constexpr int q = I.value % 3, k = I.value / 3;
          if constexpr (k == 0) mx[q] = mx2(sc[Y][0][q][0], sc[Y][0][q][1]);
          else { constexpr int e = 2 * k; mx[q] = mx3c(mx[q], sc[Y][e >> 2][q][e & 3], sc[Y][e >> 2][q][(e & 3) + 1]); }
          PINF(mx[q]);
        };
        rdv(IC<0>{}); rdv(IC<1>{});
        SBAR();
        sfor<78>([&](auto Mc) AI {
          constexpr int m = Mc.value, G = m / 3, q = m % 3, t = G / 13, gi = G % 13;
          if constexpr (m == 72) {
            if constexpr (j < 6) { WAITV_BAR(12); khead(IC<(24 + 2 * j) % NSLOT>{}); }
            else if constexpr (j == 6) { WAITV_BAR(18); }
            else { WAITV_BAR(12); sl = 0; p1head(); }
            SBAR();
          }
          if constexpr (gi == 0) {
            if constexpr (j == 0 && t == 0) { MFMA_O0(12, q, ones, pf[t][q]); } else { MFMA_O(12, q, ones, pf[t][q]); }
          } else {
            constexpr int d = gi - 1, f = 12 * t + d;
            if constexpr (q == 0 && f + 2 < 24) rdv(IC<f + 2>{});
            if constexpr (j == 0 && t == 0) { MFMA_O0(d, q, vf[f % 3], pf[t][q]); } else { MFMA_O(d, q, vf[f % 3], pf[t][q]); }
          }
          if constexpr (j < 7 && q < 2) {
            if constexpr (G >= 2 && G < 8) { constexpr int i = 4 * (G - 2) + 2 * q; mop(IC<i>{}); mop(IC<i + 1>{}); }
            if constexpr (G == 8 && q == 0) {
              pend = __any(mx3c(mx[0], mx[1], mx[2]) > THR);
              if (__builtin_expect(pend, 0)) {
#pragma unroll
                for (int qq_ = 0; qq_ < 3; ++qq_) {
                  const float gr = xmax(mx[qq_]);
                  const unsigned db = (__float_as_uint(fmaxf(gr, 0.f)) + 0xFFFFu) & 0xFFFF0000u;
                  fct[qq_] = __builtin_amdgcn_exp2f(-__uint_as_float(db));
                  u32x4 bw = {g == 0 ? ((db >> 16) | 0x8000u) : 0u, 0u, 0u, 0u};
                  bf16x8 bfr = __builtin_bit_cast(bf16x8, bw);
                  asm volatile("s_nop 1" : "+v"(bfr));
                  mfma_ip_(negm[qq_], ones, bfr);
                  mfma_ip_(sc[Y][0][qq_], ones, bfr); mfma_ip_(sc[Y][1][qq_], ones, bfr); mfma_ip_(sc[Y][2][qq_], ones, bfr); mfma_ip_(sc[Y][3][qq_], ones, bfr);
                }
                NOPS(7);
              }
            }
            if constexpr (G >= 9 && G < 15) { constexpr int e = 2 * (G - 9) + q; EXP1(Y, 0, e >> 2, e & 3); }
          }
          if constexpr (j < 7 && q == 1 && G >= 16 && G < 22) pf_(IC<26 + 2 * j>{}, IC<G - 16>{});
          SBAR();
        });
        if constexpr (j < 7) {
          if (__builtin_expect(pend, 0)) {
            NOPS(7);
            sfor<156>([&](auto N) AI { ASCALE(N.value, fct[(N.value >> 2) % 3]); });
            NOPS(1);
          }
        }
      }
    });
    if (it == 1) break;
    kg += (size_t)4 * NH * SK * DH * 2; vg += (size_t)4 * NH * SK * DH * 2;
    zero_qa();
    sfor<10>([&](auto C) AI { p1step(C.value, IC<(C.value >= 8)>{}, IC<C.value>{}, IC<(C.value == 9)>{}); });
    og += (size_t)4 * 192 * 12 * 1024;
  }
  NOPS(7);
  sfor<18>([&](auto U) AI { epi(U, IC<0>{}); epi(U, IC<1>{}); epi(U, IC<2>{}); epi(U, IC<3>{}); });
  asm volatile("s_waitcnt vmcnt(0)" ::: "memory");
}

constexpr int ST2 = 128 * 320;
constexpr int LDS2 = 3 * ST2;
constexpr size_t WS_SLAB = (size_t)64 << 20;
constexpr size_t WS_CNT = (size_t)48 << 20;

DI void gload16(bf16x8& d, unsigned voff, const char* sbase) { asm volatile("global_load_dwordx4 %0, %1, %2" : "=v"(d) : "v"(voff), "s"(sbase) : "memory"); }
template <int N> DI void wait3(bf16x8& a, bf16x8& b, bf16x8& c) { asm volatile("s_waitcnt vmcnt(%3)" : "+v"(a), "+v"(b), "+v"(c) : "i"(N) : "memory"); }

__global__ __launch_bounds__(512, 2) void k_oproj(Ptrs P, char* __restrict__ ws, char* __restrict__ out) {
  extern __shared__ __attribute__((aligned(16))) char smem[];
  lds_ptr lds = (lds_ptr)smem;
  const int tid = threadIdx.x, l = tid & 63, w = __builtin_amdgcn_readfirstlane(tid >> 6);
  const int i16 = l & 15, g = l >> 4, qq = i16 >> 2, pp = i16 & 3;
  const int xcd = blockIdx.x & 7, idx = blockIdx.x >> 3;
  const int br = xcd >> 2, ks = xcd & 3, b = idx >> 2, nq = idx & 3;
  const int kh = w >> 2, qg = w & 3;
  const unsigned lds0 = (unsigned)(uintptr_t)lds;
  const char* wsrc = P.wo[br] + (size_t)ks * 1536 * DM * 2 + nq * 320;
  const char* osrc = ws + (((size_t)(br * NB + b) * 192 + 48 * ks + kh) * 12 + 3 * qg) * 1024;
  const unsigned l16 = l * 16;
  unsigned offW[5];
#pragma unroll
  for (int u = 0; u < 5; ++u) {
    const int o = 1024 * u + 16 * l, r = o / 320, chp = (o % 320) >> 4;
    offW[u] = (16 * w + r) * (DM * 2) + ((chp ^ (((r >> 2) & 1) << 1)) << 4);
  }
  const unsigned o0 = offW[0], o1 = offW[1], o2 = offW[2], o3 = offW[3], o4 = offW[4];
  const int s0 = 0;
  auto wpiece = [&](const char* src, unsigned d, auto Uc) AI {
    constexpr int u = Uc.value;
    glds16(u == 0 ? o0 : u == 1 ? o1 : u == 2 ? o2 : u == 3 ? o3 : o4, src, d + u * 1024);
  };
  auto wsrc_of = [&](int s) AI { s += s0; s = s >= 12 ? s - 12 : s; return wsrc + (size_t)s * 128 * DM * 2; };
  auto osrc_of = [&](int u) AI { u += 2 * s0; u = u >= 24 ? u - 24 : u; return osrc + (size_t)u * 24576; };
  auto issueW = [&](int s, int stg) AI {
    const unsigned d = lds0 + stg * ST2 + w * 5120;
    const char* src = wsrc_of(s);
    sfor<5>([&](auto U) AI { wpiece(src, d, U); });
  };
  bf16x8 of[4][3];
  auto loadO = [&](int u, auto Bc) AI {
    constexpr int B_ = Bc.value;
    const char* src = osrc_of(u);
    gload16(of[B_][0], l16, src); gload16(of[B_][1], l16, src + 1024); gload16(of[B_][2], l16, src + 2048);
  };
  f32x4 acc[10][3];
#pragma unroll
  for (int n = 0; n < 10; ++n)
#pragma unroll
    for (int q = 0; q < 3; ++q) acc[n][q] = f32x4{0.f, 0.f, 0.f, 0.f};
  const unsigned wb0 = lds0 + (32 * kh + 4 * g + qq) * 320 + 8 * pp + ((g & 1) << 5);
  {
    unsigned tch;
    const char* wo = P.wo[br] + (size_t)ks * 1536 * DM * 2 + (size_t)(idx * 8 + w) * 60 * 128;
    asm volatile("global_load_dword %0, %1, %2" : "=v"(tch) : "v"((l < 60 ? l : 59) * 128), "s"(wo) : "memory");
    issueW(0, 0); loadO(0, IC<0>{}); loadO(1, IC<1>{}); loadO(2, IC<2>{});
    issueW(1, 1);
    asm volatile("s_waitcnt vmcnt(19)" : "+v"(tch) :: "memory");
  }
  int stg = 0;
  auto kstep = [&](unsigned ae, unsigned ao, auto Vc, auto Bc, auto Lc, const char* lsrc, const char* dsrc, unsigned dd, auto Dc) AI {
    constexpr int v = Vc.value, B_ = Bc.value, LB = Lc.value, DN = Dc.value;
    bf16x8 wf[3];
    auto rdw = [&](auto N) AI { constexpr int n = N.value; const unsigned p = ((n & 1) ? ao : ae) + v * 64 * 320 + (n >> 1) * 64; wf[n % 3] = cat8(ldstr(p), ldstr(p + 16 * 320)); };
    rdw(IC<0>{}); rdw(IC<1>{});
    SBAR();
    sfor<10>([&](auto N) AI {
      constexpr int n = N.value;
      if constexpr (n + 2 < 10) rdw(IC<n + 2>{});
      acc[n][0] = MFMA16(wf[n % 3], of[B_][0], acc[n][0]);
      if constexpr (LB >= 0 && n < 3) { SBAR(); gload16(of[LB][n], l16, lsrc + n * 1024); SBAR(); }
      if constexpr (DN > 0 && n >= 4 && n < 4 + 2 * DN && (n & 1) == 0) { SBAR(); wpiece(dsrc, dd, IC<(v ? 2 : 0) + (n - 4) / 2>{}); SBAR(); }
      acc[n][1] = MFMA16(wf[n % 3], of[B_][1], acc[n][1]);
      acc[n][2] = MFMA16(wf[n % 3], of[B_][2], acc[n][2]);
      SBAR();
    });
  };
  auto stage = [&](int s, auto Pc, auto Mc) AI {
    constexpr int PAR = Pc.value, MODE = Mc.value, B0 = 2 * PAR, B1 = 2 * PAR + 1;
    if constexpr (MODE == 2) { asm volatile("s_waitcnt vmcnt(3) lgkmcnt(0)\n\ts_barrier" ::: "memory"); }
    else { asm volatile("s_waitcnt vmcnt(11) lgkmcnt(0)\n\ts_barrier" ::: "memory"); }
    wait3<63>(of[B0][0], of[B0][1], of[B0][2]);
    const unsigned ae = opq(wb0 + stg * ST2), ao = opq((wb0 ^ 32) + stg * ST2);
    const unsigned dd = lds0 + (stg == 0 ? 2 : stg - 1) * ST2 + w * 5120;
    const char* dsrc = wsrc_of(s + 2);
    kstep(ae, ao, IC<0>{}, IC<B0>{}, IC<(MODE <= 1 ? ((B0 + 3) & 3) : -1)>{}, osrc_of(2 * s + 3), dsrc, dd, IC<(MODE == 0 ? 2 : 0)>{});
    wait3<MODE == 0 ? 13 : MODE == 1 ? 11 : 0>(of[B1][0], of[B1][1], of[B1][2]);
    kstep(ae, ao, IC<1>{}, IC<B1>{}, IC<(MODE == 0 ? B0 : -1)>{}, osrc_of(2 * s + 4), dsrc, dd, IC<(MODE == 0 ? 3 : 0)>{});
    stg = stg == 2 ? 0 : stg + 1;
  };
#pragma unroll 1
  for (int s = 0; s < 10; s += 2) { stage(s, IC<0>{}, IC<0>{}); stage(s + 1, IC<1>{}, IC<0>{}); }
  stage(10, IC<0>{}, IC<1>{}); stage(11, IC<1>{}, IC<2>{});
  asm volatile("s_waitcnt vmcnt(0) lgkmcnt(0)\n\ts_barrier" ::: "memory");
  {
    const unsigned xo = lds0 + ((kh ^ 1) * 4 + qg) * 15360 + l16, xi = lds0 + (kh * 4 + qg) * 15360 + l16;
#pragma unroll
    for (int n = 0; n < 5; ++n)
#pragma unroll
      for (int q = 0; q < 3; ++q) *(__attribute__((address_space(3))) f32x4*)(xo + (n * 3 + q) * 1024) = kh ? acc[n][q] : acc[5 + n][q];
    __syncthreads();
#pragma unroll
    for (int n = 0; n < 5; ++n)
#pragma unroll
      for (int q = 0; q < 3; ++q) {
        const f32x4 o = *(const __attribute__((address_space(3))) f32x4*)(xi + (n * 3 + q) * 1024);
        acc[n][q] = (kh ? acc[5 + n][q] : acc[n][q]) + o;
      }
  }
  const int tile = (br * NB + b) * 4 + nq;
  char* slab = ws + WS_SLAB + (size_t)tile * (16 * 8 * 2048);
  {
#pragma unroll
    for (int r = 0; r < 4; ++r) {
      if (r != ks) {
        char* dst = slab + (size_t)((r * 4 + ks) * 8 + w) * 2048 + l16;
#pragma unroll
        for (int m = 0; m < 2; ++m) {
          const int i0_ = 8 * m + r, i1_ = (8 * m + 4 + r) < 15 ? 8 * m + 4 + r : i0_;
          const f32x4 a = acc[i0_ / 3][i0_ % 3], c = acc[i1_ / 3][i1_ % 3];
          u32x4 v = {cvtpk(a[0], a[1]), cvtpk(a[2], a[3]), cvtpk(c[0], c[1]), cvtpk(c[2], c[3])};
          asm volatile("global_store_dwordx4 %0, %1, off sc0 sc1\n\ts_nop 1" :: "v"(dst + m * 1024), "v"(v) : "memory");
        }
      }
    }
    asm volatile("s_waitcnt vmcnt(0)" ::: "memory");
    __syncthreads();
    unsigned* cnt = (unsigned*)(ws + WS_CNT) + tile * 16;
    if (tid == 0) {
      __hip_atomic_fetch_add(cnt, 1u, __ATOMIC_RELAXED, __HIP_MEMORY_SCOPE_AGENT);
      unsigned spins = 0;
      while (__hip_atomic_load(cnt, __ATOMIC_RELAXED, __HIP_MEMORY_SCOPE_AGENT) < 4u && ++spins < (1u << 24)) __builtin_amdgcn_s_sleep(1);
    }
    __syncthreads();
  }
  {
    u32x4 p[3][2];
#pragma unroll
    for (int r = 0; r < 3; ++r) {
      const int src = (ks + 1 + r) & 3;
      const char* th = slab + (size_t)((ks * 4 + src) * 8 + w) * 2048 + l16;
      asm volatile("global_load_dwordx4 %0, %1, off sc0 sc1" : "=v"(p[r][0]) : "v"(th) : "memory");
      asm volatile("global_load_dwordx4 %0, %1, off sc0 sc1" : "=v"(p[r][1]) : "v"(th + 1024) : "memory");
    }
    asm volatile("s_waitcnt vmcnt(0)" : "+v"(p[0][0]), "+v"(p[0][1]), "+v"(p[1][0]), "+v"(p[1][1]), "+v"(p[2][0]), "+v"(p[2][1]) :: "memory");
#pragma unroll
    for (int i = 0; i < 15; ++i) {
      if ((i & 3) == ks) {
        const int m = i >> 3, hf = (i >> 2) & 1;
        f32x4 o = acc[i / 3][i % 3];
#pragma unroll
        for (int r = 0; r < 3; ++r) {
          const unsigned u0 = p[r][m][2 * hf], u1 = p[r][m][2 * hf + 1];
          o[0] += __uint_as_float(u0 << 16); o[1] += __uint_as_float(u0 & 0xFFFF0000u);
          o[2] += __uint_as_float(u1 << 16); o[3] += __uint_as_float(u1 & 0xFFFF0000u);
        }
        u32x2 f = {cvtpk(o[0], o[1]), cvtpk(o[2], o[3])};
        char* dst = out + ((size_t)(b * SQ + (3 * qg + i % 3) * 16 + i16) * (2 * DM) + br * DM + nq * 160 + (5 * kh + i / 3) * 16 + 4 * g) * 2;
        *(u32x2*)dst = f;
      }
    }
  }
}

extern "C" void kernel_launch(void* const* d_in, const int* in_sizes, int n_in, void* d_out, int out_size, void* d_ws, size_t ws_size,
                              hipStream_t stream) {
  Ptrs P;
  for (int r = 0; r < 2; ++r) {
    P.x[r] = (const char*)d_in[5 * r + 0];
    P.k[r] = (const char*)d_in[5 * r + 1];
    P.v[r] = (const char*)d_in[5 * r + 2];
    P.wq[r] = (const char*)d_in[5 * r + 3];
    P.wo[r] = (const char*)d_in[5 * r + 4];
  }
  (void)hipFuncSetAttribute((const void*)k_attn, hipFuncAttributeMaxDynamicSharedMemorySize, LDS1);
  (void)hipFuncSetAttribute((const void*)k_oproj, hipFuncAttributeMaxDynamicSharedMemorySize, LDS2);
  hipLaunchKernelGGL(k_attn, dim3(256), dim3(256), LDS1, stream, P, (char*)d_ws);
  hipLaunchKernelGGL(k_oproj, dim3(256), dim3(512), LDS2, stream, P, (char*)d_ws, (char*)d_out);
}
```

```cpp
#include <hip/hip_runtime.h>
#include <hip/hip_bf16.h>
#include <stdint.h>
#include <utility>

using bf16x8 = __attribute__((ext_vector_type(8))) short;
using s16x4 = __attribute__((ext_vector_type(4))) short;
using f32x4 = __attribute__((ext_vector_type(4))) float;
using u32x4 = __attribute__((ext_vector_type(4))) unsigned;
using u32x2 = __attribute__((ext_vector_type(2))) unsigned;
typedef short v4i16_t __attribute__((ext_vector_type(4)));
typedef __attribute__((address_space(3))) char* lds_ptr;
typedef __attribute__((address_space(3))) const char* lds_cptr;

constexpr int NB = 8, SQ = 192, DM = 640, NH = 32, SK = 512, DH = 192;
constexpr int HD = NH * DH;
constexpr int SLOT = 24576;
constexpr int NSLOT = 6;
constexpr int LDS1 = SLOT * NSLOT;
constexpr float QSCALE = 0.07216878364870322f * 1.4426950408889634f;
constexpr float THR = 8.0f;

#define DI __device__ __forceinline__
#define MFMA16(a, b, c) __builtin_amdgcn_mfma_f32_16x16x32_bf16((a), (b), (c), 0, 0, 0)
#define ACLOB "a0","a1","a2","a3","a4","a5","a6","a7","a8","a9","a10","a11","a12","a13","a14","a15","a16","a17","a18","a19","a20","a21","a22","a23","a24","a25","a26","a27","a28","a29","a30","a31","a32","a33","a34","a35","a36","a37","a38","a39","a40","a41","a42","a43","a44","a45","a46","a47","a48","a49","a50","a51","a52","a53","a54","a55","a56","a57","a58","a59","a60","a61","a62","a63","a64","a65","a66","a67","a68","a69","a70","a71","a72","a73","a74","a75","a76","a77","a78","a79","a80","a81","a82","a83","a84","a85","a86","a87","a88","a89","a90","a91","a92","a93","a94","a95","a96","a97","a98","a99","a100","a101","a102","a103","a104","a105","a106","a107","a108","a109","a110","a111","a112","a113","a114","a115","a116","a117","a118","a119","a120","a121","a122","a123","a124","a125","a126","a127","a128","a129","a130","a131","a132","a133","a134","a135","a136","a137","a138","a139","a140","a141","a142","a143","a144","a145","a146","a147","a148","a149","a150","a151","a152","a153","a154","a155","a156","a157","a158","a159","a160","a161","a162","a163","a164","a165","a166","a167","a168","a169","a170","a171","a172","a173","a174","a175","a176","a177","a178","a179","a180","a181","a182","a183","a184","a185","a186","a187","a188","a189","a190","a191","a192","a193","a194","a195","a196","a197","a198","a199","a200","a201","a202","a203","a204","a205","a206","a207","a208","a209","a210","a211","a212","a213","a214","a215","a216","a217","a218","a219","a220","a221","a222","a223","a224","a225","a226","a227"
#define AO(d, q) (4 * (3 * (d) + (q)))
#define AQ(s, q) (156 + 4 * (3 * (s) + (q)))
template <int N> DI void mfma_o_(const bf16x8& a, const bf16x8& b) { asm volatile("v_mfma_f32_16x16x32_bf16 a[%c2:%c3], %0, %1, a[%c2:%c3]" :: "v"(a), "v"(b), "i"(N), "i"(N + 3) : ACLOB); }
template <int N> DI void mfma_o0_(const bf16x8& a, const bf16x8& b) { asm volatile("v_mfma_f32_16x16x32_bf16 a[%c2:%c3], %0, %1, 0" :: "v"(a), "v"(b), "i"(N), "i"(N + 3) : ACLOB); }
template <int N> DI void mfma_s_(f32x4& acc, const bf16x8& a) { asm volatile("v_mfma_f32_16x16x32_bf16 %0, %1, a[%c2:%c3], %0" : "+v"(acc) : "v"(a), "i"(N), "i"(N + 3) : ACLOB); }
template <int N> DI void mfma_sc_(f32x4& acc, const bf16x8& a, const f32x4& c) { asm volatile("v_mfma_f32_16x16x32_bf16 %0, %1, a[%c2:%c3], %4" : "=&v"(acc) : "v"(a), "i"(N), "i"(N + 3), "v"(c) : ACLOB); }
template <int N> DI void mfma_s0_(f32x4& acc, const bf16x8& a) { asm volatile("v_mfma_f32_16x16x32_bf16 %0, %1, a[%c2:%c3], 0" : "=&v"(acc) : "v"(a), "i"(N), "i"(N + 3) : ACLOB); }
template <int N> DI float ard_() { float r; asm volatile("v_accvgpr_read_b32 %0, a[%c1]" : "=v"(r) : "i"(N)); return r; }
template <int N, class T> DI void awr_(T src) { asm volatile("v_accvgpr_write_b32 a[%c1], %0" :: "v"(src), "i"(N)); }
template <int N> DI void ascale_(float f) { float t; asm volatile("v_accvgpr_read_b32 %0, a[%c1]\n\tv_mul_f32 %0, %2, %0\n\tv_accvgpr_write_b32 a[%c1], %0" : "=&v"(t) : "i"(N), "v"(f)); }
DI float subip_(float x, float d) { asm volatile("v_sub_f32 %0, %0, %1" : "+v"(x) : "v"(d)); return x; }
DI void mfma_ip_(f32x4& x, const bf16x8& a, const bf16x8& b) { asm volatile("v_mfma_f32_16x16x32_bf16 %0, %1, %2, %0" : "+v"(x) : "v"(a), "v"(b) : ACLOB); }
template <class T> DI T pin_(T x) { asm volatile("" : "+v"(x)); return x; }
template <class T> DI void pin3n_(T& a, T& b, T& c) { asm volatile("s_nop 1" : "+v"(a), "+v"(b), "+v"(c)); }
#define MFMA_O(d, q, a, b) mfma_o_<AO(d, q)>(a, b)
#define MFMA_O0(d, q, a, b) mfma_o0_<AO(d, q)>(a, b)
#define MFMA_S(acc, a, s, q) mfma_s_<AQ(s, q)>(acc, a)
#define MFMA_SC(acc, a, s, q, c) mfma_sc_<AQ(s, q)>(acc, a, c)
#define ARD(dst, n) dst = ard_<n>()
#define AWR(n, src) awr_<n>(src)
#define ASCALE(n, f) ascale_<n>(f)
#define SUBIP(x, d) x = subip_(x, d)
#define NOPS(n) asm volatile("s_nop " #n ::: "memory")
#define SBAR() __builtin_amdgcn_sched_barrier(0)
#define PINF(x) x = pin_(x)
#define PINU(x) x = pin_(x)
#define WAITV_BAR(N) asm volatile("s_waitcnt vmcnt(" #N ") lgkmcnt(0)\n\ts_barrier" ::: "memory")

DI unsigned cvtpk(float lo, float hi) {
  typedef float f32x2 __attribute__((ext_vector_type(2)));
  typedef __bf16 bf16x2 __attribute__((ext_vector_type(2)));
  f32x2 v = {lo, hi}; bf16x2 b = __builtin_convertvector(v, bf16x2); return __builtin_bit_cast(unsigned, b);
}
DI void glds16(unsigned voff, const char* sbase, unsigned ldsaddr) {
  asm volatile("s_mov_b32 m0, %2\n\ts_nop 0\n\tglobal_load_lds_dwordx4 %0, %1" :: "v"(voff), "s"(sbase), "s"(ldsaddr) : "memory");
}
DI void glds16nt(unsigned voff, const char* sbase, unsigned ldsaddr) {
  asm volatile("s_mov_b32 m0, %2\n\ts_nop 0\n\tglobal_load_lds_dwordx4 %0, %1 nt" :: "v"(voff), "s"(sbase), "s"(ldsaddr) : "memory");
}
DI void setm0(unsigned ldsaddr) { asm volatile("s_mov_b32 m0, %0\n\ts_nop 0" :: "s"(ldsaddr) : "memory"); }
template <int OFF, int NT> DI void gldso(unsigned voff, const char* sbase) {
  if constexpr (NT) asm volatile("global_load_lds_dwordx4 %0, %1 offset:%2 nt" :: "v"(voff), "s"(sbase), "i"(OFF) : "memory");
  else asm volatile("global_load_lds_dwordx4 %0, %1 offset:%2" :: "v"(voff), "s"(sbase), "i"(OFF) : "memory");
}
DI bf16x8 ldsb128(unsigned p) { return *(const __attribute__((address_space(3))) bf16x8*)p; }
DI s16x4 ldstr(unsigned p) { return __builtin_bit_cast(s16x4, __builtin_amdgcn_ds_read_tr16_b64_v4i16((__attribute__((address_space(3))) v4i16_t*)p)); }
template <int X> DI unsigned adr(unsigned base, unsigned off) {
  unsigned r;
  if constexpr (X == 0) asm volatile("v_add_u32 %0, %2, %1" : "=v"(r) : "v"(base), "s"(off));
  else asm volatile("v_xor_b32 %0, %3, %1\n\tv_add_u32 %0, %2, %0" : "=&v"(r) : "v"(base), "s"(off), "i"(X));
  return r;
}
DI unsigned opq(unsigned x) { asm volatile("" : "+v"(x)); return x; }
DI bf16x8 cat8(s16x4 lo, s16x4 hi) { return __builtin_shufflevector(lo, hi, 0, 1, 2, 3, 4, 5, 6, 7); }
DI float mx3(float a, float b, float c) { float r; asm("v_max3_f32 %0, %1, %2, %3" : "=v"(r) : "v"(a), "v"(b), "v"(c)); return r; }
DI float mx3c(float a, float b, float c) { return __builtin_fmaxf(__builtin_fmaxf(a, b), c); }
DI float mx2(float a, float b) { float r; asm("v_max_f32_e32 %0, %1, %2" : "=v"(r) : "v"(a), "v"(b)); return r; }
DI float xmax(float v) {
  auto a = __builtin_amdgcn_permlane32_swap(__float_as_uint(v), __float_as_uint(v), false, false);
  v = mx2(__uint_as_float(a[0]), __uint_as_float(a[1]));
  auto b = __builtin_amdgcn_permlane16_swap(__float_as_uint(v), __float_as_uint(v), false, false);
  return mx2(__uint_as_float(b[0]), __uint_as_float(b[1]));
}
DI float xsum(float v) {
  auto a = __builtin_amdgcn_permlane32_swap(__float_as_uint(v), __float_as_uint(v), false, false);
  v = __uint_as_float(a[0]) + __uint_as_float(a[1]);
  auto b = __builtin_amdgcn_permlane16_swap(__float_as_uint(v), __float_as_uint(v), false, false);
  return __uint_as_float(b[0]) + __uint_as_float(b[1]);
}

template <int N> struct IC { static constexpr int value = N; };
#define AI __attribute__((always_inline))
template <class F, int... Is> DI void sfor_(F&& f, std::integer_sequence<int, Is...>) { (f(IC<Is>{}), ...); }
template <int N, class F> DI void sfor(F&& f) { sfor_(f, std::make_integer_sequence<int, N>{}); }
struct Ptrs { const char* x[2]; const char* k[2]; const char* v[2]; const char* wq[2]; const char* wo[2]; };

__global__ __launch_bounds__(256, 1) void k_attn(Ptrs P, char* __restrict__ ofr) {
  extern __shared__ __attribute__((aligned(16))) char smem[];
  lds_ptr lds = (lds_ptr)smem;
  const int tid = threadIdx.x, l = tid & 63, w = __builtin_amdgcn_readfirstlane(tid >> 6);
  const int i16 = l & 15, g = l >> 4, qq = i16 >> 2, pp = i16 & 3;
  const int xcd = blockIdx.x & 7, idx = blockIdx.x >> 3;
  const int br = xcd >> 2, h = 8 * (xcd & 3) + (idx & 7), b0 = idx >> 3;
  const char* xg = P.x[br] + (size_t)b0 * SQ * DM * 2;
  const char* wg = P.wq[br] + (size_t)h * DH * 2;
  const char* kg = P.k[br] + (size_t)(b0 * NH + h) * SK * DH * 2;
  const char* vg = P.v[br] + (size_t)(b0 * NH + h) * SK * DH * 2;
  char* og = ofr + (((size_t)(br * NB + b0) * 192 + h * 6) * 12 + 3 * w) * 1024 + l * 16;

  if (blockIdx.x == 0 && tid < 64) ((unsigned*)(ofr + ((size_t)48 << 20)))[tid * 16] = 0u;
  unsigned offK[3], offV[3], offW[3], offX[6];
#pragma unroll
  for (int u = 0; u < 3; ++u) {
    const int o = 1024 * u + 16 * l, r = o / 384, chp = (o % 384) >> 4, row = 8 * w + r;
    const int aK = (row >> 1) & 7;
    offK[u] = row * 384 + (((chp & ~7) | ((chp ^ aK) & 7)) << 4) + 128 - 1024 * u;
    offV[u] = row * 384 + ((chp ^ (((row >> 1) & 3) << 1)) << 4) + 128 - 1024 * u;
    const int sW = ((row >> 1) & 1) | (((row >> 3) & 1) << 1);
    offW[u] = row * (HD * 2) + ((chp ^ (sW << 1)) << 4) + 128 - 1024 * u;
  }
#pragma unroll
  for (int n = 0; n < 6; ++n) {
    const int row = 48 * w + 8 * n + (l >> 3), a = (row >> 1) & 7;
    offX[n] = row * (DM * 2) + (((l & 7) ^ a) << 4) - 1024 * (n % 3);
  }
  xg -= 0; wg -= 128; kg -= 128; vg -= 128;

  const unsigned oK0 = offK[0], oK1 = offK[1], oK2 = offK[2], oV0 = offV[0], oV1 = offV[1], oV2 = offV[2], oW0 = offW[0], oW1 = offW[1], oW2 = offW[2];
  const unsigned oX0 = offX[0], oX1 = offX[1], oX2 = offX[2], oX3 = offX[3], oX4 = offX[4], oX5 = offX[5];
  const unsigned lds0 = (unsigned)(uintptr_t)lds;
  auto px = [&](int c, int slot, auto N) AI {
    constexpr int n = N.value, u = n % 3, hf = n / 3;
    if constexpr (u == 0) setm0(lds0 + slot * SLOT + w * 6144 + hf * 3072);
    gldso<1024 * u, 0>(n == 0 ? oX0 : n == 1 ? oX1 : n == 2 ? oX2 : n == 3 ? oX3 : n == 4 ? oX4 : oX5, xg + c * 128);
  };
  auto pw_ = [&](int c, int slot, auto N) AI {
    constexpr int n = N.value, u = n % 3, hf = n / 3;
    if constexpr (u == 0) setm0(lds0 + slot * SLOT + w * 3072 + hf * 12288);
    gldso<1024 * u, 0>(u == 0 ? oW0 : u == 1 ? oW1 : oW2, wg + (size_t)(c * 64 + hf * 32) * HD * 2);
  };
  auto pk = [&](int j, int slot, auto N) AI {
    constexpr int n = N.value, u = n % 3, hf = n / 3;
    if constexpr (u == 0) setm0(lds0 + slot * SLOT + w * 3072 + hf * 12288);
    gldso<1024 * u, 1>(u == 0 ? oK0 : u == 1 ? oK1 : oK2, kg + (size_t)j * SLOT + hf * 12288);
  };
  auto pv = [&](int j, int slot, auto N) AI {
    constexpr int n = N.value, u = n % 3, hf = n / 3;
    if constexpr (u == 0) setm0(lds0 + slot * SLOT + w * 3072 + hf * 12288);
    gldso<1024 * u, 1>(u == 0 ? oV0 : u == 1 ? oV1 : oV2, vg + (size_t)j * SLOT + hf * 12288);
  };
  auto pf_ = [&](auto F, auto N) AI {
    constexpr int f = F.value;
    if constexpr (f >= 36) { if constexpr ((f & 1) == 0) px((f - 36) >> 1, f % NSLOT, N); else pw_((f - 36) >> 1, f % NSLOT, N); }
    else if constexpr ((f & 1) == 0) pk((f - 20) >> 1, f % NSLOT, N); else pv((f - 20) >> 1, f % NSLOT, N);
  };

  const int a8 = i16 >> 1;
  const unsigned kb0 = lds0 + i16 * 384 + ((g ^ a8) << 4);
  const unsigned xb0 = lds0 + (48 * w + i16) * 128 + ((g ^ a8) << 4);
  const int swz = 2 * (g & 1) + (qq >> 1);
  const unsigned vb0 = lds0 + (4 * g + qq) * 384 + (swz << 5) + 8 * pp;
  const unsigned wb0 = lds0 + (8 * g + qq) * 384 + (swz << 5) + 8 * pp;

  asm volatile("" ::: ACLOB);
  f32x4 qa[12][3];
  float inv = 0.f; f32x4 eX, eY;
  auto epi = [&](auto U, auto Pt) AI {
    constexpr int u = U.value, q = u / 6, t = u % 6, part = Pt.value;
    if constexpr (part == 0) {
      if constexpr (t == 0) { float lq; ARD(lq, AO(12, q)); inv = __builtin_amdgcn_rcpf(lq); }
      sfor<4>([&](auto R) AI { ARD(eX[R.value], AO(2 * t, q) + R.value); });
    } else if constexpr (part == 1) {
      sfor<4>([&](auto R) AI { ARD(eY[R.value], AO(2 * t + 1, q) + R.value); });
    } else if constexpr (part == 2) { eX *= inv; eY *= inv; PINF(eX); PINF(eY); }
    else if constexpr (part == 3) {
      u32x4 f = {cvtpk(eX[0], eX[1]), cvtpk(eX[2], eX[3]), cvtpk(eY[0], eY[1]), cvtpk(eY[2], eY[3])};
      *(u32x4*)(og + q * 1024 + t * 12288) = f;
    }
  };
  int sl = 0;
  unsigned ax[2], aw[4], ak[2];
  bf16x8 xf[2][3], wf[4], kf[3];
  auto rdw = [&](auto F) AI {
    constexpr int f = F.value, s = f / 12, d = f % 12;
    const unsigned p = aw[d & 3] + s * 32 * 384 + (d >> 2) * 128;
    wf[f & 3] = cat8(ldstr(p), ldstr(p + 4 * 384));
  };
  auto rdk = [&](auto F) AI { constexpr int f = F.value, s = f >> 2, kb = f & 3; kf[f % 3] = ldsb128(ak[s & 1] + kb * 6144 + (s >> 1) * 128); };
  auto p1head = [&]() AI {
    ax[0] = adr<0>(xb0, sl * SLOT); ax[1] = adr<64>(xb0, sl * SLOT);
    aw[0] = adr<0>(wb0, (sl + 1) * SLOT); aw[1] = adr<32>(wb0, (sl + 1) * SLOT); aw[2] = adr<64>(wb0, (sl + 1) * SLOT); aw[3] = adr<96>(wb0, (sl + 1) * SLOT);
#pragma unroll
    for (int q = 0; q < 3; ++q) xf[0][q] = ldsb128(ax[0] + q * 2048);
    rdw(IC<0>{}); rdw(IC<1>{});
  };
  auto khead = [&](auto Sc) AI { ak[0] = adr<0>(kb0, Sc.value * SLOT); ak[1] = adr<64>(kb0, Sc.value * SLOT); rdk(IC<0>{}); rdk(IC<1>{}); };
  auto p1step = [&](int c, auto Tc, auto Ec, auto Nc) AI {
    constexpr int TAIL = Tc.value, EPI = Ec.value, NEXT = Nc.value;
    const int sn = sl >= 2 ? sl - 2 : sl + 4;
    SBAR();
    sfor<24>([&](auto F) AI {
      constexpr int f = F.value, s = f / 12, d = f % 12;
      if constexpr (f == 22) {
        sl = sl == 4 ? 0 : sl + 2;
        if constexpr (NEXT == 0) { WAITV_BAR(12); p1head(); } else { WAITV_BAR(18); khead(IC<2>{}); }
        SBAR();
      }
      if constexpr (f + 2 < 24) rdw(IC<f + 2>{});
      if constexpr (f >= 2 && f < 5) xf[1][f - 2] = ldsb128(ax[1] + (f - 2) * 2048);
      mfma_ip_(qa[d][0], wf[f & 3], xf[s][0]);
      if constexpr (EPI >= 0 && EPI < 9) { constexpr int k = f / 12, part = (f % 12) / 2 - 1;
        if constexpr ((f & 1) == 1 && part >= 0 && part < 4) { epi(IC<2 * EPI + k>{}, IC<part>{}); SBAR(); } }
      mfma_ip_(qa[d][1], wf[f & 3], xf[s][1]);
      if constexpr (((f & 1) == 0 && f <= 20) || f == 21) {
        constexpr int n = f == 21 ? 11 : f >> 1;
        if constexpr (n < 6) { if constexpr (!TAIL) px(c + 2, sn, IC<n>{}); else pk(c - 8, sn, IC<n>{}); }
        else { if constexpr (!TAIL) pw_(c + 2, sn + 1, IC<n - 6>{}); else pv(c - 8, sn + 1, IC<n - 6>{}); }
      }
      mfma_ip_(qa[d][2], wf[f & 3], xf[s][2]);
      SBAR();
    });
  };
  auto zero_qa = [&]() AI {
    float z = 0.f; PINF(z);
#pragma unroll
    for (int d = 0; d < 12; ++d) {
#pragma unroll
      for (int q = 0; q < 3; ++q) qa[d][q] = f32x4{z, z, z, z};
      pin3n_(qa[d][0], qa[d][1], qa[d][2]);
    }
  };

  f32x4 sc[2][4][3];
  f32x4 negm[3];
  unsigned pw[2][3][4];
  bf16x8 ones = {0x3F80, 0x3F80, 0x3F80, 0x3F80, 0x3F80, 0x3F80, 0x3F80, 0x3F80};
  asm volatile("" : "+v"(ones));

#define EXP1(X, kb, q, r) do { sc[X][kb][q][r] = __builtin_amdgcn_exp2f(sc[X][kb][q][r]); PINF(sc[X][kb][q][r]); } while (0)
#define CVT1(X, c) do { constexpr int kb_ = (c) / 6, q_ = ((c) % 6) >> 1, hf_ = (c) & 1; \
    pw[kb_ >> 1][q_][2 * (kb_ & 1) + hf_] = cvtpk(sc[X][kb_][q_][2 * hf_], sc[X][kb_][q_][2 * hf_ + 1]); PINU(pw[kb_ >> 1][q_][2 * (kb_ & 1) + hf_]); } while (0)

  auto conv = [&](auto S, auto Q) AI {
    constexpr int s = S.value, q = Q.value;
    const f32x4 X = qa[2 * s][q] * QSCALE, Y = qa[2 * s + 1][q] * QSCALE;
    unsigned x0 = cvtpk(X[0], X[1]), x1 = cvtpk(X[2], X[3]), y0 = cvtpk(Y[0], Y[1]), y1 = cvtpk(Y[2], Y[3]);
    auto r0 = __builtin_amdgcn_permlane32_swap(x0, y0, false, false);
    auto r1 = __builtin_amdgcn_permlane32_swap(x1, y1, false, false);
    auto t0 = __builtin_amdgcn_permlane16_swap(r0[0], r0[1], false, false);
    auto t1 = __builtin_amdgcn_permlane16_swap(r1[0], r1[1], false, false);
    AWR(AQ(s, q) + 0, t0[0]); AWR(AQ(s, q) + 1, t1[0]); AWR(AQ(s, q) + 2, t0[1]); AWR(AQ(s, q) + 3, t1[1]);
  };

  sfor<6>([&](auto N) AI { px(0, 0, N); }); sfor<6>([&](auto N) AI { pw_(0, 1, N); });
  sfor<6>([&](auto N) AI { px(1, 2, N); }); sfor<6>([&](auto N) AI { pw_(1, 3, N); });
  zero_qa();
  WAITV_BAR(12); p1head();
#pragma unroll 1
  for (int c = 0; c < 8; ++c) p1step(c, IC<0>{}, IC<-1>{}, IC<0>{});
  p1step(8, IC<1>{}, IC<-1>{}, IC<0>{}); p1step(9, IC<1>{}, IC<-1>{}, IC<1>{});

#pragma unroll 1
  for (int it = 0;; ++it) {
    asm volatile("" : "+s"(wg));
    if (it == 0) xg += (size_t)4 * SQ * DM * 2;
    {
      NOPS(7);
      SBAR();
      conv(IC<0>{}, IC<0>{}); conv(IC<0>{}, IC<1>{}); conv(IC<0>{}, IC<2>{});
      conv(IC<1>{}, IC<0>{}); conv(IC<1>{}, IC<1>{}); conv(IC<1>{}, IC<2>{});
      NOPS(1);
      SBAR();
      sfor<24>([&](auto F) AI {
        constexpr int f = F.value, s = f >> 2, kb = f & 3;
        if constexpr (f + 2 < 24) rdk(IC<f + 2>{});
        sfor<3>([&](auto Q) AI {
          constexpr int q = Q.value;
          if constexpr (s == 0) { mfma_s0_<AQ(s, q)>(sc[0][kb][q], kf[f % 3]); } else { MFMA_S(sc[0][kb][q], kf[f % 3], s, q); }
        });
        if constexpr (s < 4 && kb < 3) conv(IC<s + 2>{}, IC<kb>{});
        if constexpr (kb == 3) { pf_(IC<24 + s / 3>{}, IC<2 * (s % 3)>{}); pf_(IC<24 + s / 3>{}, IC<2 * (s % 3) + 1>{}); }
        SBAR();
      });
      WAITV_BAR(18); khead(IC<22 % NSLOT>{});
      asm volatile("s_nop 7" : "+v"(sc[0][0][0]), "+v"(sc[0][0][1]), "+v"(sc[0][0][2]), "+v"(sc[0][1][0]), "+v"(sc[0][1][1]), "+v"(sc[0][1][2]),
                               "+v"(sc[0][2][0]), "+v"(sc[0][2][1]), "+v"(sc[0][2][2]), "+v"(sc[0][3][0]), "+v"(sc[0][3][1]), "+v"(sc[0][3][2]));
#pragma unroll
      for (int q = 0; q < 3; ++q) {
        float m0 = mx3(sc[0][0][q][0], sc[0][0][q][1], sc[0][0][q][2]);
        m0 = mx2(m0, sc[0][0][q][3]);
#pragma unroll
        for (int kb = 1; kb < 4; ++kb) { m0 = mx3(m0, sc[0][kb][q][0], sc[0][kb][q][1]); m0 = mx3(m0, sc[0][kb][q][2], sc[0][kb][q][3]); }
        m0 = xmax(m0);
        negm[q] = f32x4{-m0, -m0, -m0, -m0};
#pragma unroll
        for (int kb = 0; kb < 4; ++kb) sc[0][kb][q] -= m0;
#pragma unroll
        for (int r = 0; r < 4; ++r) sc[0][0][q][r] = __builtin_amdgcn_exp2f(sc[0][0][q][r]);
      }
    }
    sfor<8>([&](auto Jc) AI {
      constexpr int j = Jc.value;
      constexpr int X = j & 1, Y = X ^ 1;
      if constexpr (j < 7) {
        pin3n_(negm[0], negm[1], negm[2]);
        SBAR();
        sfor<24>([&](auto F) AI {
          constexpr int f = F.value, s = f >> 2, kb = f & 3;
          if constexpr (f + 2 < 24) rdk(IC<f + 2>{});
          sfor<3>([&](auto Q) AI {
            constexpr int q = Q.value;
            if constexpr (s == 0) { MFMA_SC(sc[Y][kb][q], kf[f % 3], s, q, negm[q]); } else { MFMA_S(sc[Y][kb][q], kf[f % 3], s, q); }
            if constexpr (q == 0) { CVT1(X, f); }
            else {
              constexpr int e = 2 * f + q - 1;
              if constexpr (e < 36) { EXP1(X, 1 + e / 12, (e % 12) >> 2, e & 3); }
              else if constexpr (q == 1 && j >= 1) pf_(IC<25 + 2 * j>{}, IC<f - 18>{});
            }
            SBAR();
          });
        });
      } else {
        sfor<36>([&](auto E) AI { constexpr int e = E.value; EXP1(X, 1 + e / 12, (e % 12) >> 2, e & 3);
          if constexpr ((e % 6) == 5) pf_(IC<39>{}, IC<e / 6>{}); });
        sfor<24>([&](auto C) AI { CVT1(X, C.value); });
      }
      {
        bf16x8 pf[2][3];
#pragma unroll
        for (int t = 0; t < 2; ++t)
#pragma unroll
          for (int q = 0; q < 3; ++q) { u32x4 f = {pw[t][q][0], pw[t][q][1], pw[t][q][2], pw[t][q][3]}; pf[t][q] = __builtin_bit_cast(bf16x8, f); }
        pin3n_(pf[0][0], pf[0][1], pf[0][2]); pin3n_(pf[1][0], pf[1][1], pf[1][2]);
        unsigned av[4];
        { constexpr unsigned so = ((21 + 2 * j) % NSLOT) * SLOT;
          av[0] = adr<0>(vb0, so); av[1] = adr<32>(vb0, so); av[2] = adr<64>(vb0, so); av[3] = adr<96>(vb0, so); }
        bf16x8 vf[3];
        auto rdv = [&](auto F) AI {
          constexpr int f = F.value, t = f / 12, d = f % 12;
          const unsigned p = av[d & 3] + t * 32 * 384 + (d >> 2) * 128;
          vf[f % 3] = cat8(ldstr(p), ldstr(p + 16 * 384));
        };
        float mx[3], fct[3];
        bool pend = false;
        auto mop = [&](auto I) AI {
          constexpr int q = I.value % 3, k = I.value / 3;
          if constexpr (k == 0) mx[q] = mx2(sc[Y][0][q][0], sc[Y][0][q][1]);
          else { constexpr int e = 2 * k; mx[q] = mx3c(mx[q], sc[Y][e >> 2][q][e & 3], sc[Y][e >> 2][q][(e & 3) + 1]); }
          PINF(mx[q]);
        };
        rdv(IC<0>{}); rdv(IC<1>{});
        SBAR();
        sfor<78>([&](auto Mc) AI {
          constexpr int m = Mc.value, G = m / 3, q = m % 3, t = G / 13, gi = G % 13;
          if constexpr (m == 72) {
            if constexpr (j < 6) { WAITV_BAR(12); khead(IC<(24 + 2 * j) % NSLOT>{}); }
            else if constexpr (j == 6) { WAITV_BAR(18); }
            else { WAITV_BAR(12); sl = 0; p1head(); }
            SBAR();
          }
          if constexpr (gi == 0) {
            if constexpr (j == 0 && t == 0) { MFMA_O0(12, q, ones, pf[t][q]); } else { MFMA_O(12, q, ones, pf[t][q]); }
          } else {
            constexpr int d = gi - 1, f = 12 * t + d;
            if constexpr (q == 0 && f + 2 < 24) rdv(IC<f + 2>{});
            if constexpr (j == 0 && t == 0) { MFMA_O0(d, q, vf[f % 3], pf[t][q]); } else { MFMA_O(d, q, vf[f % 3], pf[t][q]); }
          }
          if constexpr (j < 7 && q < 2) {
            if constexpr (G >= 2 && G < 8) { constexpr int i = 4 * (G - 2) + 2 * q; mop(IC<i>{}); mop(IC<i + 1>{}); }
            if constexpr (G == 8 && q == 0) {
              pend = __any(mx3c(mx[0], mx[1], mx[2]) > THR);
              if (__builtin_expect(pend, 0)) {
#pragma unroll
                for (int qq_ = 0; qq_ < 3; ++qq_) {
                  const float gr = xmax(mx[qq_]);
                  const unsigned db = (__float_as_uint(fmaxf(gr, 0.f)) + 0xFFFFu) & 0xFFFF0000u;
                  fct[qq_] = __builtin_amdgcn_exp2f(-__uint_as_float(db));
                  u32x4 bw = {g == 0 ? ((db >> 16) | 0x8000u) : 0u, 0u, 0u, 0u};
                  bf16x8 bfr = __builtin_bit_cast(bf16x8, bw);
                  asm volatile("s_nop 1" : "+v"(bfr));
                  mfma_ip_(negm[qq_], ones, bfr);
                  mfma_ip_(sc[Y][0][qq_], ones, bfr); mfma_ip_(sc[Y][1][qq_], ones, bfr); mfma_ip_(sc[Y][2][qq_], ones, bfr); mfma_ip_(sc[Y][3][qq_], ones, bfr);
                }
                NOPS(7);
              }
            }
            if constexpr (G >= 9 && G < 15) { constexpr int e = 2 * (G - 9) + q; EXP1(Y, 0, e >> 2, e & 3); }
          }
          if constexpr (j < 7 && q == 1 && G >= 16 && G < 22) pf_(IC<26 + 2 * j>{}, IC<G - 16>{});
          SBAR();
        });
        if constexpr (j < 7) {
          if (__builtin_expect(pend, 0)) {
            NOPS(7);
            sfor<156>([&](auto N) AI { ASCALE(N.value, fct[(N.value >> 2) % 3]); });
            NOPS(1);
          }
        }
      }
    });
    if (it == 1) break;
    kg += (size_t)4 * NH * SK * DH * 2; vg += (size_t)4 * NH * SK * DH * 2;
    zero_qa();
    sfor<10>([&](auto C) AI { p1step(C.value, IC<(C.value >= 8)>{}, IC<C.value>{}, IC<(C.value == 9)>{}); });
    og += (size_t)4 * 192 * 12 * 1024;
  }
  NOPS(7);
  sfor<18>([&](auto U) AI { epi(U, IC<0>{}); epi(U, IC<1>{}); epi(U, IC<2>{}); epi(U, IC<3>{}); });
  asm volatile("s_waitcnt vmcnt(0)" ::: "memory");
}

constexpr int ST2 = 128 * 320;
constexpr int LDS2 = 3 * ST2;
constexpr size_t WS_SLAB = (size_t)64 << 20;
constexpr size_t WS_CNT = (size_t)48 << 20;

DI void gload16(bf16x8& d, unsigned voff, const char* sbase) { asm volatile("global_load_dwordx4 %0, %1, %2" : "=v"(d) : "v"(voff), "s"(sbase) : "memory"); }
template <int N> DI void wait3(bf16x8& a, bf16x8& b, bf16x8& c) { asm volatile("s_waitcnt vmcnt(%3)" : "+v"(a), "+v"(b), "+v"(c) : "i"(N) : "memory"); }

__global__ __launch_bounds__(512, 2) void k_oproj(Ptrs P, char* __restrict__ ws, char* __restrict__ out) {
  extern __shared__ __attribute__((aligned(16))) char smem[];
  lds_ptr lds = (lds_ptr)smem;
  const int tid = threadIdx.x, l = tid & 63, w = __builtin_amdgcn_readfirstlane(tid >> 6);
  const int i16 = l & 15, g = l >> 4, qq = i16 >> 2, pp = i16 & 3;
  const int xcd = blockIdx.x & 7, idx = blockIdx.x >> 3;
  const int br = xcd >> 2, ks = xcd & 3, b = idx >> 2, nq = idx & 3;
  const int kh = w >> 2, qg = w & 3;
  const unsigned lds0 = (unsigned)(uintptr_t)lds;
  const char* wsrc = P.wo[br] + (size_t)ks * 1536 * DM * 2 + nq * 320;
  const char* osrc = ws + (((size_t)(br * NB + b) * 192 + 48 * ks + kh) * 12 + 3 * qg) * 1024;
  const unsigned l16 = l * 16;
  unsigned offW[5];
#pragma unroll
  for (int u = 0; u < 5; ++u) {
    const int o = 1024 * u + 16 * l, r = o / 320, chp = (o % 320) >> 4;
    offW[u] = (16 * w + r) * (DM * 2) + ((chp ^ (((r >> 2) & 1) << 1)) << 4);
  }
  const unsigned o0 = offW[0], o1 = offW[1], o2 = offW[2], o3 = offW[3], o4 = offW[4];
  const int s0 = 0;
  auto wpiece = [&](const char* src, unsigned d, auto Uc) AI {
    constexpr int u = Uc.value;
    glds16(u == 0 ? o0 : u == 1 ? o1 : u == 2 ? o2 : u == 3 ? o3 : o4, src, d + u * 1024);
  };
  auto wsrc_of = [&](int s) AI { s += s0; s = s >= 12 ? s - 12 : s; return wsrc + (size_t)s * 128 * DM * 2; };
  auto osrc_of = [&](int u) AI { u += 2 * s0; u = u >= 24 ? u - 24 : u; return osrc + (size_t)u * 24576; };
  auto issueW = [&](int s, int stg) AI {
    const unsigned d = lds0 + stg * ST2 + w * 5120;
    const char* src = wsrc_of(s);
    sfor<5>([&](auto U) AI { wpiece(src, d, U); });
  };
  bf16x8 of[4][3];
  auto loadO = [&](int u, auto Bc) AI {
    constexpr int B_ = Bc.value;
    const char* src = osrc_of(u);
    gload16(of[B_][0], l16, src); gload16(of[B_][1], l16, src + 1024); gload16(of[B_][2], l16, src + 2048);
  };
  f32x4 acc[10][3];
#pragma unroll
  for (int n = 0; n < 10; ++n)
#pragma unroll
    for (int q = 0; q < 3; ++q) acc[n][q] = f32x4{0.f, 0.f, 0.f, 0.f};
  const unsigned wb0 = lds0 + (32 * kh + 4 * g + qq) * 320 + 8 * pp + ((g & 1) << 5);
  {
    unsigned tch;
    const char* wo = P.wo[br] + (size_t)ks * 1536 * DM * 2 + (size_t)(idx * 8 + w) * 60 * 128;
    asm volatile("global_load_dword %0, %1, %2" : "=v"(tch) : "v"((l < 60 ? l : 59) * 128), "s"(wo) : "memory");
    issueW(0, 0); loadO(0, IC<0>{}); loadO(1, IC<1>{}); loadO(2, IC<2>{});
    issueW(1, 1);
    asm volatile("s_waitcnt vmcnt(19)" : "+v"(tch) :: "memory");
  }
  int stg = 0;
  auto kstep = [&](unsigned ae, unsigned ao, auto Vc, auto Bc, auto Lc, const char* lsrc, const char* dsrc, unsigned dd, auto Dc) AI {
    constexpr int v = Vc.value, B_ = Bc.value, LB = Lc.value, DN = Dc.value;
    bf16x8 wf[3];
    auto rdw = [&](auto N) AI { constexpr int n = N.value; const unsigned p = ((n & 1) ? ao : ae) + v * 64 * 320 + (n >> 1) * 64; wf[n % 3] = cat8(ldstr(p), ldstr(p + 16 * 320)); };
    rdw(IC<0>{}); rdw(IC<1>{});
    SBAR();
    sfor<10>([&](auto N) AI {
      constexpr int n = N.value;
      if constexpr (n + 2 < 10) rdw(IC<n + 2>{});
      acc[n][0] = MFMA16(wf[n % 3], of[B_][0], acc[n][0]);
      if constexpr (LB >= 0 && n < 3) { SBAR(); gload16(of[LB][n], l16, lsrc + n * 1024); SBAR(); }
      if constexpr (DN > 0 && n >= 4 && n < 4 + 2 * DN && (n & 1) == 0) { SBAR(); wpiece(dsrc, dd, IC<(v ? 2 : 0) + (n - 4) / 2>{}); SBAR(); }
      acc[n][1] = MFMA16(wf[n % 3], of[B_][1], acc[n][1]);
      acc[n][2] = MFMA16(wf[n % 3], of[B_][2], acc[n][2]);
      SBAR();
    });
  };
  auto stage = [&](int s, auto Pc, auto Mc) AI {
    constexpr int PAR = Pc.value, MODE = Mc.value, B0 = 2 * PAR, B1 = 2 * PAR + 1;
    if constexpr (MODE == 2) { asm volatile("s_waitcnt vmcnt(3) lgkmcnt(0)\n\ts_barrier" ::: "memory"); }
    else { asm volatile("s_waitcnt vmcnt(11) lgkmcnt(0)\n\ts_barrier" ::: "memory"); }
    wait3<63>(of[B0][0], of[B0][1], of[B0][2]);
    const unsigned ae = opq(wb0 + stg * ST2), ao = opq((wb0 ^ 32) + stg * ST2);
    const unsigned dd = lds0 + (stg == 0 ? 2 : stg - 1) * ST2 + w * 5120;
    const char* dsrc = wsrc_of(s + 2);
    kstep(ae, ao, IC<0>{}, IC<B0>{}, IC<(MODE <= 1 ? ((B0 + 3) & 3) : -1)>{}, osrc_of(2 * s + 3), dsrc, dd, IC<(MODE == 0 ? 2 : 0)>{});
    wait3<MODE == 0 ? 13 : MODE == 1 ? 11 : 0>(of[B1][0], of[B1][1], of[B1][2]);
    kstep(ae, ao, IC<1>{}, IC<B1>{}, IC<(MODE == 0 ? B0 : -1)>{}, osrc_of(2 * s + 4), dsrc, dd, IC<(MODE == 0 ? 3 : 0)>{});
    stg = stg == 2 ? 0 : stg + 1;
  };
#pragma unroll 1
  for (int s = 0; s < 10; s += 2) { stage(s, IC<0>{}, IC<0>{}); stage(s + 1, IC<1>{}, IC<0>{}); }
  stage(10, IC<0>{}, IC<1>{}); stage(11, IC<1>{}, IC<2>{});
  asm volatile("s_waitcnt vmcnt(0) lgkmcnt(0)\n\ts_barrier" ::: "memory");
  {
    const unsigned xo = lds0 + ((kh ^ 1) * 4 + qg) * 15360 + l16, xi = lds0 + (kh * 4 + qg) * 15360 + l16;
#pragma unroll
    for (int n = 0; n < 5; ++n)
#pragma unroll
      for (int q = 0; q < 3; ++q) *(__attribute__((address_space(3))) f32x4*)(xo + (n * 3 + q) * 1024) = kh ? acc[n][q] : acc[5 + n][q];
    __syncthreads();
#pragma unroll
    for (int n = 0; n < 5; ++n)
#pragma unroll
      for (int q = 0; q < 3; ++q) {
        const f32x4 o = *(const __attribute__((address_space(3))) f32x4*)(xi + (n * 3 + q) * 1024);
        acc[n][q] = (kh ? acc[5 + n][q] : acc[n][q]) + o;
      }
  }
  const int tile = (br * NB + b) * 4 + nq;
  char* slab = ws + WS_SLAB + (size_t)tile * (16 * 8 * 2048);
  {
#pragma unroll
    for (int r = 0; r < 4; ++r) {
      if (r != ks) {
        char* dst = slab + (size_t)((r * 4 + ks) * 8 + w) * 2048 + l16;
#pragma unroll
        for (int m = 0; m < 2; ++m) {
          const int i0_ = 8 * m + r, i1_ = (8 * m + 4 + r) < 15 ? 8 * m + 4 + r : i0_;
          const f32x4 a = acc[i0_ / 3][i0_ % 3], c = acc[i1_ / 3][i1_ % 3];
          u32x4 v = {cvtpk(a[0], a[1]), cvtpk(a[2], a[3]), cvtpk(c[0], c[1]), cvtpk(c[2], c[3])};
          asm volatile("global_store_dwordx4 %0, %1, off sc0 sc1\n\ts_nop 1" :: "v"(dst + m * 1024), "v"(v) : "memory");
        }
      }
    }
    asm volatile("s_waitcnt vmcnt(0)" ::: "memory");
    __syncthreads();
    unsigned* cnt = (unsigned*)(ws + WS_CNT) + tile * 16;
    if (tid == 0) {
      __hip_atomic_fetch_add(cnt, 1u, __ATOMIC_RELAXED, __HIP_MEMORY_SCOPE_AGENT);
      unsigned spins = 0;
      while (__hip_atomic_load(cnt, __ATOMIC_RELAXED, __HIP_MEMORY_SCOPE_AGENT) < 4u && ++spins < (1u << 24)) __builtin_amdgcn_s_sleep(1);
    }
    __syncthreads();
  }
  {
    u32x4 p[3][2];
#pragma unroll
    for (int r = 0; r < 3; ++r) {
      const int src = (ks + 1 + r) & 3;
      const char* th = slab + (size_t)((ks * 4 + src) * 8 + w) * 2048 + l16;
      asm volatile("global_load_dwordx4 %0, %1, off sc0 sc1" : "=v"(p[r][0]) : "v"(th) : "memory");
      asm volatile("global_load_dwordx4 %0, %1, off sc0 sc1" : "=v"(p[r][1]) : "v"(th + 1024) : "memory");
    }
    asm volatile("s_waitcnt vmcnt(0)" : "+v"(p[0][0]), "+v"(p[0][1]), "+v"(p[1][0]), "+v"(p[1][1]), "+v"(p[2][0]), "+v"(p[2][1]) :: "memory");
#pragma unroll
    for (int i = 0; i < 15; ++i) {
      if ((i & 3) == ks) {
        const int m = i >> 3, hf = (i >> 2) & 1;
        f32x4 o = acc[i / 3][i % 3];
#pragma unroll
        for (int r = 0; r < 3; ++r) {
          const unsigned u0 = p[r][m][2 * hf], u1 = p[r][m][2 * hf + 1];
          o[0] += __uint_as_float(u0 << 16); o[1] += __uint_as_float(u0 & 0xFFFF0000u);
          o[2] += __uint_as_float(u1 << 16); o[3] += __uint_as_float(u1 & 0xFFFF0000u);
        }
        u32x2 f = {cvtpk(o[0], o[1]), cvtpk(o[2], o[3])};
        char* dst = out + ((size_t)(b * SQ + (3 * qg + i % 3) * 16 + i16) * (2 * DM) + br * DM + nq * 160 + (5 * kh + i / 3) * 16 + 4 * g) * 2;
        *(u32x2*)dst = f;
      }
    }
  }
}

extern "C" void kernel_launch(void* const* d_in, const int* in_sizes, int n_in, void* d_out, int out_size, void* d_ws, size_t ws_size,
                              hipStream_t stream) {
  Ptrs P;
  for (int r = 0; r < 2; ++r) {
    P.x[r] = (const char*)d_in[5 * r + 0];
    P.k[r] = (const char*)d_in[5 * r + 1];
    P.v[r] = (const char*)d_in[5 * r + 2];
    P.wq[r] = (const char*)d_in[5 * r + 3];
    P.wo[r] = (const char*)d_in[5 * r + 4];
  }
  (void)hipFuncSetAttribute((const void*)k_attn, hipFuncAttributeMaxDynamicSharedMemorySize, LDS1);
  (void)hipFuncSetAttribute((const void*)k_oproj, hipFuncAttributeMaxDynamicSharedMemorySize, LDS2);
  hipLaunchKernelGGL(k_attn, dim3(256), dim3(256), LDS1, stream, P, (char*)d_ws);
  hipLaunchKernelGGL(k_oproj, dim3(256), dim3(512), LDS2, stream, P, (char*)d_ws, (char*)d_out);
}
```

```cpp
#include <hip/hip_runtime.h>
#include <hip/hip_bf16.h>
#include <stdint.h>
#include <utility>

using bf16x8 = __attribute__((ext_vector_type(8))) short;
using s16x4 = __attribute__((ext_vector_type(4))) short;
using f32x4 = __attribute__((ext_vector_type(4))) float;
using u32x4 = __attribute__((ext_vector_type(4))) unsigned;
using u32x2 = __attribute__((ext_vector_type(2))) unsigned;
typedef short v4i16_t __attribute__((ext_vector_type(4)));
typedef __attribute__((address_space(3))) char* lds_ptr;
typedef __attribute__((address_space(3))) const char* lds_cptr;

constexpr int NB = 8, SQ = 192, DM = 640, NH = 32, SK = 512, DH = 192;
constexpr int HD = NH * DH;
constexpr int SLOT = 24576;
constexpr int NSLOT = 6;
constexpr int LDS1 = SLOT * NSLOT;
constexpr float QSCALE = 0.07216878364870322f * 1.4426950408889634f;
constexpr float THR = 8.0f;

#define DI __device__ __forceinline__
#define MFMA16(a, b, c) __builtin_amdgcn_mfma_f32_16x16x32_bf16((a), (b), (c), 0, 0, 0)
#define ACLOB "a0","a1","a2","a3","a4","a5","a6","a7","a8","a9","a10","a11","a12","a13","a14","a15","a16","a17","a18","a19","a20","a21","a22","a23","a24","a25","a26","a27","a28","a29","a30","a31","a32","a33","a34","a35","a36","a37","a38","a39","a40","a41","a42","a43","a44","a45","a46","a47","a48","a49","a50","a51","a52","a53","a54","a55","a56","a57","a58","a59","a60","a61","a62","a63","a64","a65","a66","a67","a68","a69","a70","a71","a72","a73","a74","a75","a76","a77","a78","a79","a80","a81","a82","a83","a84","a85","a86","a87","a88","a89","a90","a91","a92","a93","a94","a95","a96","a97","a98","a99","a100","a101","a102","a103","a104","a105","a106","a107","a108","a109","a110","a111","a112","a113","a114","a115","a116","a117","a118","a119","a120","a121","a122","a123","a124","a125","a126","a127","a128","a129","a130","a131","a132","a133","a134","a135","a136","a137","a138","a139","a140","a141","a142","a143","a144","a145","a146","a147","a148","a149","a150","a151","a152","a153","a154","a155","a156","a157","a158","a159","a160","a161","a162","a163","a164","a165","a166","a167","a168","a169","a170","a171","a172","a173","a174","a175","a176","a177","a178","a179","a180","a181","a182","a183","a184","a185","a186","a187","a188","a189","a190","a191","a192","a193","a194","a195","a196","a197","a198","a199","a200","a201","a202","a203","a204","a205","a206","a207","a208","a209","a210","a211","a212","a213","a214","a215","a216","a217","a218","a219","a220","a221","a222","a223","a224","a225","a226","a227"
#define AO(d, q) (4 * (3 * (d) + (q)))
#define AQ(s, q) (156 + 4 * (3 * (s) + (q)))
template <int N> DI void mfma_o_(const bf16x8& a, const bf16x8& b) { asm volatile("v_mfma_f32_16x16x32_bf16 a[%c2:%c3], %0, %1, a[%c2:%c3]" :: "v"(a), "v"(b), "i"(N), "i"(N + 3) : ACLOB); }
template <int N> DI void mfma_o0_(const bf16x8& a, const bf16x8& b) { asm volatile("v_mfma_f32_16x16x32_bf16 a[%c2:%c3], %0, %1, 0" :: "v"(a), "v"(b), "i"(N), "i"(N + 3) : ACLOB); }
template <int N> DI void mfma_s_(f32x4& acc, const bf16x8& a) { asm volatile("v_mfma_f32_16x16x32_bf16 %0, %1, a[%c2:%c3], %0" : "+v"(acc) : "v"(a), "i"(N), "i"(N + 3) : ACLOB); }
template <int N> DI void mfma_sc_(f32x4& acc, const bf16x8& a, const f32x4& c) { asm volatile("v_mfma_f32_16x16x32_bf16 %0, %1, a[%c2:%c3], %4" : "=&v"(acc) : "v"(a), "i"(N), "i"(N + 3), "v"(c) : ACLOB); }
template <int N> DI void mfma_s0_(f32x4& acc, const bf16x8& a) { asm volatile("v_mfma_f32_16x16x32_bf16 %0, %1, a[%c2:%c3], 0" : "=&v"(acc) : "v"(a), "i"(N), "i"(N + 3) : ACLOB); }
template <int N> DI float ard_() { float r; asm volatile("v_accvgpr_read_b32 %0, a[%c1]" : "=v"(r) : "i"(N)); return r; }
template <int N, class T> DI void awr_(T src) { asm volatile("v_accvgpr_write_b32 a[%c1], %0" :: "v"(src), "i"(N)); }
template <int N> DI void ascale_(float f) { float t; asm volatile("v_accvgpr_read_b32 %0, a[%c1]\n\tv_mul_f32 %0, %2, %0\n\tv_accvgpr_write_b32 a[%c1], %0" : "=&v"(t) : "i"(N), "v"(f)); }
DI float subip_(float x, float d) { asm volatile("v_sub_f32 %0, %0, %1" : "+v"(x) : "v"(d)); return x; }
DI void mfma_ip_(f32x4& x, const bf16x8& a, const bf16x8& b) { asm volatile("v_mfma_f32_16x16x32_bf16 %0, %1, %2, %0" : "+v"(x) : "v"(a), "v"(b) : ACLOB); }
template <class T> DI T pin_(T x) { asm volatile("" : "+v"(x)); return x; }
template <class T> DI void pin3n_(T& a, T& b, T& c) { asm volatile("s_nop 1" : "+v"(a), "+v"(b), "+v"(c)); }
#define MFMA_O(d, q, a, b) mfma_o_<AO(d, q)>(a, b)
#define MFMA_O0(d, q, a, b) mfma_o0_<AO(d, q)>(a, b)
#define MFMA_S(acc, a, s, q) mfma_s_<AQ(s, q)>(acc, a)
#define MFMA_SC(acc, a, s, q, c) mfma_sc_<AQ(s, q)>(acc, a, c)
#define ARD(dst, n) dst = ard_<n>()
#define AWR(n, src) awr_<n>(src)
#define ASCALE(n, f) ascale_<n>(f)
#define SUBIP(x, d) x = subip_(x, d)
#define NOPS(n) asm volatile("s_nop " #n ::: "memory")
#define SBAR() __builtin_amdgcn_sched_barrier(0)
#define PINF(x) x = pin_(x)
#define PINU(x) x = pin_(x)
#define WAITV_BAR(N) asm volatile("s_waitcnt vmcnt(" #N ") lgkmcnt(0)\n\ts_barrier" ::: "memory")

DI unsigned cvtpk(float lo, float hi) {
  typedef float f32x2 __attribute__((ext_vector_type(2)));
  typedef __bf16 bf16x2 __attribute__((ext_vector_type(2)));
  f32x2 v = {lo, hi}; bf16x2 b = __builtin_convertvector(v, bf16x2); return __builtin_bit_cast(unsigned, b);
}
DI void glds16(unsigned voff, const char* sbase, unsigned ldsaddr) {
  asm volatile("s_mov_b32 m0, %2\n\ts_nop 0\n\tglobal_load_lds_dwordx4 %0, %1" :: "v"(voff), "s"(sbase), "s"(ldsaddr) : "memory");
}
DI void glds16nt(unsigned voff, const char* sbase, unsigned ldsaddr) {
  asm volatile("s_mov_b32 m0, %2\n\ts_nop 0\n\tglobal_load_lds_dwordx4 %0, %1 nt" :: "v"(voff), "s"(sbase), "s"(ldsaddr) : "memory");
}
DI void setm0(unsigned ldsaddr) { asm volatile("s_mov_b32 m0, %0\n\ts_nop 0" :: "s"(ldsaddr) : "memory"); }
template <int OFF, int NT> DI void gldso(unsigned voff, const char* sbase) {
  if constexpr (NT) asm volatile("global_load_lds_dwordx4 %0, %1 offset:%2 nt" :: "v"(voff), "s"(sbase), "i"(OFF) : "memory");
  else asm volatile("global_load_lds_dwordx4 %0, %1 offset:%2" :: "v"(voff), "s"(sbase), "i"(OFF) : "memory");
}
DI bf16x8 ldsb128(unsigned p) { return *(const __attribute__((address_space(3))) bf16x8*)p; }
DI s16x4 ldstr(unsigned p) { return __builtin_bit_cast(s16x4, __builtin_amdgcn_ds_read_tr16_b64_v4i16((__attribute__((address_space(3))) v4i16_t*)p)); }
template <int X> DI unsigned adr(unsigned base, unsigned off) {
  unsigned r;
  if constexpr (X == 0) asm volatile("v_add_u32 %0, %2, %1" : "=v"(r) : "v"(base), "s"(off));
  else asm volatile("v_xor_b32 %0, %3, %1\n\tv_add_u32 %0, %2, %0" : "=&v"(r) : "v"(base), "s"(off), "i"(X));
  return r;
}
DI unsigned opq(unsigned x) { asm volatile("" : "+v"(x)); return x; }
DI bf16x8 cat8(s16x4 lo, s16x4 hi) { return __builtin_shufflevector(lo, hi, 0, 1, 2, 3, 4, 5, 6, 7); }
DI float mx3(float a, float b, float c) { float r; asm("v_max3_f32 %0, %1, %2, %3" : "=v"(r) : "v"(a), "v"(b), "v"(c)); return r; }
DI float mx3c(float a, float b, float c) { return __builtin_fmaxf(__builtin_fmaxf(a, b), c); }
DI float mx2(float a, float b) { float r; asm("v_max_f32_e32 %0, %1, %2" : "=v"(r) : "v"(a), "v"(b)); return r; }
DI float xmax(float v) {
  auto a = __builtin_amdgcn_permlane32_swap(__float_as_uint(v), __float_as_uint(v), false, false);
  v = mx2(__uint_as_float(a[0]), __uint_as_float(a[1]));
  auto b = __builtin_amdgcn_permlane16_swap(__float_as_uint(v), __float_as_uint(v), false, false);
  return mx2(__uint_as_float(b[0]), __uint_as_float(b[1]));
}
DI float xsum(float v) {
  auto a = __builtin_amdgcn_permlane32_swap(__float_as_uint(v), __float_as_uint(v), false, false);
  v = __uint_as_float(a[0]) + __uint_as_float(a[1]);
  auto b = __builtin_amdgcn_permlane16_swap(__float_as_uint(v), __float_as_uint(v), false, false);
  return __uint_as_float(b[0]) + __uint_as_float(b[1]);
}

template <int N> struct IC { static constexpr int value = N; };
#define AI __attribute__((always_inline))
template <class F, int... Is> DI void sfor_(F&& f, std::integer_sequence<int, Is...>) { (f(IC<Is>{}), ...); }
template <int N, class F> DI void sfor(F&& f) { sfor_(f, std::make_integer_sequence<int, N>{}); }
struct Ptrs { const char* x[2]; const char* k[2]; const char* v[2]; const char* wq[2]; const char* wo[2]; };

__global__ __launch_bounds__(256, 1) void k_attn(Ptrs P, char* __restrict__ ofr) {
  extern __shared__ __attribute__((aligned(16))) char smem[];
  lds_ptr lds = (lds_ptr)smem;
  const int tid = threadIdx.x, l = tid & 63, w = __builtin_amdgcn_readfirstlane(tid >> 6);
  const int i16 = l & 15, g = l >> 4, qq = i16 >> 2, pp = i16 & 3;
  const int xcd = blockIdx.x & 7, idx = blockIdx.x >> 3;
  const int br = xcd >> 2, h = 8 * (xcd & 3) + (idx & 7), b0 = idx >> 3;
  const char* xg = P.x[br] + (size_t)b0 * SQ * DM * 2;
  const char* wg = P.wq[br] + (size_t)h * DH * 2;
  const char* kg = P.k[br] + (size_t)(b0 * NH + h) * SK * DH * 2;
  const char* vg = P.v[br] + (size_t)(b0 * NH + h) * SK * DH * 2;
  char* og = ofr + (((size_t)(br * NB + b0) * 192 + h * 6) * 12 + 3 * w) * 1024 + l * 16;

  if (blockIdx.x == 0 && tid < 64) ((unsigned*)(ofr + ((size_t)48 << 20)))[tid * 16] = 0u;
  unsigned offK[3], offV[3], offW[3], offX[6];
#pragma unroll
  for (int u = 0; u < 3; ++u) {
    const int o = 1024 * u + 16 * l, r = o / 384, chp = (o % 384) >> 4, row = 8 * w + r;
    const int aK = (row >> 1) & 7;
    offK[u] = row * 384 + (((chp & ~7) | ((chp ^ aK) & 7)) << 4) + 128 - 1024 * u;
    offV[u] = row * 384 + ((chp ^ (((row >> 1) & 3) << 1)) << 4) + 128 - 1024 * u;
    const int sW = ((row >> 1) & 1) | (((row >> 3) & 1) << 1);
    offW[u] = row * (HD * 2) + ((chp ^ (sW << 1)) << 4) + 128 - 1024 * u;
  }
#pragma unroll
  for (int n = 0; n < 6; ++n) {
    const int row = 48 * w + 8 * n + (l >> 3), a = (row >> 1) & 7;
    offX[n] = row * (DM * 2) + (((l & 7) ^ a) << 4) - 1024 * (n % 3);
  }
  xg -= 0; wg -= 128; kg -= 128; vg -= 128;

  const unsigned oK0 = offK[0], oK1 = offK[1], oK2 = offK[2], oV0 = offV[0], oV1 = offV[1], oV2 = offV[2], oW0 = offW[0], oW1 = offW[1], oW2 = offW[2];
  const unsigned oX0 = offX[0], oX1 = offX[1], oX2 = offX[2], oX3 = offX[3], oX4 = offX[4], oX5 = offX[5];
  const unsigned lds0 = (unsigned)(uintptr_t)lds;
  auto px = [&](int c, int slot, auto N) AI {
    constexpr int n = N.value, u = n % 3, hf = n / 3;
    if constexpr (u == 0) setm0(lds0 + slot * SLOT + w * 6144 + hf * 3072);
    gldso<1024 * u, 0>(n == 0 ? oX0 : n == 1 ? oX1 : n == 2 ? oX2 : n == 3 ? oX3 : n == 4 ? oX4 : oX5, xg + c * 128);
  };
  auto pw_ = [&](int c, int slot, auto N) AI {
    constexpr int n = N.value, u = n % 3, hf = n / 3;
    if constexpr (u == 0) setm0(lds0 + slot * SLOT + w * 3072 + hf * 12288);
    gldso<1024 * u, 0>(u == 0 ? oW0 : u == 1 ? oW1 : oW2, wg + (size_t)(c * 64 + hf * 32) * HD * 2);
  };
  auto pk = [&](int j, int slot, auto N) AI {
    constexpr int n = N.value, u = n % 3, hf = n / 3;
    if constexpr (u == 0) setm0(lds0 + slot * SLOT + w * 3072 + hf * 12288);
    gldso<1024 * u, 1>(u == 0 ? oK0 : u == 1 ? oK1 : oK2, kg + (size_t)j * SLOT + hf * 12288);
  };
  auto pv = [&](int j, int slot, auto N) AI {
    constexpr int n = N.value, u = n % 3, hf = n / 3;
    if constexpr (u == 0) setm0(lds0 + slot * SLOT + w * 3072 + hf * 12288);
    gldso<1024 * u, 1>(u == 0 ? oV0 : u == 1 ? oV1 : oV2, vg + (size_t)j * SLOT + hf * 12288);
  };
  auto pf_ = [&](auto F, auto N) AI {
    constexpr int f = F.value;
    if constexpr (f >= 36) { if constexpr ((f & 1) == 0) px((f - 36) >> 1, f % NSLOT, N); else pw_((f - 36) >> 1, f % NSLOT, N); }
    else if constexpr ((f & 1) == 0) pk((f - 20) >> 1, f % NSLOT, N); else pv((f - 20) >> 1, f % NSLOT, N);
  };

  const int a8 = i16 >> 1;
  const unsigned kb0 = lds0 + i16 * 384 + ((g ^ a8) << 4);
  const unsigned xb0 = lds0 + (48 * w + i16) * 128 + ((g ^ a8) << 4);
  const int swz = 2 * (g & 1) + (qq >> 1);
  const unsigned vb0 = lds0 + (4 * g + qq) * 384 + (swz << 5) + 8 * pp;
  const unsigned wb0 = lds0 + (8 * g + qq) * 384 + (swz << 5) + 8 * pp;

  asm volatile("" ::: ACLOB);
  f32x4 qa[12][3];
  float inv = 0.f; f32x4 eX, eY;
  auto epi = [&](auto U, auto Pt) AI {
    constexpr int u = U.value, q = u / 6, t = u % 6, part = Pt.value;
    if constexpr (part == 0) {
      if constexpr (t == 0) { float lq; ARD(lq, AO(12, q)); inv = __builtin_amdgcn_rcpf(lq); }
      sfor<4>([&](auto R) AI { ARD(eX[R.value], AO(2 * t, q) + R.value); });
    } else if constexpr (part == 1) {
      sfor<4>([&](auto R) AI { ARD(eY[R.value], AO(2 * t + 1, q) + R.value); });
    } else if constexpr (part == 2) { eX *= inv; eY *= inv; PINF(eX); PINF(eY); }
    else if constexpr (part == 3) {
      u32x4 f = {cvtpk(eX[0], eX[1]), cvtpk(eX[2], eX[3]), cvtpk(eY[0], eY[1]), cvtpk(eY[2], eY[3])};
      *(u32x4*)(og + q * 1024 + t * 12288) = f;
    }
  };
  int sl = 0;
  unsigned ax[2], aw[4], ak[2];
  bf16x8 xf[2][3], wf[4], kf[3];
  auto rdw = [&](auto F) AI {
    constexpr int f = F.value, s = f / 12, d = f % 12;
    const unsigned p = aw[d & 3] + s * 32 * 384 + (d >> 2) * 128;
    wf[f & 3] = cat8(ldstr(p), ldstr(p + 4 * 384));
  };
  auto rdk = [&](auto F) AI { constexpr int f = F.value, s = f >> 2, kb = f & 3; kf[f % 3] = ldsb128(ak[s & 1] + kb * 6144 + (s >> 1) * 128); };
  auto p1head = [&]() AI {
    ax[0] = adr<0>(xb0, sl * SLOT); ax[1] = adr<64>(xb0, sl * SLOT);
    aw[0] = adr<0>(wb0, (sl + 1) * SLOT); aw[1] = adr<32>(wb0, (sl + 1) * SLOT); aw[2] = adr<64>(wb0, (sl + 1) * SLOT); aw[3] = adr<96>(wb0, (sl + 1) * SLOT);
#pragma unroll
    for (int q = 0; q < 3; ++q) xf[0][q] = ldsb128(ax[0] + q * 2048);
    rdw(IC<0>{}); rdw(IC<1>{});
  };
  auto khead = [&](auto Sc) AI { ak[0] = adr<0>(kb0, Sc.value * SLOT); ak[1] = adr<64>(kb0, Sc.value * SLOT); rdk(IC<0>{}); rdk(IC<1>{}); };
  auto p1step = [&](int c, auto Tc, auto Ec, auto Nc) AI {
    constexpr int TAIL = Tc.value, EPI = Ec.value, NEXT = Nc.value;
    const int sn = sl >= 2 ? sl - 2 : sl + 4;
    SBAR();
    sfor<24>([&](auto F) AI {
      constexpr int f = F.value, s = f / 12, d = f % 12;
      if constexpr (f == 22) {
        sl = sl == 4 ? 0 : sl + 2;
        if constexpr (NEXT == 0) { WAITV_BAR(12); p1head(); } else { WAITV_BAR(18); khead(IC<2>{}); }
        SBAR();
      }
      if constexpr (f + 2 < 24) rdw(IC<f + 2>{});
      if constexpr (f >= 2 && f < 5) xf[1][f - 2] = ldsb128(ax[1] + (f - 2) * 2048);
      mfma_ip_(qa[d][0], wf[f & 3], xf[s][0]);
      if constexpr (EPI >= 0 && EPI < 9) { constexpr int k = f / 12, part = (f % 12) / 2 - 1;
        if constexpr ((f & 1) == 1 && part >= 0 && part < 4) { epi(IC<2 * EPI + k>{}, IC<part>{}); SBAR(); } }
      mfma_ip_(qa[d][1], wf[f & 3], xf[s][1]);
      if constexpr (((f & 1) == 0 && f <= 20) || f == 21) {
        constexpr int n = f == 21 ? 11 : f >> 1;
        if constexpr (n < 6) { if constexpr (!TAIL) px(c + 2, sn, IC<n>{}); else pk(c - 8, sn, IC<n>{}); }
        else { if constexpr (!TAIL) pw_(c + 2, sn + 1, IC<n - 6>{}); else pv(c - 8, sn + 1, IC<n - 6>{}); }
      }
      mfma_ip_(qa[d][2], wf[f & 3], xf[s][2]);
      SBAR();
    });
  };
  auto zero_qa = [&]() AI {
    float z = 0.f; PINF(z);
#pragma unroll
    for (int d = 0; d < 12; ++d) {
#pragma unroll
      for (int q = 0; q < 3; ++q) qa[d][q] = f32x4{z, z, z, z};
      pin3n_(qa[d][0], qa[d][1], qa[d][2]);
    }
  };

  f32x4 sc[2][4][3];
  f32x4 negm[3];
  unsigned pw[2][3][4];
  bf16x8 ones = {0x3F80, 0x3F80, 0x3F80, 0x3F80, 0x3F80, 0x3F80, 0x3F80, 0x3F80};
  asm volatile("" : "+v"(ones));

#define EXP1(X, kb, q, r) do { sc[X][kb][q][r] = __builtin_amdgcn_exp2f(sc[X][kb][q][r]); PINF(sc[X][kb][q][r]); } while (0)
#define CVT1(X, c) do { constexpr int kb_ = (c) / 6, q_ = ((c) % 6) >> 1, hf_ = (c) & 1; \
    pw[kb_ >> 1][q_][2 * (kb_ & 1) + hf_] = cvtpk(sc[X][kb_][q_][2 * hf_], sc[X][kb_][q_][2 * hf_ + 1]); PINU(pw[kb_ >> 1][q_][2 * (kb_ & 1) + hf_]); } while (0)

  auto conv = [&](auto S, auto Q) AI {
    constexpr int s = S.value, q = Q.value;
    const f32x4 X = qa[2 * s][q] * QSCALE, Y = qa[2 * s + 1][q] * QSCALE;
    unsigned x0 = cvtpk(X[0], X[1]), x1 = cvtpk(X[2], X[3]), y0 = cvtpk(Y[0], Y[1]), y1 = cvtpk(Y[2], Y[3]);
    auto r0 = __builtin_amdgcn_permlane32_swap(x0, y0, false, false);
    auto r1 = __builtin_amdgcn_permlane32_swap(x1, y1, false, false);
    auto t0 = __builtin_amdgcn_permlane16_swap(r0[0], r0[1], false, false);
    auto t1 = __builtin_amdgcn_permlane16_swap(r1[0], r1[1], false, false);
    AWR(AQ(s, q) + 0, t0[0]); AWR(AQ(s, q) + 1, t1[0]); AWR(AQ(s, q) + 2, t0[1]); AWR(AQ(s, q) + 3, t1[1]);
  };

  sfor<6>([&](auto N) AI { px(0, 0, N); }); sfor<6>([&](auto N) AI { pw_(0, 1, N); });
  sfor<6>([&](auto N) AI { px(1, 2, N); }); sfor<6>([&](auto N) AI { pw_(1, 3, N); });
  zero_qa();
  WAITV_BAR(12); p1head();
#pragma unroll 1
  for (int c = 0; c < 8; ++c) p1step(c, IC<0>{}, IC<-1>{}, IC<0>{});
  p1step(8, IC<1>{}, IC<-1>{}, IC<0>{}); p1step(9, IC<1>{}, IC<-1>{}, IC<1>{});

#pragma unroll 1
  for (int it = 0;; ++it) {
    asm volatile("" : "+s"(wg));
    if (it == 0) xg += (size_t)4 * SQ * DM * 2;
    {
      NOPS(7);
      SBAR();
      conv(IC<0>{}, IC<0>{}); conv(IC<0>{}, IC<1>{}); conv(IC<0>{}, IC<2>{});
      conv(IC<1>{}, IC<0>{}); conv(IC<1>{}, IC<1>{}); conv(IC<1>{}, IC<2>{});
      NOPS(1);
      SBAR();
      sfor<24>([&](auto F) AI {
        constexpr int f = F.value, s = f >> 2, kb = f & 3;
        if constexpr (f + 2 < 24) rdk(IC<f + 2>{});
        sfor<3>([&](auto Q) AI {
          constexpr int q = Q.value;
          if constexpr (s == 0) { mfma_s0_<AQ(s, q)>(sc[0][kb][q], kf[f % 3]); } else { MFMA_S(sc[0][kb][q], kf[f % 3], s, q); }
        });
        if constexpr (s < 4 && kb < 3) conv(IC<s + 2>{}, IC<kb>{});
        if constexpr (kb == 3) { pf_(IC<24 + s / 3>{}, IC<2 * (s % 3)>{}); pf_(IC<24 + s / 3>{}, IC<2 * (s % 3) + 1>{}); }
        SBAR();
      });
      WAITV_BAR(18); khead(IC<22 % NSLOT>{});
      asm volatile("s_nop 7" : "+v"(sc[0][0][0]), "+v"(sc[0][0][1]), "+v"(sc[0][0][2]), "+v"(sc[0][1][0]), "+v"(sc[0][1][1]), "+v"(sc[0][1][2]),
                               "+v"(sc[0][2][0]), "+v"(sc[0][2][1]), "+v"(sc[0][2][2]), "+v"(sc[0][3][0]), "+v"(sc[0][3][1]), "+v"(sc[0][3][2]));
#pragma unroll
      for (int q = 0; q < 3; ++q) {
        float m0 = mx3(sc[0][0][q][0], sc[0][0][q][1], sc[0][0][q][2]);
        m0 = mx2(m0, sc[0][0][q][3]);
#pragma unroll
        for (int kb = 1; kb < 4; ++kb) { m0 = mx3(m0, sc[0][kb][q][0], sc[0][kb][q][1]); m0 = mx3(m0, sc[0][kb][q][2], sc[0][kb][q][3]); }
        m0 = xmax(m0);
        negm[q] = f32x4{-m0, -m0, -m0, -m0};
#pragma unroll
        for (int kb = 0; kb < 4; ++kb) sc[0][kb][q] -= m0;
#pragma unroll
        for (int r = 0; r < 4; ++r) sc[0][0][q][r] = __builtin_amdgcn_exp2f(sc[0][0][q][r]);
      }
    }
    sfor<8>([&](auto Jc) AI {
      constexpr int j = Jc.value;
      constexpr int X = j & 1, Y = X ^ 1;
      if constexpr (j < 7) {
        pin3n_(negm[0], negm[1], negm[2]);
        SBAR();
        sfor<24>([&](auto F) AI {
          constexpr int f = F.value, s = f >> 2, kb = f & 3;
          if constexpr (f + 2 < 24) rdk(IC<f + 2>{});
          sfor<3>([&](auto Q) AI {
            constexpr int q = Q.value;
            if constexpr (s == 0) { MFMA_SC(sc[Y][kb][q], kf[f % 3], s, q, negm[q]); } else { MFMA_S(sc[Y][kb][q], kf[f % 3], s, q); }
            if constexpr (q == 0) { CVT1(X, f); }
            else {
              constexpr int e = 2 * f + q - 1;
              if constexpr (e < 36) { EXP1(X, 1 + e / 12, (e % 12) >> 2, e & 3); }
              else if constexpr (q == 1 && j >= 1) pf_(IC<25 + 2 * j>{}, IC<f - 18>{});
            }
            SBAR();
          });
        });
      } else {
        sfor<36>([&](auto E) AI { constexpr int e = E.value; EXP1(X, 1 + e / 12, (e % 12) >> 2, e & 3);
          if constexpr ((e % 6) == 5) pf_(IC<39>{}, IC<e / 6>{}); });
        sfor<24>([&](auto C) AI { CVT1(X, C.value); });
      }
      {
        bf16x8 pf[2][3];
#pragma unroll
        for (int t = 0; t < 2; ++t)
#pragma unroll
          for (int q = 0; q < 3; ++q) { u32x4 f = {pw[t][q][0], pw[t][q][1], pw[t][q][2], pw[t][q][3]}; pf[t][q] = __builtin_bit_cast(bf16x8, f); }
        pin3n_(pf[0][0], pf[0][1], pf[0][2]); pin3n_(pf[1][0], pf[1][1], pf[1][2]);
        unsigned av[4];
        { constexpr unsigned so = ((21 + 2 * j) % NSLOT) * SLOT;
          av[0] = adr<0>(vb0, so); av[1] = adr<32>(vb0, so); av[2] = adr<64>(vb0, so); av[3] = adr<96>(vb0, so); }
        bf16x8 vf[3];
        auto rdv = [&](auto F) AI {
          constexpr int f = F.value, t = f / 12, d = f % 12;
          const unsigned p = av[d & 3] + t * 32 * 384 + (d >> 2) * 128;
          vf[f % 3] = cat8(ldstr(p), ldstr(p + 16 * 384));
        };
        float mx[3], fct[3];
        bool pend = false;
        auto mop = [&](auto I) AI {
          constexpr int q = I.value % 3, k = I.value / 3;
          if constexpr (k == 0) mx[q] = mx2(sc[Y][0][q][0], sc[Y][0][q][1]);
          else { constexpr int e = 2 * k; mx[q] = mx3c(mx[q], sc[Y][e >> 2][q][e & 3], sc[Y][e >> 2][q][(e & 3) + 1]); }
          PINF(mx[q]);
        };
        rdv(IC<0>{}); rdv(IC<1>{});
        SBAR();
        sfor<78>([&](auto Mc) AI {
          constexpr int m = Mc.value, G = m / 3, q = m % 3, t = G / 13, gi = G % 13;
          if constexpr (m == 72) {
            if constexpr (j < 6) { WAITV_BAR(12); khead(IC<(24 + 2 * j) % NSLOT>{}); }
            else if constexpr (j == 6) { WAITV_BAR(18); }
            else { WAITV_BAR(12); sl = 0; p1head(); }
            SBAR();
          }
          if constexpr (gi == 0) {
            if constexpr (j == 0 && t == 0) { MFMA_O0(12, q, ones, pf[t][q]); } else { MFMA_O(12, q, ones, pf[t][q]); }
          } else {
            constexpr int d = gi - 1, f = 12 * t + d;
            if constexpr (q == 0 && f + 2 < 24) rdv(IC<f + 2>{});
            if constexpr (j == 0 && t == 0) { MFMA_O0(d, q, vf[f % 3], pf[t][q]); } else { MFMA_O(d, q, vf[f % 3], pf[t][q]); }
          }
          if constexpr (j < 7 && q < 2) {
            if constexpr (G >= 2 && G < 8) { constexpr int i = 4 * (G - 2) + 2 * q; mop(IC<i>{}); mop(IC<i + 1>{}); }
            if constexpr (G == 8 && q == 0) {
              pend = __any(mx3c(mx[0], mx[1], mx[2]) > THR);
              if (__builtin_expect(pend, 0)) {
#pragma unroll
                for (int qq_ = 0; qq_ < 3; ++qq_) {
                  const float gr = xmax(mx[qq_]);
                  const unsigned db = (__float_as_uint(fmaxf(gr, 0.f)) + 0xFFFFu) & 0xFFFF0000u;
                  fct[qq_] = __builtin_amdgcn_exp2f(-__uint_as_float(db));
                  u32x4 bw = {g == 0 ? ((db >> 16) | 0x8000u) : 0u, 0u, 0u, 0u};
                  bf16x8 bfr = __builtin_bit_cast(bf16x8, bw);
                  asm volatile("s_nop 1" : "+v"(bfr));
                  mfma_ip_(negm[qq_], ones, bfr);
                  mfma_ip_(sc[Y][0][qq_], ones, bfr); mfma_ip_(sc[Y][1][qq_], ones, bfr); mfma_ip_(sc[Y][2][qq_], ones, bfr); mfma_ip_(sc[Y][3][qq_], ones, bfr);
                }
                NOPS(7);
              }
            }
            if constexpr (G >= 9 && G < 15) { constexpr int e = 2 * (G - 9) + q; EXP1(Y, 0, e >> 2, e & 3); }
          }
          if constexpr (j < 7 && q == 1 && G >= 16 && G < 22) pf_(IC<26 + 2 * j>{}, IC<G - 16>{});
          SBAR();
        });
        if constexpr (j < 7) {
          if (__builtin_expect(pend, 0)) {
            NOPS(7);
            sfor<156>([&](auto N) AI { ASCALE(N.value, fct[(N.value >> 2) % 3]); });
            NOPS(1);
          }
        }
      }
    });
    if (it == 1) break;
    kg += (size_t)4 * NH * SK * DH * 2; vg += (size_t)4 * NH * SK * DH * 2;
    zero_qa();
    sfor<10>([&](auto C) AI { p1step(C.value, IC<(C.value >= 8)>{}, IC<C.value>{}, IC<(C.value == 9)>{}); });
    og += (size_t)4 * 192 * 12 * 1024;
  }
  NOPS(7);
  sfor<18>([&](auto U) AI { epi(U, IC<0>{}); epi(U, IC<1>{}); epi(U, IC<2>{}); epi(U, IC<3>{}); });
  asm volatile("s_waitcnt vmcnt(0)" ::: "memory");
}

constexpr int ST2 = 128 * 320;
constexpr int LDS2 = 3 * ST2;
constexpr size_t WS_SLAB = (size_t)64 << 20;
constexpr size_t WS_CNT = (size_t)48 << 20;

DI void gload16(bf16x8& d, unsigned voff, const char* sbase) { asm volatile("global_load_dwordx4 %0, %1, %2" : "=v"(d) : "v"(voff), "s"(sbase) : "memory"); }
template <int N> DI void wait3(bf16x8& a, bf16x8& b, bf16x8& c) { asm volatile("s_waitcnt vmcnt(%3)" : "+v"(a), "+v"(b), "+v"(c) : "i"(N) : "memory"); }

__global__ __launch_bounds__(512, 2) void k_oproj(Ptrs P, char* __restrict__ ws, char* __restrict__ out) {
  extern __shared__ __attribute__((aligned(16))) char smem[];
  lds_ptr lds = (lds_ptr)smem;
  const int tid = threadIdx.x, l = tid & 63, w = __builtin_amdgcn_readfirstlane(tid >> 6);
  const int i16 = l & 15, g = l >> 4, qq = i16 >> 2, pp = i16 & 3;
  const int xcd = blockIdx.x & 7, idx = blockIdx.x >> 3;
  const int br = xcd >> 2, ks = xcd & 3, b = idx >> 2, nq = idx & 3;
  const int kh = w >> 2, qg = w & 3;
  const unsigned lds0 = (unsigned)(uintptr_t)lds;
  const char* wsrc = P.wo[br] + (size_t)ks * 1536 * DM * 2 + nq * 320;
  const char* osrc = ws + (((size_t)(br * NB + b) * 192 + 48 * ks + kh) * 12 + 3 * qg) * 1024;
  const unsigned l16 = l * 16;
  unsigned offW[5];
#pragma unroll
  for (int u = 0; u < 5; ++u) {
    const int o = 1024 * u + 16 * l, r = o / 320, chp = (o % 320) >> 4;
    offW[u] = (16 * w + r) * (DM * 2) + ((chp ^ (((r >> 2) & 1) << 1)) << 4);
  }
  const unsigned o0 = offW[0], o1 = offW[1], o2 = offW[2], o3 = offW[3], o4 = offW[4];
  const int s0 = 0;
  auto wpiece = [&](const char* src, unsigned d, auto Uc) AI {
    constexpr int u = Uc.value;
    glds16(u == 0 ? o0 : u == 1 ? o1 : u == 2 ? o2 : u == 3 ? o3 : o4, src, d + u * 1024);
  };
  auto wsrc_of = [&](int s) AI { s += s0; s = s >= 12 ? s - 12 : s; return wsrc + (size_t)s * 128 * DM * 2; };
  auto osrc_of = [&](int u) AI { u += 2 * s0; u = u >= 24 ? u - 24 : u; return osrc + (size_t)u * 24576; };
  auto issueW = [&](int s, int stg) AI {
    const unsigned d = lds0 + stg * ST2 + w * 5120;
    const char* src = wsrc_of(s);
    sfor<5>([&](auto U) AI { wpiece(src, d, U); });
  };
  bf16x8 of[6][3];
  auto loadO = [&](int u, auto Bc) AI {
    constexpr int B_ = Bc.value;
    const char* src = osrc_of(u);
    gload16(of[B_][0], l16, src); gload16(of[B_][1], l16, src + 1024); gload16(of[B_][2], l16, src + 2048);
  };
  f32x4 acc[10][3];
#pragma unroll
  for (int n = 0; n < 10; ++n)
#pragma unroll
    for (int q = 0; q < 3; ++q) acc[n][q] = f32x4{0.f, 0.f, 0.f, 0.f};
  const unsigned wb0 = lds0 + (32 * kh + 4 * g + qq) * 320 + 8 * pp + ((g & 1) << 5);
  {
    unsigned tch;
    const char* wo = P.wo[br] + (size_t)ks * 1536 * DM * 2 + (size_t)(idx * 8 + w) * 60 * 128;
    asm volatile("global_load_dword %0, %1, %2" : "=v"(tch) : "v"((l < 60 ? l : 59) * 128), "s"(wo) : "memory");
    issueW(0, 0); loadO(0, IC<0>{}); loadO(1, IC<1>{}); loadO(2, IC<2>{}); loadO(3, IC<3>{}); loadO(4, IC<4>{});
    issueW(1, 1);
    asm volatile("s_waitcnt vmcnt(25)" : "+v"(tch) :: "memory");
  }
  int stg = 0;
  auto kstep = [&](unsigned ae, unsigned ao, auto Vc, auto Bc, auto Lc, const char* lsrc, const char* dsrc, unsigned dd, auto Dc) AI {
    constexpr int v = Vc.value, B_ = Bc.value, LB = Lc.value, DN = Dc.value;
    bf16x8 wf[3];
    auto rdw = [&](auto N) AI { constexpr int n = N.value; const unsigned p = ((n & 1) ? ao : ae) + v * 64 * 320 + (n >> 1) * 64; wf[n % 3] = cat8(ldstr(p), ldstr(p + 16 * 320)); };
    rdw(IC<0>{}); rdw(IC<1>{});
    SBAR();
    sfor<10>([&](auto N) AI {
      constexpr int n = N.value;
      if constexpr (n + 2 < 10) rdw(IC<n + 2>{});
      acc[n][0] = MFMA16(wf[n % 3], of[B_][0], acc[n][0]);
      if constexpr (LB >= 0 && n < 3) { SBAR(); gload16(of[LB][n], l16, lsrc + n * 1024); SBAR(); }
      if constexpr (DN > 0 && n >= 4 && n < 4 + 2 * DN && (n & 1) == 0) { SBAR(); wpiece(dsrc, dd, IC<(v ? 2 : 0) + (n - 4) / 2>{}); SBAR(); }
      acc[n][1] = MFMA16(wf[n % 3], of[B_][1], acc[n][1]);
      acc[n][2] = MFMA16(wf[n % 3], of[B_][2], acc[n][2]);
      SBAR();
    });
  };
  auto stage = [&](int s, auto Pc, auto Mc) AI {
    constexpr int PH = Pc.value, MODE = Mc.value, B0 = 2 * PH, B1 = 2 * PH + 1;
    if constexpr (MODE == 3) { asm volatile("s_waitcnt vmcnt(0) lgkmcnt(0)\n\ts_barrier" ::: "memory"); }
    else if constexpr (MODE == 2) { asm volatile("s_waitcnt vmcnt(8) lgkmcnt(0)\n\ts_barrier" ::: "memory"); }
    else { asm volatile("s_waitcnt vmcnt(11) lgkmcnt(0)\n\ts_barrier" ::: "memory"); }
    wait3<63>(of[B0][0], of[B0][1], of[B0][2]); wait3<63>(of[B1][0], of[B1][1], of[B1][2]);
    const unsigned ae = opq(wb0 + stg * ST2), ao = opq((wb0 ^ 32) + stg * ST2);
    const unsigned dd = lds0 + (stg == 0 ? 2 : stg - 1) * ST2 + w * 5120;
    const char* dsrc = wsrc_of(s + 2);
    kstep(ae, ao, IC<0>{}, IC<B0>{}, IC<(MODE <= 1 ? (B0 + 5) % 6 : -1)>{}, osrc_of(2 * s + 5), dsrc, dd, IC<(MODE <= 1 ? 2 : 0)>{});
    kstep(ae, ao, IC<1>{}, IC<B1>{}, IC<(MODE == 0 ? B0 : -1)>{}, osrc_of(2 * s + 6), dsrc, dd, IC<(MODE <= 1 ? 3 : 0)>{});
    stg = stg == 2 ? 0 : stg + 1;
  };
#pragma unroll 1
  for (int s = 0; s < 9; s += 3) { stage(s, IC<0>{}, IC<0>{}); stage(s + 1, IC<1>{}, IC<0>{}); stage(s + 2, IC<2>{}, IC<0>{}); }
  stage(9, IC<0>{}, IC<1>{}); stage(10, IC<1>{}, IC<2>{}); stage(11, IC<2>{}, IC<3>{});
  asm volatile("s_waitcnt vmcnt(0) lgkmcnt(0)\n\ts_barrier" ::: "memory");
  {
    const unsigned xo = lds0 + ((kh ^ 1) * 4 + qg) * 15360 + l16, xi = lds0 + (kh * 4 + qg) * 15360 + l16;
#pragma unroll
    for (int n = 0; n < 5; ++n)
#pragma unroll
      for (int q = 0; q < 3; ++q) *(__attribute__((address_space(3))) f32x4*)(xo + (n * 3 + q) * 1024) = kh ? acc[n][q] : acc[5 + n][q];
    __syncthreads();
#pragma unroll
    for (int n = 0; n < 5; ++n)
#pragma unroll
      for (int q = 0; q < 3; ++q) {
        const f32x4 o = *(const __attribute__((address_space(3))) f32x4*)(xi + (n * 3 + q) * 1024);
        acc[n][q] = (kh ? acc[5 + n][q] : acc[n][q]) + o;
      }
  }
  const int tile = (br * NB + b) * 4 + nq;
  char* slab = ws + WS_SLAB + (size_t)tile * (16 * 8 * 2048);
  {
#pragma unroll
    for (int r = 0; r < 4; ++r) {
      if (r != ks) {
        char* dst = slab + (size_t)((r * 4 + ks) * 8 + w) * 2048 + l16;
#pragma unroll
        for (int m = 0; m < 2; ++m) {
          const int i0_ = 8 * m + r, i1_ = (8 * m + 4 + r) < 15 ? 8 * m + 4 + r : i0_;
          const f32x4 a = acc[i0_ / 3][i0_ % 3], c = acc[i1_ / 3][i1_ % 3];
          u32x4 v = {cvtpk(a[0], a[1]), cvtpk(a[2], a[3]), cvtpk(c[0], c[1]), cvtpk(c[2], c[3])};
          asm volatile("global_store_dwordx4 %0, %1, off sc0 sc1\n\ts_nop 1" :: "v"(dst + m * 1024), "v"(v) : "memory");
        }
      }
    }
    asm volatile("s_waitcnt vmcnt(0)" ::: "memory");
    __syncthreads();
    unsigned* cnt = (unsigned*)(ws + WS_CNT) + tile * 16;
    if (tid == 0) {
      __hip_atomic_fetch_add(cnt, 1u, __ATOMIC_RELAXED, __HIP_MEMORY_SCOPE_AGENT);
      unsigned spins = 0;
      while (__hip_atomic_load(cnt, __ATOMIC_RELAXED, __HIP_MEMORY_SCOPE_AGENT) < 4u && ++spins < (1u << 24)) __builtin_amdgcn_s_sleep(1);
    }
    __syncthreads();
  }
  {
    u32x4 p[3][2];
#pragma unroll
    for (int r = 0; r < 3; ++r) {
      const int src = (ks + 1 + r) & 3;
      const char* th = slab + (size_t)((ks * 4 + src) * 8 + w) * 2048 + l16;
      asm volatile("global_load_dwordx4 %0, %1, off sc0 sc1" : "=v"(p[r][0]) : "v"(th) : "memory");
      asm volatile("global_load_dwordx4 %0, %1, off sc0 sc1" : "=v"(p[r][1]) : "v"(th + 1024) : "memory");
    }
    asm volatile("s_waitcnt vmcnt(0)" : "+v"(p[0][0]), "+v"(p[0][1]), "+v"(p[1][0]), "+v"(p[1][1]), "+v"(p[2][0]), "+v"(p[2][1]) :: "memory");
#pragma unroll
    for (int i = 0; i < 15; ++i) {
      if ((i & 3) == ks) {
        const int m = i >> 3, hf = (i >> 2) & 1;
        f32x4 o = acc[i / 3][i % 3];
#pragma unroll
        for (int r = 0; r < 3; ++r) {
          const unsigned u0 = p[r][m][2 * hf], u1 = p[r][m][2 * hf + 1];
          o[0] += __uint_as_float(u0 << 16); o[1] += __uint_as_float(u0 & 0xFFFF0000u);
          o[2] += __uint_as_float(u1 << 16); o[3] += __uint_as_float(u1 & 0xFFFF0000u);
        }
        u32x2 f = {cvtpk(o[0], o[1]), cvtpk(o[2], o[3])};
        char* dst = out + ((size_t)(b * SQ + (3 * qg + i % 3) * 16 + i16) * (2 * DM) + br * DM + nq * 160 + (5 * kh + i / 3) * 16 + 4 * g) * 2;
        *(u32x2*)dst = f;
      }
    }
  }
}

extern "C" void kernel_launch(void* const* d_in, const int* in_sizes, int n_in, void* d_out, int out_size, void* d_ws, size_t ws_size,
                              hipStream_t stream) {
  Ptrs P;
  for (int r = 0; r < 2; ++r) {
    P.x[r] = (const char*)d_in[5 * r + 0];
    P.k[r] = (const char*)d_in[5 * r + 1];
    P.v[r] = (const char*)d_in[5 * r + 2];
    P.wq[r] = (const char*)d_in[5 * r + 3];
    P.wo[r] = (const char*)d_in[5 * r + 4];
  }
  (void)hipFuncSetAttribute((const void*)k_attn, hipFuncAttributeMaxDynamicSharedMemorySize, LDS1);
  (void)hipFuncSetAttribute((const void*)k_oproj, hipFuncAttributeMaxDynamicSharedMemorySize, LDS2);
  hipLaunchKernelGGL(k_attn, dim3(256), dim3(256), LDS1, stream, P, (char*)d_ws);
  hipLaunchKernelGGL(k_oproj, dim3(256), dim3(512), LDS2, stream, P, (char*)d_ws, (char*)d_out);
}
```

```cpp
#include <hip/hip_runtime.h>
#include <hip/hip_bf16.h>
#include <stdint.h>
#include <utility>

using bf16x8 = __attribute__((ext_vector_type(8))) short;
using s16x4 = __attribute__((ext_vector_type(4))) short;
using f32x4 = __attribute__((ext_vector_type(4))) float;
using u32x4 = __attribute__((ext_vector_type(4))) unsigned;
using u32x2 = __attribute__((ext_vector_type(2))) unsigned;
typedef short v4i16_t __attribute__((ext_vector_type(4)));
typedef __attribute__((address_space(3))) char* lds_ptr;
typedef __attribute__((address_space(3))) const char* lds_cptr;

constexpr int NB = 8, SQ = 192, DM = 640, NH = 32, SK = 512, DH = 192;
constexpr int HD = NH * DH;
constexpr int SLOT = 24576;
constexpr int NSLOT = 6;
constexpr int LDS1 = SLOT * NSLOT;
constexpr float QSCALE = 0.07216878364870322f * 1.4426950408889634f;
constexpr float THR = 8.0f;

#define DI __device__ __forceinline__
#define MFMA16(a, b, c) __builtin_amdgcn_mfma_f32_16x16x32_bf16((a), (b), (c), 0, 0, 0)
#define ACLOB "a0","a1","a2","a3","a4","a5","a6","a7","a8","a9","a10","a11","a12","a13","a14","a15","a16","a17","a18","a19","a20","a21","a22","a23","a24","a25","a26","a27","a28","a29","a30","a31","a32","a33","a34","a35","a36","a37","a38","a39","a40","a41","a42","a43","a44","a45","a46","a47","a48","a49","a50","a51","a52","a53","a54","a55","a56","a57","a58","a59","a60","a61","a62","a63","a64","a65","a66","a67","a68","a69","a70","a71","a72","a73","a74","a75","a76","a77","a78","a79","a80","a81","a82","a83","a84","a85","a86","a87","a88","a89","a90","a91","a92","a93","a94","a95","a96","a97","a98","a99","a100","a101","a102","a103","a104","a105","a106","a107","a108","a109","a110","a111","a112","a113","a114","a115","a116","a117","a118","a119","a120","a121","a122","a123","a124","a125","a126","a127","a128","a129","a130","a131","a132","a133","a134","a135","a136","a137","a138","a139","a140","a141","a142","a143","a144","a145","a146","a147","a148","a149","a150","a151","a152","a153","a154","a155","a156","a157","a158","a159","a160","a161","a162","a163","a164","a165","a166","a167","a168","a169","a170","a171","a172","a173","a174","a175","a176","a177","a178","a179","a180","a181","a182","a183","a184","a185","a186","a187","a188","a189","a190","a191","a192","a193","a194","a195","a196","a197","a198","a199","a200","a201","a202","a203","a204","a205","a206","a207","a208","a209","a210","a211","a212","a213","a214","a215","a216","a217","a218","a219","a220","a221","a222","a223","a224","a225","a226","a227"
#define AO(d, q) (4 * (3 * (d) + (q)))
#define AQ(s, q) (156 + 4 * (3 * (s) + (q)))
template <int N> DI void mfma_o_(const bf16x8& a, const bf16x8& b) { asm volatile("v_mfma_f32_16x16x32_bf16 a[%c2:%c3], %0, %1, a[%c2:%c3]" :: "v"(a), "v"(b), "i"(N), "i"(N + 3) : ACLOB); }
template <int N> DI void mfma_o0_(const bf16x8& a, const bf16x8& b) { asm volatile("v_mfma_f32_16x16x32_bf16 a[%c2:%c3], %0, %1, 0" :: "v"(a), "v"(b), "i"(N), "i"(N + 3) : ACLOB); }
template <int N> DI void mfma_s_(f32x4& acc, const bf16x8& a) { asm volatile("v_mfma_f32_16x16x32_bf16 %0, %1, a[%c2:%c3], %0" : "+v"(acc) : "v"(a), "i"(N), "i"(N + 3) : ACLOB); }
template <int N> DI void mfma_sc_(f32x4& acc, const bf16x8& a, const f32x4& c) { asm volatile("v_mfma_f32_16x16x32_bf16 %0, %1, a[%c2:%c3], %4" : "=&v"(acc) : "v"(a), "i"(N), "i"(N + 3), "v"(c) : ACLOB); }
template <int N> DI void mfma_s0_(f32x4& acc, const bf16x8& a) { asm volatile("v_mfma_f32_16x16x32_bf16 %0, %1, a[%c2:%c3], 0" : "=&v"(acc) : "v"(a), "i"(N), "i"(N + 3) : ACLOB); }
template <int N> DI float ard_() { float r; asm volatile("v_accvgpr_read_b32 %0, a[%c1]" : "=v"(r) : "i"(N)); return r; }
template <int N, class T> DI void awr_(T src) { asm volatile("v_accvgpr_write_b32 a[%c1], %0" :: "v"(src), "i"(N)); }
template <int N> DI void ascale_(float f) { float t; asm volatile("v_accvgpr_read_b32 %0, a[%c1]\n\tv_mul_f32 %0, %2, %0\n\tv_accvgpr_write_b32 a[%c1], %0" : "=&v"(t) : "i"(N), "v"(f)); }
DI void mfma_ip_(f32x4& x, const bf16x8& a, const bf16x8& b) { asm volatile("v_mfma_f32_16x16x32_bf16 %0, %1, %2, %0" : "+v"(x) : "v"(a), "v"(b) : ACLOB); }
template <class T> DI T pin_(T x) { asm volatile("" : "+v"(x)); return x; }
template <class T> DI void pin3n_(T& a, T& b, T& c) { asm volatile("s_nop 1" : "+v"(a), "+v"(b), "+v"(c)); }
#define MFMA_O(d, q, a, b) mfma_o_<AO(d, q)>(a, b)
#define MFMA_O0(d, q, a, b) mfma_o0_<AO(d, q)>(a, b)
#define MFMA_S(acc, a, s, q) mfma_s_<AQ(s, q)>(acc, a)
#define MFMA_SC(acc, a, s, q, c) mfma_sc_<AQ(s, q)>(acc, a, c)
#define ARD(dst, n) dst = ard_<n>()
#define AWR(n, src) awr_<n>(src)
#define ASCALE(n, f) ascale_<n>(f)
#define NOPS(n) asm volatile("s_nop " #n ::: "memory")
#define SBAR() __builtin_amdgcn_sched_barrier(0)
#define PINF(x) x = pin_(x)
#define PINU(x) x = pin_(x)
#define WAITV_BAR(N) asm volatile("s_waitcnt vmcnt(" #N ") lgkmcnt(0)\n\ts_barrier" ::: "memory")

DI unsigned cvtpk(float lo, float hi) {
  typedef float f32x2 __attribute__((ext_vector_type(2)));
  typedef __bf16 bf16x2 __attribute__((ext_vector_type(2)));
  f32x2 v = {lo, hi}; bf16x2 b = __builtin_convertvector(v, bf16x2); return __builtin_bit_cast(unsigned, b);
}
DI void glds16(unsigned voff, const char* sbase, unsigned ldsaddr) {
  asm volatile("s_mov_b32 m0, %2\n\ts_nop 0\n\tglobal_load_lds_dwordx4 %0, %1" :: "v"(voff), "s"(sbase), "s"(ldsaddr) : "memory");
}
DI void setm0(unsigned ldsaddr) { asm volatile("s_mov_b32 m0, %0\n\ts_nop 0" :: "s"(ldsaddr) : "memory"); }
template <int OFF, int NT> DI void gldso(unsigned voff, const char* sbase) {
  if constexpr (NT) asm volatile("global_load_lds_dwordx4 %0, %1 offset:%2 nt" :: "v"(voff), "s"(sbase), "i"(OFF) : "memory");
  else asm volatile("global_load_lds_dwordx4 %0, %1 offset:%2" :: "v"(voff), "s"(sbase), "i"(OFF) : "memory");
}
DI bf16x8 ldsb128(unsigned p) { return *(const __attribute__((address_space(3))) bf16x8*)p; }
DI s16x4 ldstr(unsigned p) { return __builtin_bit_cast(s16x4, __builtin_amdgcn_ds_read_tr16_b64_v4i16((__attribute__((address_space(3))) v4i16_t*)p)); }
template <int X> DI unsigned adr(unsigned base, unsigned off) {
  unsigned r;
  if constexpr (X == 0) asm volatile("v_add_u32 %0, %2, %1" : "=v"(r) : "v"(base), "s"(off));
  else asm volatile("v_xor_b32 %0, %3, %1\n\tv_add_u32 %0, %2, %0" : "=&v"(r) : "v"(base), "s"(off), "i"(X));
  return r;
}
DI unsigned opq(unsigned x) { asm volatile("" : "+v"(x)); return x; }
DI bf16x8 cat8(s16x4 lo, s16x4 hi) { return __builtin_shufflevector(lo, hi, 0, 1, 2, 3, 4, 5, 6, 7); }
DI float mx3(float a, float b, float c) { float r; asm("v_max3_f32 %0, %1, %2, %3" : "=v"(r) : "v"(a), "v"(b), "v"(c)); return r; }
DI float mx3c(float a, float b, float c) { return __builtin_fmaxf(__builtin_fmaxf(a, b), c); }
DI float mx2(float a, float b) { float r; asm("v_max_f32_e32 %0, %1, %2" : "=v"(r) : "v"(a), "v"(b)); return r; }
DI float xmax(float v) {
  auto a = __builtin_amdgcn_permlane32_swap(__float_as_uint(v), __float_as_uint(v), false, false);
  v = mx2(__uint_as_float(a[0]), __uint_as_float(a[1]));
  auto b = __builtin_amdgcn_permlane16_swap(__float_as_uint(v), __float_as_uint(v), false, false);
  return mx2(__uint_as_float(b[0]), __uint_as_float(b[1]));
}
template <int N> struct IC { static constexpr int value = N; };
#define AI __attribute__((always_inline))
template <class F, int... Is> DI void sfor_(F&& f, std::integer_sequence<int, Is...>) { (f(IC<Is>{}), ...); }
template <int N, class F> DI void sfor(F&& f) { sfor_(f, std::make_integer_sequence<int, N>{}); }
struct Ptrs { const char* x[2]; const char* k[2]; const char* v[2]; const char* wq[2]; const char* wo[2]; };

__global__ __launch_bounds__(256, 1) void k_attn(Ptrs P, char* __restrict__ ofr) {
  extern __shared__ __attribute__((aligned(16))) char smem[];
  lds_ptr lds = (lds_ptr)smem;
  const int tid = threadIdx.x, l = tid & 63, w = __builtin_amdgcn_readfirstlane(tid >> 6);
  const int i16 = l & 15, g = l >> 4, qq = i16 >> 2, pp = i16 & 3;
  const int xcd = blockIdx.x & 7, idx = blockIdx.x >> 3;
  const int br = xcd >> 2, h = 8 * (xcd & 3) + (idx & 7), b0 = idx >> 3;
  const char* xg = P.x[br] + (size_t)b0 * SQ * DM * 2;
  const char* wg = P.wq[br] + (size_t)h * DH * 2;
  const char* kg = P.k[br] + (size_t)(b0 * NH + h) * SK * DH * 2;
  const char* vg = P.v[br] + (size_t)(b0 * NH + h) * SK * DH * 2;
  char* og = ofr + (((size_t)(br * NB + b0) * 192 + h * 6) * 12 + 3 * w) * 1024 + l * 16;

  if (blockIdx.x == 0 && tid < 64) ((unsigned*)(ofr + ((size_t)48 << 20)))[tid * 16] = 0u;
  unsigned offK[3], offV[3], offW[3], offX[6];
#pragma unroll
  for (int u = 0; u < 3; ++u) {
    const int o = 1024 * u + 16 * l, r = o / 384, chp = (o % 384) >> 4, row = 8 * w + r;
    const int aK = (row >> 1) & 7;
    offK[u] = row * 384 + (((chp & ~7) | ((chp ^ aK) & 7)) << 4) + 128 - 1024 * u;
    offV[u] = row * 384 + ((chp ^ (((row >> 1) & 3) << 1)) << 4) + 128 - 1024 * u;
    const int sW = ((row >> 1) & 1) | (((row >> 3) & 1) << 1);
    offW[u] = row * (HD * 2) + ((chp ^ (sW << 1)) << 4) + 128 - 1024 * u;
  }
#pragma unroll
  for (int n = 0; n < 6; ++n) {
    const int row = 48 * w + 8 * n + (l >> 3), a = (row >> 1) & 7;
    offX[n] = row * (DM * 2) + (((l & 7) ^ a) << 4) - 1024 * (n % 3);
  }
  wg -= 128; kg -= 128; vg -= 128;

  const unsigned oK0 = offK[0], oK1 = offK[1], oK2 = offK[2], oV0 = offV[0], oV1 = offV[1], oV2 = offV[2], oW0 = offW[0], oW1 = offW[1], oW2 = offW[2];
  const unsigned oX0 = offX[0], oX1 = offX[1], oX2 = offX[2], oX3 = offX[3], oX4 = offX[4], oX5 = offX[5];
  const unsigned lds0 = (unsigned)(uintptr_t)lds;
  auto px = [&](int c, int slot, auto N) AI {
    constexpr int n = N.value, u = n % 3, hf = n / 3;
    if constexpr (u == 0) setm0(lds0 + slot * SLOT + w * 6144 + hf * 3072);
    gldso<1024 * u, 0>(n == 0 ? oX0 : n == 1 ? oX1 : n == 2 ? oX2 : n == 3 ? oX3 : n == 4 ? oX4 : oX5, xg + c * 128);
  };
  auto pw_ = [&](int c, int slot, auto N) AI {
    constexpr int n = N.value, u = n % 3, hf = n / 3;
    if constexpr (u == 0) setm0(lds0 + slot * SLOT + w * 3072 + hf * 12288);
    gldso<1024 * u, 0>(u == 0 ? oW0 : u == 1 ? oW1 : oW2, wg + (size_t)(c * 64 + hf * 32) * HD * 2);
  };
  auto pk = [&](int j, int slot, auto N) AI {
    constexpr int n = N.value, u = n % 3, hf = n / 3;
    if constexpr (u == 0) setm0(lds0 + slot * SLOT + w * 3072 + hf * 12288);
    gldso<1024 * u, 1>(u == 0 ? oK0 : u == 1 ? oK1 : oK2, kg + (size_t)j * SLOT + hf * 12288);
  };
  auto pv = [&](int j, int slot, auto N) AI {
    constexpr int n = N.value, u = n % 3, hf = n / 3;
    if constexpr (u == 0) setm0(lds0 + slot * SLOT + w * 3072 + hf * 12288);
    gldso<1024 * u, 1>(u == 0 ? oV0 : u == 1 ? oV1 : oV2, vg + (size_t)j * SLOT + hf * 12288);
  };
  auto pf_ = [&](auto F, auto N) AI {
    constexpr int f = F.value;
    if constexpr (f >= 36) { if constexpr ((f & 1) == 0) px((f - 36) >> 1, f % NSLOT, N); else pw_((f - 36) >> 1, f % NSLOT, N); }
    else if constexpr ((f & 1) == 0) pk((f - 20) >> 1, f % NSLOT, N); else pv((f - 20) >> 1, f % NSLOT, N);
  };

  const int a8 = i16 >> 1;
  const unsigned kb0 = lds0 + i16 * 384 + ((g ^ a8) << 4);
  const unsigned xb0 = lds0 + (48 * w + i16) * 128 + ((g ^ a8) << 4);
  const int swz = 2 * (g & 1) + (qq >> 1);
  const unsigned vb0 = lds0 + (4 * g + qq) * 384 + (swz << 5) + 8 * pp;
  const unsigned wb0 = lds0 + (8 * g + qq) * 384 + (swz << 5) + 8 * pp;

  asm volatile("" ::: ACLOB);
  f32x4 qa[12][3];
  float inv = 0.f; f32x4 eX, eY;
  auto epi = [&](auto U, auto Pt) AI {
    constexpr int u = U.value, q = u / 6, t = u % 6, part = Pt.value;
    if constexpr (part == 0) {
      if constexpr (t == 0) { float lq; ARD(lq, AO(12, q)); inv = __builtin_amdgcn_rcpf(lq); }
      sfor<4>([&](auto R) AI { ARD(eX[R.value], AO(2 * t, q) + R.value); });
    } else if constexpr (part == 1) {
      sfor<4>([&](auto R) AI { ARD(eY[R.value], AO(2 * t + 1, q) + R.value); });
    } else if constexpr (part == 2) { eX *= inv; eY *= inv; PINF(eX); PINF(eY); }
    else if constexpr (part == 3) {
      u32x4 f = {cvtpk(eX[0], eX[1]), cvtpk(eX[2], eX[3]), cvtpk(eY[0], eY[1]), cvtpk(eY[2], eY[3])};
      *(u32x4*)(og + q * 1024 + t * 12288) = f;
    }
  };
  int sl = 0;
  unsigned ax[2], aw[4], ak[2];
  bf16x8 xf[2][3], wf[4], kf[3];
  auto rdw = [&](auto F) AI {
    constexpr int f = F.value, s = f / 12, d = f % 12;
    const unsigned p = aw[d & 3] + s * 32 * 384 + (d >> 2) * 128;
    wf[f & 3] = cat8(ldstr(p), ldstr(p + 4 * 384));
  };
  auto rdk = [&](auto F) AI { constexpr int f = F.value, s = f >> 2, kb = f & 3; kf[f % 3] = ldsb128(ak[s & 1] + kb * 6144 + (s >> 1) * 128); };
  auto p1head = [&]() AI {
    ax[0] = adr<0>(xb0, sl * SLOT); ax[1] = adr<64>(xb0, sl * SLOT);
    aw[0] = adr<0>(wb0, (sl + 1) * SLOT); aw[1] = adr<32>(wb0, (sl + 1) * SLOT); aw[2] = adr<64>(wb0, (sl + 1) * SLOT); aw[3] = adr<96>(wb0, (sl + 1) * SLOT);
#pragma unroll
    for (int q = 0; q < 3; ++q) xf[0][q] = ldsb128(ax[0] + q * 2048);
    rdw(IC<0>{}); rdw(IC<1>{});
  };
  auto khead = [&](auto Sc) AI { ak[0] = adr<0>(kb0, Sc.value * SLOT); ak[1] = adr<64>(kb0, Sc.value * SLOT); rdk(IC<0>{}); rdk(IC<1>{}); };
  auto p1step = [&](int c, auto Tc, auto Ec, auto Nc) AI {
    constexpr int TAIL = Tc.value, EPI = Ec.value, NEXT = Nc.value;
    const int sn = sl >= 2 ? sl - 2 : sl + 4;
    SBAR();
    sfor<24>([&](auto F) AI {
      constexpr int f = F.value, s = f / 12, d = f % 12;
      if constexpr (f == 22) {
        sl = sl == 4 ? 0 : sl + 2;
        if constexpr (NEXT == 0) { WAITV_BAR(12); p1head(); } else { WAITV_BAR(18); khead(IC<2>{}); }
        SBAR();
      }
      if constexpr (f + 2 < 24) rdw(IC<f + 2>{});
      if constexpr (f >= 2 && f < 5) xf[1][f - 2] = ldsb128(ax[1] + (f - 2) * 2048);
      mfma_ip_(qa[d][0], wf[f & 3], xf[s][0]);
      if constexpr (EPI >= 0 && EPI < 9) { constexpr int k = f / 12, part = (f % 12) / 2 - 1;
        if constexpr ((f & 1) == 1 && part >= 0 && part < 4) { epi(IC<2 * EPI + k>{}, IC<part>{}); SBAR(); } }
      mfma_ip_(qa[d][1], wf[f & 3], xf[s][1]);
      if constexpr (((f & 1) == 0 && f <= 20) || f == 21) {
        constexpr int n = f == 21 ? 11 : f >> 1;
        if constexpr (n < 6) { if constexpr (!TAIL) px(c + 2, sn, IC<n>{}); else pk(c - 8, sn, IC<n>{}); }
        else { if constexpr (!TAIL) pw_(c + 2, sn + 1, IC<n - 6>{}); else pv(c - 8, sn + 1, IC<n - 6>{}); }
      }
      mfma_ip_(qa[d][2], wf[f & 3], xf[s][2]);
      SBAR();
    });
  };
  auto zero_qa = [&]() AI {
    float z = 0.f; PINF(z);
#pragma unroll
    for (int d = 0; d < 12; ++d) {
#pragma unroll
      for (int q = 0; q < 3; ++q) qa[d][q] = f32x4{z, z, z, z};
      pin3n_(qa[d][0], qa[d][1], qa[d][2]);
    }
  };

  f32x4 sc[2][4][3];
  f32x4 negm[3];
  unsigned pw[2][3][4];
  bf16x8 ones = {0x3F80, 0x3F80, 0x3F80, 0x3F80, 0x3F80, 0x3F80, 0x3F80, 0x3F80};
  asm volatile("" : "+v"(ones));

#define EXP1(X, kb, q, r) do { sc[X][kb][q][r] = __builtin_amdgcn_exp2f(sc[X][kb][q][r]); PINF(sc[X][kb][q][r]); } while (0)
#define CVT1(X, c) do { constexpr int kb_ = (c) / 6, q_ = ((c) % 6) >> 1, hf_ = (c) & 1; \
    pw[kb_ >> 1][q_][2 * (kb_ & 1) + hf_] = cvtpk(sc[X][kb_][q_][2 * hf_], sc[X][kb_][q_][2 * hf_ + 1]); PINU(pw[kb_ >> 1][q_][2 * (kb_ & 1) + hf_]); } while (0)

  auto conv = [&](auto S, auto Q) AI {
    constexpr int s = S.value, q = Q.value;
    const f32x4 X = qa[2 * s][q] * QSCALE, Y = qa[2 * s + 1][q] * QSCALE;
    unsigned x0 = cvtpk(X[0], X[1]), x1 = cvtpk(X[2], X[3]), y0 = cvtpk(Y[0], Y[1]), y1 = cvtpk(Y[2], Y[3]);
    auto r0 = __builtin_amdgcn_permlane32_swap(x0, y0, false, false);
    auto r1 = __builtin_amdgcn_permlane32_swap(x1, y1, false, false);
    auto t0 = __builtin_amdgcn_permlane16_swap(r0[0], r0[1], false, false);
    auto t1 = __builtin_amdgcn_permlane16_swap(r1[0], r1[1], false, false);
    AWR(AQ(s, q) + 0, t0[0]); AWR(AQ(s, q) + 1, t1[0]); AWR(AQ(s, q) + 2, t0[1]); AWR(AQ(s, q) + 3, t1[1]);
  };

  sfor<6>([&](auto N) AI { px(0, 0, N); }); sfor<6>([&](auto N) AI { pw_(0, 1, N); });
  sfor<6>([&](auto N) AI { px(1, 2, N); }); sfor<6>([&](auto N) AI { pw_(1, 3, N); });
  zero_qa();
  WAITV_BAR(12); p1head();
#pragma unroll 1
  for (int c = 0; c < 8; ++c) p1step(c, IC<0>{}, IC<-1>{}, IC<0>{});
  p1step(8, IC<1>{}, IC<-1>{}, IC<0>{}); p1step(9, IC<1>{}, IC<-1>{}, IC<1>{});

#pragma unroll 1
  for (int it = 0;; ++it) {
    asm volatile("" : "+s"(wg));
    if (it == 0) xg += (size_t)4 * SQ * DM * 2;
    {
      NOPS(7);
      SBAR();
      conv(IC<0>{}, IC<0>{}); conv(IC<0>{}, IC<1>{}); conv(IC<0>{}, IC<2>{});
      conv(IC<1>{}, IC<0>{}); conv(IC<1>{}, IC<1>{}); conv(IC<1>{}, IC<2>{});
      NOPS(1);
      SBAR();
      sfor<24>([&](auto F) AI {
        constexpr int f = F.value, s = f >> 2, kb = f & 3;
        if constexpr (f + 2 < 24) rdk(IC<f + 2>{});
        sfor<3>([&](auto Q) AI {
          constexpr int q = Q.value;
          if constexpr (s == 0) { mfma_s0_<AQ(s, q)>(sc[0][kb][q], kf[f % 3]); } else { MFMA_S(sc[0][kb][q], kf[f % 3], s, q); }
        });
        if constexpr (s < 4 && kb < 3) conv(IC<s + 2>{}, IC<kb>{});
        if constexpr (kb == 3) { pf_(IC<24 + s / 3>{}, IC<2 * (s % 3)>{}); pf_(IC<24 + s / 3>{}, IC<2 * (s % 3) + 1>{}); }
        SBAR();
      });
      WAITV_BAR(18); khead(IC<22 % NSLOT>{});
      asm volatile("s_nop 7" : "+v"(sc[0][0][0]), "+v"(sc[0][0][1]), "+v"(sc[0][0][2]), "+v"(sc[0][1][0]), "+v"(sc[0][1][1]), "+v"(sc[0][1][2]),
                               "+v"(sc[0][2][0]), "+v"(sc[0][2][1]), "+v"(sc[0][2][2]), "+v"(sc[0][3][0]), "+v"(sc[0][3][1]), "+v"(sc[0][3][2]));
#pragma unroll
      for (int q = 0; q < 3; ++q) {
        float m0 = mx3(sc[0][0][q][0], sc[0][0][q][1], sc[0][0][q][2]);
        m0 = mx2(m0, sc[0][0][q][3]);
#pragma unroll
        for (int kb = 1; kb < 4; ++kb) { m0 = mx3(m0, sc[0][kb][q][0], sc[0][kb][q][1]); m0 = mx3(m0, sc[0][kb][q][2], sc[0][kb][q][3]); }
        m0 = xmax(m0);
        negm[q] = f32x4{-m0, -m0, -m0, -m0};
#pragma unroll
        for (int kb = 0; kb < 4; ++kb) sc[0][kb][q] -= m0;
#pragma unroll
        for (int r = 0; r < 4; ++r) sc[0][0][q][r] = __builtin_amdgcn_exp2f(sc[0][0][q][r]);
      }
    }
    sfor<8>([&](auto Jc) AI {
      constexpr int j = Jc.value;
      constexpr int X = j & 1, Y = X ^ 1;
      if constexpr (j < 7) {
        pin3n_(negm[0], negm[1], negm[2]);
        SBAR();
        sfor<24>([&](auto F) AI {
          constexpr int f = F.value, s = f >> 2, kb = f & 3;
          if constexpr (f + 2 < 24) rdk(IC<f + 2>{});
          sfor<3>([&](auto Q) AI {
            constexpr int q = Q.value;
            if constexpr (s == 0) { MFMA_SC(sc[Y][kb][q], kf[f % 3], s, q, negm[q]); } else { MFMA_S(sc[Y][kb][q], kf[f % 3], s, q); }
            if constexpr (q == 0) { CVT1(X, f); }
            else {
              constexpr int e = 2 * f + q - 1;
              if constexpr (e < 36) { EXP1(X, 1 + e / 12, (e % 12) >> 2, e & 3); }
              else if constexpr (q == 1 && j >= 1) pf_(IC<25 + 2 * j>{}, IC<f - 18>{});
            }
            SBAR();
          });
        });
      } else {
        sfor<36>([&](auto E) AI { constexpr int e = E.value; EXP1(X, 1 + e / 12, (e % 12) >> 2, e & 3);
          if constexpr ((e % 6) == 5) pf_(IC<39>{}, IC<e / 6>{}); });
        sfor<24>([&](auto C) AI { CVT1(X, C.value); });
      }
      {
        bf16x8 pf[2][3];
#pragma unroll
        for (int t = 0; t < 2; ++t)
#pragma unroll
          for (int q = 0; q < 3; ++q) { u32x4 f = {pw[t][q][0], pw[t][q][1], pw[t][q][2], pw[t][q][3]}; pf[t][q] = __builtin_bit_cast(bf16x8, f); }
        pin3n_(pf[0][0], pf[0][1], pf[0][2]); pin3n_(pf[1][0], pf[1][1], pf[1][2]);
        unsigned av[4];
        { constexpr unsigned so = ((21 + 2 * j) % NSLOT) * SLOT;
          av[0] = adr<0>(vb0, so); av[1] = adr<32>(vb0, so); av[2] = adr<64>(vb0, so); av[3] = adr<96>(vb0, so); }
        bf16x8 vf[3];
        auto rdv = [&](auto F) AI {
          constexpr int f = F.value, t = f / 12, d = f % 12;
          const unsigned p = av[d & 3] + t * 32 * 384 + (d >> 2) * 128;
          vf[f % 3] = cat8(ldstr(p), ldstr(p + 16 * 384));
        };
        float mx[3], fct[3];
        bool pend = false;
        auto mop = [&](auto I) AI {
          constexpr int q = I.value % 3, k = I.value / 3;
          if constexpr (k == 0) mx[q] = mx2(sc[Y][0][q][0], sc[Y][0][q][1]);
          else { constexpr int e = 2 * k; mx[q] = mx3c(mx[q], sc[Y][e >> 2][q][e & 3], sc[Y][e >> 2][q][(e & 3) + 1]); }
          PINF(mx[q]);
        };
        rdv(IC<0>{}); rdv(IC<1>{});
        SBAR();
        sfor<78>([&](auto Mc) AI {
          constexpr int m = Mc.value, G = m / 3, q = m % 3, t = G / 13, gi = G % 13;
          if constexpr (m == 72) {
            if constexpr (j < 6) { WAITV_BAR(12); khead(IC<(24 + 2 * j) % NSLOT>{}); }
            else if constexpr (j == 6) { WAITV_BAR(18); }
            else { WAITV_BAR(12); sl = 0; p1head(); }
            SBAR();
          }
          if constexpr (gi == 0) {
            if constexpr (j == 0 && t == 0) { MFMA_O0(12, q, ones, pf[t][q]); } else { MFMA_O(12, q, ones, pf[t][q]); }
          } else {
            constexpr int d = gi - 1, f = 12 * t + d;
            if constexpr (q == 0 && f + 2 < 24) rdv(IC<f + 2>{});
            if constexpr (j == 0 && t == 0) { MFMA_O0(d, q, vf[f % 3], pf[t][q]); } else { MFMA_O(d, q, vf[f % 3], pf[t][q]); }
          }
          if constexpr (j < 7 && q < 2) {
            if constexpr (G >= 2 && G < 8) { constexpr int i = 4 * (G - 2) + 2 * q; mop(IC<i>{}); mop(IC<i + 1>{}); }
            if constexpr (G == 8 && q == 0) {
              pend = __any(mx3c(mx[0], mx[1], mx[2]) > THR);
              if (__builtin_expect(pend, 0)) {
#pragma unroll
                for (int qq_ = 0; qq_ < 3; ++qq_) {
                  const float gr = xmax(mx[qq_]);
                  const unsigned db = (__float_as_uint(fmaxf(gr, 0.f)) + 0xFFFFu) & 0xFFFF0000u;
                  fct[qq_] = __builtin_amdgcn_exp2f(-__uint_as_float(db));
                  u32x4 bw = {g == 0 ? ((db >> 16) | 0x8000u) : 0u, 0u, 0u, 0u};
                  bf16x8 bfr = __builtin_bit_cast(bf16x8, bw);
                  asm volatile("s_nop 1" : "+v"(bfr));
                  mfma_ip_(negm[qq_], ones, bfr);
                  mfma_ip_(sc[Y][0][qq_], ones, bfr); mfma_ip_(sc[Y][1][qq_], ones, bfr); mfma_ip_(sc[Y][2][qq_], ones, bfr); mfma_ip_(sc[Y][3][qq_], ones, bfr);
                }
                NOPS(7);
              }
            }
            if constexpr (G >= 9 && G < 15) { constexpr int e = 2 * (G - 9) + q; EXP1(Y, 0, e >> 2, e & 3); }
          }
          if constexpr (j < 7 && q == 1 && G >= 16 && G < 22) pf_(IC<26 + 2 * j>{}, IC<G - 16>{});
          SBAR();
        });
        if constexpr (j < 7) {
          if (__builtin_expect(pend, 0)) {
            NOPS(7);
            sfor<156>([&](auto N) AI { ASCALE(N.value, fct[(N.value >> 2) % 3]); });
            NOPS(1);
          }
        }
      }
    });
    if (it == 1) break;
    kg += (size_t)4 * NH * SK * DH * 2; vg += (size_t)4 * NH * SK * DH * 2;
    zero_qa();
    sfor<10>([&](auto C) AI { p1step(C.value, IC<(C.value >= 8)>{}, IC<C.value>{}, IC<(C.value == 9)>{}); });
    og += (size_t)4 * 192 * 12 * 1024;
  }
  NOPS(7);
  sfor<18>([&](auto U) AI { epi(U, IC<0>{}); epi(U, IC<1>{}); epi(U, IC<2>{}); epi(U, IC<3>{}); });
  asm volatile("s_waitcnt vmcnt(0)" ::: "memory");
}

constexpr int ST2 = 128 * 320;
constexpr int LDS2 = 3 * ST2;
constexpr size_t WS_SLAB = (size_t)64 << 20;
constexpr size_t WS_CNT = (size_t)48 << 20;

DI void gload16(bf16x8& d, unsigned voff, const char* sbase) { asm volatile("global_load_dwordx4 %0, %1, %2" : "=v"(d) : "v"(voff), "s"(sbase) : "memory"); }
template <int N> DI void wait3(bf16x8& a, bf16x8& b, bf16x8& c) { asm volatile("s_waitcnt vmcnt(%3)" : "+v"(a), "+v"(b), "+v"(c) : "i"(N) : "memory"); }

__global__ __launch_bounds__(512, 2) void k_oproj(Ptrs P, char* __restrict__ ws, char* __restrict__ out) {
  extern __shared__ __attribute__((aligned(16))) char smem[];
  lds_ptr lds = (lds_ptr)smem;
  const int tid = threadIdx.x, l = tid & 63, w = __builtin_amdgcn_readfirstlane(tid >> 6);
  const int i16 = l & 15, g = l >> 4, qq = i16 >> 2, pp = i16 & 3;
  const int xcd = blockIdx.x & 7, idx = blockIdx.x >> 3;
  const int br = xcd >> 2, ks = xcd & 3, b = idx >> 2, nq = idx & 3;
  const int kh = w >> 2, qg = w & 3;
  const unsigned lds0 = (unsigned)(uintptr_t)lds;
  const char* wsrc = P.wo[br] + (size_t)ks * 1536 * DM * 2 + nq * 320;
  const char* osrc = ws + (((size_t)(br * NB + b) * 192 + 48 * ks + kh) * 12 + 3 * qg) * 1024;
  const unsigned l16 = l * 16;
  unsigned offW[5];
#pragma unroll
  for (int u = 0; u < 5; ++u) {
    const int o = 1024 * u + 16 * l, r = o / 320, chp = (o % 320) >> 4;
    offW[u] = (16 * w + r) * (DM * 2) + ((chp ^ (((r >> 2) & 1) << 1)) << 4);
  }
  const unsigned o0 = offW[0], o1 = offW[1], o2 = offW[2], o3 = offW[3], o4 = offW[4];
  const int s0 = 0;
  auto wpiece = [&](const char* src, unsigned d, auto Uc) AI {
    constexpr int u = Uc.value;
    glds16(u == 0 ? o0 : u == 1 ? o1 : u == 2 ? o2 : u == 3 ? o3 : o4, src, d + u * 1024);
  };
  auto wsrc_of = [&](int s) AI { s += s0; s = s >= 12 ? s - 12 : s; return wsrc + (size_t)s * 128 * DM * 2; };
  auto osrc_of = [&](int u) AI { u += 2 * s0; u = u >= 24 ? u - 24 : u; return osrc + (size_t)u * 24576; };
  auto issueW = [&](int s, int stg) AI {
    const unsigned d = lds0 + stg * ST2 + w * 5120;
    const char* src = wsrc_of(s);
    sfor<5>([&](auto U) AI { wpiece(src, d, U); });
  };
  bf16x8 of[4][3];
  auto loadO = [&](int u, auto Bc) AI {
    constexpr int B_ = Bc.value;
    const char* src = osrc_of(u);
    gload16(of[B_][0], l16, src); gload16(of[B_][1], l16, src + 1024); gload16(of[B_][2], l16, src + 2048);
  };
  f32x4 acc[10][3];
#pragma unroll
  for (int n = 0; n < 10; ++n)
#pragma unroll
    for (int q = 0; q < 3; ++q) acc[n][q] = f32x4{0.f, 0.f, 0.f, 0.f};
  const unsigned wb0 = lds0 + (32 * kh + 4 * g + qq) * 320 + 8 * pp + ((g & 1) << 5);
  {
    unsigned tch;
    const char* wo = P.wo[br] + (size_t)ks * 1536 * DM * 2 + (size_t)(idx * 8 + w) * 60 * 128;
    asm volatile("global_load_dword %0, %1, %2" : "=v"(tch) : "v"((l < 60 ? l : 59) * 128), "s"(wo) : "memory");
    issueW(0, 0); loadO(0, IC<0>{}); loadO(1, IC<1>{}); loadO(2, IC<2>{});
    issueW(1, 1);
    asm volatile("s_waitcnt vmcnt(19)" : "+v"(tch) :: "memory");
  }
  int stg = 0;
  auto kstep = [&](unsigned ae, unsigned ao, auto Vc, auto Bc, auto Lc, const char* lsrc, const char* dsrc, unsigned dd, auto Dc) AI {
    constexpr int v = Vc.value, B_ = Bc.value, LB = Lc.value, DN = Dc.value;
    bf16x8 wf[3];
    auto rdw = [&](auto N) AI { constexpr int n = N.value; const unsigned p = ((n & 1) ? ao : ae) + v * 64 * 320 + (n >> 1) * 64; wf[n % 3] = cat8(ldstr(p), ldstr(p + 16 * 320)); };
    rdw(IC<0>{}); rdw(IC<1>{});
    SBAR();
    sfor<10>([&](auto N) AI {
      constexpr int n = N.value;
      if constexpr (n + 2 < 10) rdw(IC<n + 2>{});
      acc[n][0] = MFMA16(wf[n % 3], of[B_][0], acc[n][0]);
      if constexpr (LB >= 0 && n < 3) { SBAR(); gload16(of[LB][n], l16, lsrc + n * 1024); SBAR(); }
      if constexpr (DN > 0 && n >= 4 && n < 4 + 2 * DN && (n & 1) == 0) { SBAR(); wpiece(dsrc, dd, IC<(v ? 2 : 0) + (n - 4) / 2>{}); SBAR(); }
      acc[n][1] = MFMA16(wf[n % 3], of[B_][1], acc[n][1]);
      acc[n][2] = MFMA16(wf[n % 3], of[B_][2], acc[n][2]);
      SBAR();
    });
  };
  auto stage = [&](int s, auto Pc, auto Mc) AI {
    constexpr int PAR = Pc.value, MODE = Mc.value, B0 = 2 * PAR, B1 = 2 * PAR + 1;
    if constexpr (MODE == 2) { asm volatile("s_waitcnt vmcnt(3) lgkmcnt(0)\n\ts_barrier" ::: "memory"); }
    else { asm volatile("s_waitcnt vmcnt(11) lgkmcnt(0)\n\ts_barrier" ::: "memory"); }
    wait3<63>(of[B0][0], of[B0][1], of[B0][2]);
    const unsigned ae = opq(wb0 + stg * ST2), ao = opq((wb0 ^ 32) + stg * ST2);
    const unsigned dd = lds0 + (stg == 0 ? 2 : stg - 1) * ST2 + w * 5120;
    const char* dsrc = wsrc_of(s + 2);
    kstep(ae, ao, IC<0>{}, IC<B0>{}, IC<(MODE <= 1 ? ((B0 + 3) & 3) : -1)>{}, osrc_of(2 * s + 3), dsrc, dd, IC<(MODE == 0 ? 2 : 0)>{});
    wait3<MODE == 0 ? 13 : MODE == 1 ? 11 : 0>(of[B1][0], of[B1][1], of[B1][2]);
    kstep(ae, ao, IC<1>{}, IC<B1>{}, IC<(MODE == 0 ? B0 : -1)>{}, osrc_of(2 * s + 4), dsrc, dd, IC<(MODE == 0 ? 3 : 0)>{});
    stg = stg == 2 ? 0 : stg + 1;
  };
#pragma unroll 1
  for (int s = 0; s < 10; s += 2) { stage(s, IC<0>{}, IC<0>{}); stage(s + 1, IC<1>{}, IC<0>{}); }
  stage(10, IC<0>{}, IC<1>{}); stage(11, IC<1>{}, IC<2>{});
  asm volatile("s_waitcnt vmcnt(0) lgkmcnt(0)\n\ts_barrier" ::: "memory");
  {
    const unsigned xo = lds0 + ((kh ^ 1) * 4 + qg) * 15360 + l16, xi = lds0 + (kh * 4 + qg) * 15360 + l16;
#pragma unroll
    for (int n = 0; n < 5; ++n)
#pragma unroll
      for (int q = 0; q < 3; ++q) *(__attribute__((address_space(3))) f32x4*)(xo + (n * 3 + q) * 1024) = kh ? acc[n][q] : acc[5 + n][q];
    __syncthreads();
#pragma unroll
    for (int n = 0; n < 5; ++n)
#pragma unroll
      for (int q = 0; q < 3; ++q) {
        const f32x4 o = *(const __attribute__((address_space(3))) f32x4*)(xi + (n * 3 + q) * 1024);
        acc[n][q] = (kh ? acc[5 + n][q] : acc[n][q]) + o;
      }
  }
  const int tile = (br * NB + b) * 4 + nq;
  char* slab = ws + WS_SLAB + (size_t)tile * (16 * 8 * 2048);
  {
#pragma unroll
    for (int r = 0; r < 4; ++r) {
      if (r != ks) {
        char* dst = slab + (size_t)((r * 4 + ks) * 8 + w) * 2048 + l16;
#pragma unroll
        for (int m = 0; m < 2; ++m) {
          const int i0_ = 8 * m + r, i1_ = (8 * m + 4 + r) < 15 ? 8 * m + 4 + r : i0_;
          const f32x4 a = acc[i0_ / 3][i0_ % 3], c = acc[i1_ / 3][i1_ % 3];
          u32x4 v = {cvtpk(a[0], a[1]), cvtpk(a[2], a[3]), cvtpk(c[0], c[1]), cvtpk(c[2], c[3])};
          asm volatile("global_store_dwordx4 %0, %1, off sc0 sc1\n\ts_nop 1" :: "v"(dst + m * 1024), "v"(v) : "memory");
        }
      }
    }
    asm volatile("s_waitcnt vmcnt(0)" ::: "memory");
    __syncthreads();
    unsigned* cnt = (unsigned*)(ws + WS_CNT) + tile * 16;
    if (tid == 0) {
      __hip_atomic_fetch_add(cnt, 1u, __ATOMIC_RELAXED, __HIP_MEMORY_SCOPE_AGENT);
      unsigned spins = 0;
      while (__hip_atomic_load(cnt, __ATOMIC_RELAXED, __HIP_MEMORY_SCOPE_AGENT) < 4u && ++spins < (1u << 24)) __builtin_amdgcn_s_sleep(1);
    }
    __syncthreads();
  }
  {
    u32x4 p[3][2];
#pragma unroll
    for (int r = 0; r < 3; ++r) {
      const int src = (ks + 1 + r) & 3;
      const char* th = slab + (size_t)((ks * 4 + src) * 8 + w) * 2048 + l16;
      asm volatile("global_load_dwordx4 %0, %1, off sc0 sc1" : "=v"(p[r][0]) : "v"(th) : "memory");
      asm volatile("global_load_dwordx4 %0, %1, off sc0 sc1" : "=v"(p[r][1]) : "v"(th + 1024) : "memory");
    }
    asm volatile("s_waitcnt vmcnt(0)" : "+v"(p[0][0]), "+v"(p[0][1]), "+v"(p[1][0]), "+v"(p[1][1]), "+v"(p[2][0]), "+v"(p[2][1]) :: "memory");
#pragma unroll
    for (int i = 0; i < 15; ++i) {
      if ((i & 3) == ks) {
        const int m = i >> 3, hf = (i >> 2) & 1;
        f32x4 o = acc[i / 3][i % 3];
#pragma unroll
        for (int r = 0; r < 3; ++r) {
          const unsigned u0 = p[r][m][2 * hf], u1 = p[r][m][2 * hf + 1];
          o[0] += __uint_as_float(u0 << 16); o[1] += __uint_as_float(u0 & 0xFFFF0000u);
          o[2] += __uint_as_float(u1 << 16); o[3] += __uint_as_float(u1 & 0xFFFF0000u);
        }
        u32x2 f = {cvtpk(o[0], o[1]), cvtpk(o[2], o[3])};
        char* dst = out + ((size_t)(b * SQ + (3 * qg + i % 3) * 16 + i16) * (2 * DM) + br * DM + nq * 160 + (5 * kh + i / 3) * 16 + 4 * g) * 2;
        *(u32x2*)dst = f;
      }
    }
  }
}

extern "C" void kernel_launch(void* const* d_in, const int* in_sizes, int n_in, void* d_out, int out_size, void* d_ws, size_t ws_size,
                              hipStream_t stream) {
  Ptrs P;
  for (int r = 0; r < 2; ++r) {
    P.x[r] = (const char*)d_in[5 * r + 0];
    P.k[r] = (const char*)d_in[5 * r + 1];
    P.v[r] = (const char*)d_in[5 * r + 2];
    P.wq[r] = (const char*)d_in[5 * r + 3];
    P.wo[r] = (const char*)d_in[5 * r + 4];
  }
  (void)hipFuncSetAttribute((const void*)k_attn, hipFuncAttributeMaxDynamicSharedMemorySize, LDS1);
  (void)hipFuncSetAttribute((const void*)k_oproj, hipFuncAttributeMaxDynamicSharedMemorySize, LDS2);
  hipLaunchKernelGGL(k_attn, dim3(256), dim3(256), LDS1, stream, P, (char*)d_ws);
  hipLaunchKernelGGL(k_oproj, dim3(256), dim3(512), LDS2, stream, P, (char*)d_ws, (char*)d_out);
}
```
